```python
import torch

def _softmax(x, axis=-1):
    return torch.softmax(x, dim=axis)
def _gelu(x):
    return torch.nn.functional.gelu(x, approximate="tanh")
def _silu(x):
    return torch.nn.functional.silu(x)
def _sigmoid(x):
    return torch.sigmoid(x)
def _layernorm(x, axis=-1, eps=1e-5):
    m = x.mean(dim=axis, keepdim=True)
    v = x.var(dim=axis, keepdim=True, unbiased=False)
    return (x - m) / torch.sqrt(v + eps)
def _scatter_add(tgt, idx, src, axis=0):
    return tgt.clone().index_add_(axis, idx, src)
def _segment_sum(src, seg, n):
    out = torch.zeros((n,) + src.shape[1:], dtype=src.dtype)
    return out.index_add_(0, seg, src)
def _pad(x, pw):
    flat = tuple(v for pair in reversed(pw) for v in pair)
    return torch.nn.functional.pad(x, flat)

kernel_name = 'interleave_layers__grouped_kv_mqa__mixer_channel_mlp__prompt'

def setup_inputs():

    _w = (lambda a: (lambda t: t.to(torch.bfloat16) if t.is_floating_point() else t)(torch.as_tensor(a)))
    def _A_setup():
        B, S, D, H = 1, 2048, 640, 32
        Dh = D // H
        return {
            "x": _w((lambda *s: torch.randn(*s, dtype=torch.bfloat16) * 0.05)(B, S, D)),
            "wq": _w((lambda *s: torch.randn(*s, dtype=torch.bfloat16) * 0.05)(D, H * Dh)),
            "wk": _w((lambda *s: torch.randn(*s, dtype=torch.bfloat16) * 0.05)(D, Dh)),
            "wv": _w((lambda *s: torch.randn(*s, dtype=torch.bfloat16) * 0.05)(D, Dh)),
            "wo": _w((lambda *s: torch.randn(*s, dtype=torch.bfloat16) * 0.05)(H * Dh, D)),
        }
    _A_in = _A_setup()

    def _B_setup():
        B, S, D, F = 1, 2048, 640, 1728
        return {
            "x": _w((lambda *s: torch.randn(*s, dtype=torch.bfloat16) * 0.05)(B, S, D)),
            "w1": _w((lambda *s: torch.randn(*s, dtype=torch.bfloat16) * 3.2)(D, F) * (D ** -0.5)),
            "b1": _w((lambda *s: torch.randn(*s, dtype=torch.bfloat16) * 0.8)(F)),
            "w2": _w((lambda *s: torch.randn(*s, dtype=torch.bfloat16) * 0.4)(F, D) * (F ** -0.5)),
            "b2": _w((lambda *s: torch.randn(*s, dtype=torch.bfloat16) * 0.4)(D)),
        }
    _B_in = _B_setup()

    return {
        "A_x": _A_in["x"],
        "A_wq": _A_in["wq"],
        "A_wk": _A_in["wk"],
        "A_wv": _A_in["wv"],
        "A_wo": _A_in["wo"],
        "B_x": _B_in["x"],
        "B_w1": _B_in["w1"],
        "B_b1": _B_in["b1"],
        "B_w2": _B_in["w2"],
        "B_b2": _B_in["b2"],
    }

def reference(A_x, A_wq, A_wk, A_wv, A_wo, B_x, B_w1, B_b1, B_w2, B_b2):
    def _A_ref(x, wq, wk, wv, wo):
        B, S, D = x.shape
        H = 32
        Dh = D // H
        q = (lambda x, *d: x.permute(*d))((x @ wq).reshape(B, S, H, Dh), 0, 2, 1, 3)
        k = x @ wk
        v = x @ wv
        scores = torch.einsum("bhqd,bkd->bhqk", q, k) * (Dh ** -0.5)
        p = _softmax(scores, axis=-1)
        o = torch.einsum("bhqk,bkd->bhqd", p, v)
        return (lambda x, *d: x.permute(*d))(o, 0, 2, 1, 3).reshape(B, S, H * Dh) @ wo

    def _B_ref(x, w1, b1, w2, b2):
        y = _layernorm(x, axis=-1)
        h = _gelu(torch.einsum("bsd,df->bsf", y, w1) + b1)
        return torch.einsum("bsf,fd->bsd", h, w2) + b2

    x = A_x
    x = x + _A_ref(_layernorm(x, axis=-1), A_wq, A_wk, A_wv, A_wo)
    x = x + _B_ref(_layernorm(x, axis=-1), B_w1, B_b1, B_w2, B_b2)
    x = x + _A_ref(_layernorm(x, axis=-1), A_wq, A_wk, A_wv, A_wo)
    x = x + _B_ref(_layernorm(x, axis=-1), B_w1, B_b1, B_w2, B_b2)
    return x
```

```cpp
#include <hip/hip_runtime.h>
#include <hip/hip_bf16.h>
typedef unsigned short u16;
__device__ __forceinline__ float bf2f(u16 v) { return __uint_as_float(((unsigned)v) << 16); }
__device__ __forceinline__ u16 f2bf(float f) { __bf16 b = (__bf16)f; return __builtin_bit_cast(u16, b); }
constexpr int S = 2048, D = 640, H = 32, DH = 20, F = 1728;

__global__ void k_cvt(const u16* x, float* o, int n) { int i = blockIdx.x * 256 + threadIdx.x; if (i < n) o[i] = bf2f(x[i]); }
__global__ void k_out(const float* x, u16* o, int n) { int i = blockIdx.x * 256 + threadIdx.x; if (i < n) o[i] = f2bf(x[i]); }
__global__ void k_ln(const float* x, float* y) {
  int r = blockIdx.x, l = threadIdx.x; float v[10]; float s = 0;
  for (int i = 0; i < 10; i++) { v[i] = x[r * D + l + 64 * i]; s += v[i]; }
  for (int o = 32; o; o >>= 1) s += __shfl_xor(s, o);
  float m = s / D, q = 0;
  for (int i = 0; i < 10; i++) { v[i] -= m; q += v[i] * v[i]; }
  for (int o = 32; o; o >>= 1) q += __shfl_xor(q, o);
  float rs = 1.0f / sqrtf(q / D + 1e-5f);
  for (int i = 0; i < 10; i++) y[r * D + l + 64 * i] = v[i] * rs;
}
template<int MODE>
__global__ void k_gemm(const float* A, const u16* W, const u16* bias, const float* res, float* C, int N, int K) {
  int n = blockIdx.x * 64 + threadIdx.x, m0 = blockIdx.y * 8;
  if (n >= N) return;
  float acc[8] = {0};
  for (int k = 0; k < K; k++) { float w = bf2f(W[(size_t)k * N + n]);
    for (int i = 0; i < 8; i++) acc[i] += A[(size_t)(m0 + i) * K + k] * w; }
  for (int i = 0; i < 8; i++) { float v = acc[i];
    if (bias) v += bf2f(bias[n]);
    if (MODE == 1) { float u = 0.7978845608f * (v + 0.044715f * v * v * v); v = 0.5f * v * (1.0f + tanhf(u)); }
    if (res) v += res[(size_t)(m0 + i) * N + n];
    C[(size_t)(m0 + i) * N + n] = v; }
}
__global__ void k_attn(const float* Q, const float* K, const float* V, float* O) {
  int idx = blockIdx.x * 64 + threadIdx.x; int t = idx / H, h = idx % H;
  float q[DH], o[DH]; for (int d = 0; d < DH; d++) { q[d] = Q[t * D + h * DH + d] * 0.22360679775f; o[d] = 0; }
  float m = -1e30f, l = 0;
  for (int j = 0; j < S; j++) { float s = 0; for (int d = 0; d < DH; d++) s += q[d] * K[j * DH + d];
    float mn = fmaxf(m, s), a = __expf(m - mn), p = __expf(s - mn); l = l * a + p;
    for (int d = 0; d < DH; d++) o[d] = o[d] * a + p * V[j * DH + d]; m = mn; }
  for (int d = 0; d < DH; d++) O[t * D + h * DH + d] = o[d] / l;
}
extern "C" void kernel_launch(void* const* d_in, const int* in_sizes, int n_in, void* d_out, int out_size, void* d_ws, size_t ws_size, hipStream_t stream) {
  const u16 *x = (const u16*)d_in[0], *wq = (const u16*)d_in[1], *wk = (const u16*)d_in[2], *wv = (const u16*)d_in[3], *wo = (const u16*)d_in[4];
  const u16 *w1 = (const u16*)d_in[6], *b1 = (const u16*)d_in[7], *w2 = (const u16*)d_in[8], *b2 = (const u16*)d_in[9];
  float* p = (float*)d_ws; float *X = p, *X2 = X + S * D, *Y = X2 + S * D, *Y2 = Y + S * D, *Q = Y2 + S * D, *O = Q + S * D, *K = O + S * D, *V = K + S * DH, *Hh = V + S * DH;
  k_cvt<<<S * D / 256, 256, 0, stream>>>(x, X, S * D);
  for (int L = 0; L < 2; L++) {
    k_ln<<<S, 64, 0, stream>>>(X, Y);
    k_gemm<0><<<dim3(10, S / 8), 64, 0, stream>>>(Y, wq, nullptr, nullptr, Q, D, D);
    k_gemm<0><<<dim3(1, S / 8), 64, 0, stream>>>(Y, wk, nullptr, nullptr, K, DH, D);
    k_gemm<0><<<dim3(1, S / 8), 64, 0, stream>>>(Y, wv, nullptr, nullptr, V, DH, D);
    k_attn<<<S * H / 64, 64, 0, stream>>>(Q, K, V, O);
    k_gemm<0><<<dim3(10, S / 8), 64, 0, stream>>>(O, wo, nullptr, X, X2, D, D);
    k_ln<<<S, 64, 0, stream>>>(X2, Y);
    k_ln<<<S, 64, 0, stream>>>(Y, Y2);
    k_gemm<1><<<dim3(27, S / 8), 64, 0, stream>>>(Y2, w1, b1, nullptr, Hh, F, D);
    k_gemm<0><<<dim3(10, S / 8), 64, 0, stream>>>(Hh, w2, b2, X2, X, D, F);
  }
  k_out<<<S * D / 256, 256, 0, stream>>>(X, (u16*)d_out, S * D);
}
```

```cpp
#include <hip/hip_runtime.h>
#include <hip/hip_bf16.h>

__global__ void __launch_bounds__(256, 1) __attribute__((aligned(4096)))
fused_kernel(const void* x, const void* wq, const void* wk, const void* wv, const void* wo, const void* w1, const void* b1, const void* w2,
             const void* b2, void* out, void* ws) {
  __shared__ __attribute__((aligned(16))) unsigned char lds[163840];
  asm volatile("" ::"v"(lds));
  unsigned tid = threadIdx.x;
  asm volatile(
"s_load_dwordx16 s[44:59], %1, 0x0\n"
"s_load_dwordx4 s[60:63], %1, 0x40\n"
"s_load_dwordx2 s[4:5], %1, 0x50\n"
"s_mov_b32 s6, %2\n"
"v_and_b32 v4, 63, %0\n"
"v_lshrrev_b32 v3, 6, %0\n"
"s_and_b32 s26, s6, 7\n"
"s_lshr_b32 s64, s6, 3\n"
"v_readfirstlane_b32 s22, v3\n"
"s_getpc_b64 s[88:89]\n"
"Lpc_%=:\n"
"s_and_b32 s89, s89, 0xffff\n"
"s_mov_b32 s90, Lfin_%=-Lpc_%=\n"
"s_mov_b32 s91, 0x20000\n"
"s_lshl_b32 s72, s64, 2\n"
"s_add_u32 s72, s72, s22\n"
"s_lshl_b32 s73, s72, 9\n"
"v_lshlrev_b32 v166, 3, v4\n"
"buffer_load_dword v167, v166, s[88:91], s73 offen\n"
"s_and_b32 s21, s64, 7\n"
"s_lshr_b32 s65, s64, 3\n"
"s_lshl_b32 s20, s26, 2\n"
"s_add_u32 s20, s20, s65\n"
"s_lshl_b32 s24, s20, 2\n"
"s_add_u32 s24, s24, s22\n"
"s_lshl_b32 s7, s26, 5\n"
"s_add_u32 s7, s7, s64\n"
"s_lshr_b32 s34, s21, 1\n"
"s_and_b32 s35, s21, 1\n"
"s_mov_b32 s23, 0\n"
"s_lshl_b32 s74, s65, 1\n"
"s_add_u32 s74, s74, s35\n"
"s_lshl_b32 s74, s74, 2\n"
"s_add_u32 s74, s74, s22\n"
"s_mul_i32 s74, s74, 800\n"
"s_and_b32 s70, s72, 31\n"
"s_mul_i32 s70, s70, 3200\n"
"s_lshr_b32 s72, s72, 5\n"
"s_mul_i32 s72, s72, 204800\n"
"s_add_u32 s8, s70, 163840\n"
"s_cmp_ge_u32 s8, 204800\n"
"s_cselect_b32 s9, 204800, 0\n"
"s_sub_u32 s8, s8, s9\n"
"s_add_u32 s8, s8, s72\n"
"s_add_u32 s9, s70, s72\n"
"s_add_u32 s9, s9, 61440\n"
"s_mul_i32 s65, s24, 20480\n"
"s_mul_i32 s73, s21, 160\n"
"s_add_u32 s73, s73, s65\n"
"v_lshlrev_b32 v1, 4, v4\n"
"v_mov_b32 v2, 0\n"
"v_mov_b32 v11, 0\n"
"v_mul_u32_u24 v121, 52, v4\n"
"v_and_b32 v162, 15, v4\n"
"v_lshrrev_b32 v163, 4, v4\n"
"v_mul_u32_u24 v144, 1280, v162\n"
"v_add_u32 v144, s73, v144\n"
"v_lshrrev_b32 v164, 1, v163\n"
"v_and_b32 v165, 1, v163\n"
"v_lshl_add_u32 v145, v165, 4, v144\n"
"v_lshl_add_u32 v10, v164, 3, v145\n"
"v_add_u32 v10, 128, v10\n"
"v_lshl_add_u32 v6, v163, 4, v144\n"
"v_mul_u32_u24 v120, 40, v4\n"
"s_mul_i32 s73, s21, 2560\n"
"s_add_u32 s73, s73, s65\n"
"s_mul_i32 s75, s21, 10\n"
"v_lshrrev_b32 v147, 1, v162\n"
"v_add3_u32 v146, v163, s75, 0\n"
"v_lshrrev_b32 v200, 3, v146\n"
"v_lshlrev_b32 v200, 11, v200\n"
"v_lshl_add_u32 v200, v162, 7, v200\n"
"v_and_b32 v146, 7, v146\n"
"v_xor_b32 v146, v146, v147\n"
"v_lshl_add_u32 v200, v146, 4, v200\n"
"v_add3_u32 v146, v163, s75, 4\n"
"v_lshrrev_b32 v204, 3, v146\n"
"v_lshlrev_b32 v204, 11, v204\n"
"v_lshl_add_u32 v204, v162, 7, v204\n"
"v_and_b32 v146, 7, v146\n"
"v_xor_b32 v146, v146, v147\n"
"v_lshl_add_u32 v204, v146, 4, v204\n"
"v_add3_u32 v146, v165, s75, 8\n"
"v_lshrrev_b32 v208, 3, v146\n"
"v_lshlrev_b32 v208, 11, v208\n"
"v_lshl_add_u32 v208, v162, 7, v208\n"
"v_and_b32 v146, 7, v146\n"
"v_xor_b32 v146, v146, v147\n"
"v_lshl_add_u32 v208, v146, 4, v208\n"
"v_lshl_add_u32 v208, v164, 3, v208\n"
"v_lshrrev_b32 v194, 3, v4\n"
"v_mul_u32_u24 v194, 1280, v194\n"
"v_and_b32 v154, 7, v4\n"
"v_xor_b32 v154, v154, v163\n"
"v_lshl_add_u32 v194, v154, 4, v194\n"
"v_xor_b32 v195, 64, v194\n"
"v_add_u32 v195, 10240, v195\n"
"v_add_u32 v146, 0, v4\n"
"v_mul_u32_u24 v124, 6554, v146\n"
"v_lshrrev_b32 v124, 16, v124\n"
"v_mul_u32_u24 v147, 10, v124\n"
"v_sub_u32 v146, v146, v147\n"
"v_lshlrev_b32 v130, 4, v146\n"
"v_lshrrev_b32 v146, 1, v124\n"
"v_and_b32 v146, 4, v146\n"
"v_xor_b32 v146, v146, v124\n"
"v_mul_u32_u24 v146, 1280, v146\n"
"v_add_u32 v168, v146, v130\n"
"v_add_u32 v146, 64, v4\n"
"v_mul_u32_u24 v125, 6554, v146\n"
"v_lshrrev_b32 v125, 16, v125\n"
"v_mul_u32_u24 v147, 10, v125\n"
"v_sub_u32 v146, v146, v147\n"
"v_lshlrev_b32 v131, 4, v146\n"
"v_lshrrev_b32 v146, 1, v125\n"
"v_and_b32 v146, 4, v146\n"
"v_xor_b32 v146, v146, v125\n"
"v_mul_u32_u24 v146, 1280, v146\n"
"v_add_u32 v169, v146, v131\n"
"v_subrev_u32 v169, 1024, v169\n"
"v_add_u32 v146, 128, v4\n"
"v_mul_u32_u24 v126, 6554, v146\n"
"v_lshrrev_b32 v126, 16, v126\n"
"v_mul_u32_u24 v147, 10, v126\n"
"v_sub_u32 v146, v146, v147\n"
"v_lshlrev_b32 v132, 4, v146\n"
"v_lshrrev_b32 v146, 1, v126\n"
"v_and_b32 v146, 4, v146\n"
"v_xor_b32 v146, v146, v126\n"
"v_mul_u32_u24 v146, 1280, v146\n"
"v_add_u32 v170, v146, v132\n"
"v_subrev_u32 v170, 2048, v170\n"
"v_add_u32 v146, 192, v4\n"
"v_mul_u32_u24 v127, 6554, v146\n"
"v_lshrrev_b32 v127, 16, v127\n"
"v_mul_u32_u24 v147, 10, v127\n"
"v_sub_u32 v146, v146, v147\n"
"v_lshlrev_b32 v133, 4, v146\n"
"v_lshrrev_b32 v146, 1, v127\n"
"v_and_b32 v146, 4, v146\n"
"v_xor_b32 v146, v146, v127\n"
"v_mul_u32_u24 v146, 1280, v146\n"
"v_add_u32 v171, v146, v133\n"
"v_subrev_u32 v171, 3072, v171\n"
"v_add_u32 v146, 256, v4\n"
"v_mul_u32_u24 v128, 6554, v146\n"
"v_lshrrev_b32 v128, 16, v128\n"
"v_mul_u32_u24 v147, 10, v128\n"
"v_sub_u32 v146, v146, v147\n"
"v_lshlrev_b32 v134, 4, v146\n"
"v_lshrrev_b32 v146, 1, v128\n"
"v_and_b32 v146, 4, v146\n"
"v_xor_b32 v146, v146, v128\n"
"v_mul_u32_u24 v146, 1280, v146\n"
"v_add_u32 v172, v146, v134\n"
"v_lshrrev_b32 v146, 3, v4\n"
"v_add_u32 v146, s26, v146\n"
"v_and_b32 v146, 7, v146\n"
"v_lshlrev_b32 v146, 2, v146\n"
"v_add_u32 v146, s22, v146\n"
"v_and_b32 v147, 3, v4\n"
"v_lshl_add_u32 v146, v146, 2, v147\n"
"v_lshlrev_b32 v175, 7, v146\n"
"s_waitcnt lgkmcnt(0)\n"
"s_mov_b32 s16, s44\n"
"s_and_b32 s17, s45, 0xffff\n"
"s_mov_b32 s18, 2621440\n"
"s_mov_b32 s19, 0x20000\n"
"buffer_load_dword v120, v120, s[16:19], s73 offen\n"
"s_cmp_eq_u32 s34, 1\n"
"s_cselect_b32 s84, s50, s48\n"
"s_cselect_b32 s85, s51, s49\n"
"s_and_b32 s85, s85, 0xffff\n"
"s_cmp_lt_u32 s21, 4\n"
"s_cselect_b32 s86, 25600, 0\n"
"s_mov_b32 s87, 0x20000\n"
"buffer_load_dword v123, v1, s[84:87], s74 offen\n"
"s_mov_b32 s12, s4\n"
"s_and_b32 s13, s5, 0xffff\n"
"s_mov_b32 s14, 36500480\n"
"s_mov_b32 s15, 0x20000\n"
"buffer_load_dwordx2 v[214:215], v2, s[12:15], 0 offen sc1\n"
"s_mov_b32 s80, s46\n"
"s_and_b32 s81, s47, 0xffff\n"
"s_mov_b32 s82, 819200\n"
"s_mov_b32 s83, 0x20000\n"
"s_cmp_lt_u32 s7, 148\n"
"s_cselect_b32 s88, s52, s58\n"
"s_cselect_b32 s89, s53, s59\n"
"s_mov_b32 s90, 2211840\n"
"s_cselect_b32 s90, 819200, s90\n"
"s_cmp_lt_u32 s7, 108\n"
"s_cselect_b32 s88, s54, s88\n"
"s_cselect_b32 s89, s55, s89\n"
"s_cselect_b32 s90, 2211840, s90\n"
"s_and_b32 s89, s89, 0xffff\n"
"s_mov_b32 s92, s56\n"
"s_and_b32 s93, s57, 0xffff\n"
"s_mov_b32 s94, 3456\n"
"s_mov_b32 s95, 0x20000\n"
"s_mov_b32 s42, s62\n"
"s_and_b32 s43, s63, 0xffff\n"
"s_mov_b64 s[66:67], s[60:61]\n"
"s_mul_i32 s61, s22, 25600\n"
"v_lshrrev_b32 v154, 1, v162\n"
"v_xor_b32 v154, v154, v163\n"
"v_lshl_add_u32 v154, v162, 3, v154\n"
"v_lshl_add_u32 v212, v154, 4, s61\n"
"v_xor_b32 v213, 64, v212\n"
"v_add_u32 v200, s61, v200\n"
"v_add_u32 v204, s61, v204\n"
"v_add_u32 v208, s61, v208\n"
"s_mul_i32 s60, s22, 204800\n"
"s_mul_i32 s46, s21, 160\n"
"s_add_u32 s60, s60, s46\n"
"s_mov_b32 s25, 0\n"
"s_mov_b32 s27, 164864\n"
"s_mov_b32 s28, 1213440\n"
"s_mov_b32 s62, 33792\n"
"s_mov_b32 s36, 0\n"
"s_mov_b32 s37, -1\n"
"s_mul_i32 s29, s24, 20480\n"
"s_add_u32 s29, s29, 7926784\n"
"s_mul_i32 s30, s24, 22528\n"
"s_add_u32 s30, s30, 13169664\n"
"s_mov_b32 s31, 21820416\n"
"s_mul_i32 s78, s22, 5168\n"
"v_add_u32 v8, 0x10000, v1\n"
"s_mul_i32 s46, s21, 2560\n"
"s_add_u32 s46, s46, 2048\n"
"v_add_u32 v5, s46, v1\n"
"s_lshl_b32 s46, s21, 8\n"
"s_add_u32 s46, s46, 20480\n"
"v_lshl_add_u32 v146, v162, 4, s46\n"
"v_cndmask_b32 v5, v5, v146, s[36:37]\n"
"s_lshr_b32 s46, s22, 1\n"
"s_and_b32 s47, s22, 1\n"
"s_lshl_b32 s48, s46, 10\n"
"s_lshl_b32 s49, s47, 8\n"
"s_add_u32 s48, s48, s49\n"
"s_lshl_b32 s50, s46, 3\n"
"s_add_u32 s49, s49, s50\n"
"s_add_u32 s49, s49, 2048\n"
"s_mul_i32 s50, s22, 640\n"
"s_lshl_b32 s51, s35, 8\n"
"s_add_u32 s50, s50, s51\n"
"s_add_u32 s50, s50, 2560\n"
"s_cmp_eq_u32 s21, 1\n"
"s_cselect_b32 s48, s49, s48\n"
"s_cselect_b32 s38, 0xffff, -1\n"
"s_cselect_b32 s39, 0, -1\n"
"s_cmp_ge_u32 s21, 2\n"
"s_cselect_b32 s48, s50, s48\n"
"s_mov_b32 s53, 512\n"
"s_cselect_b32 s53, 320, s53\n"
"s_cmp_eq_u32 s21, 3\n"
"s_cselect_b32 s38, 0x000f000f, s38\n"
"s_cselect_b32 s39, 0x000f000f, s39\n"
"v_lshlrev_b32 v7, 4, v162\n"
"v_mad_u32_u24 v7, v164, s53, v7\n"
"v_lshl_add_u32 v7, v165, 3, v7\n"
"v_add_u32 v7, s48, v7\n"
"s_mov_b64 exec, 0\n"
"s_cmp_eq_u32 s22, 0\n"
"s_cbranch_scc1 Lpad1_%=\n"
"s_cmp_eq_u32 s22, 1\n"
"s_cbranch_scc1 Lpad2_%=\n"
"s_cmp_eq_u32 s22, 2\n"
"s_cbranch_scc1 Lpad3_%=\n"
"s_branch Lpad4_%=\n"
"Lpadback0_%=:\n"
"s_mov_b64 exec, -1\n"
"s_mul_i32 s53, s22, 7\n"
"s_min_u32 s53, s53, 18\n"
"s_lshl_b32 s53, s53, 10\n"
"s_add_u32 m0, s53, 102400\n"
"s_nop 0\n"
"buffer_load_dwordx4 v1, s[84:87], s53 offen offset:0 lds\n"
"buffer_load_dwordx4 v1, s[84:87], s53 offen offset:1024 lds\n"
"buffer_load_dwordx4 v1, s[84:87], s53 offen offset:2048 lds\n"
"buffer_load_dwordx4 v1, s[84:87], s53 offen offset:3072 lds\n"
"s_add_u32 s53, s53, 4096\n"
"s_add_u32 m0, s53, 102400\n"
"s_nop 0\n"
"buffer_load_dwordx4 v1, s[84:87], s53 offen offset:0 lds\n"
"buffer_load_dwordx4 v1, s[84:87], s53 offen offset:1024 lds\n"
"buffer_load_dwordx4 v1, s[84:87], s53 offen offset:2048 lds\n"
"s_cmp_lt_u32 s7, 108\n"
"s_cbranch_scc1 LrB5_%=\n"
"s_and_b32 s53, s7, 1\n"
"s_lshl_b32 s50, s53, 2\n"
"s_add_u32 s50, s50, s22\n"
"s_cmp_ge_u32 s7, 148\n"
"s_cbranch_scc1 Lr24_%=\n"
"s_sub_u32 s53, s7, 108\n"
"s_lshr_b32 s51, s53, 1\n"
"s_mul_i32 s53, s51, 205\n"
"s_lshr_b32 s53, s53, 10\n"
"s_mul_i32 s54, s53, 5\n"
"s_sub_u32 s52, s51, s54\n"
"s_mul_i32 s46, s53, 160\n"
"s_mul_i32 s54, s52, 40\n"
"s_mov_b32 s47, 20\n"
"s_cmp_lt_u32 s52, 4\n"
"s_cselect_b32 s54, s54, 16\n"
"s_cselect_b32 s48, 4, 80\n"
"s_cselect_b32 s49, 8, 40\n"
"s_add_u32 s46, s46, s54\n"
"s_mul_i32 s76, s50, 102400\n"
"s_add_u32 s76, s76, 1541120\n"
"s_branch LrA6_%=\n"
"Lr24_%=:\n"
"s_sub_u32 s53, s7, 148\n"
"s_lshr_b32 s51, s53, 1\n"
"s_mul_i32 s53, s51, 37\n"
"s_lshr_b32 s53, s53, 9\n"
"s_mul_i32 s54, s53, 14\n"
"s_sub_u32 s52, s51, s54\n"
"s_lshl_b32 s46, s54, 1\n"
"s_cmp_lt_u32 s53, 3\n"
"s_cselect_b32 s53, 3, 2\n"
"s_cselect_b32 s54, 14, 12\n"
"s_cmp_lt_u32 s52, 8\n"
"s_cselect_b32 s53, 4, s53\n"
"s_cselect_b32 s49, 16, 8\n"
"s_cselect_b32 s47, 0, 8\n"
"s_sub_u32 s52, s52, s47\n"
"s_cmp_ge_u32 s52, s53\n"
"s_cselect_b32 s53, s53, 0\n"
"s_cselect_b32 s54, s54, 0\n"
"s_sub_u32 s52, s52, s53\n"
"s_add_u32 s46, s46, s54\n"
"s_mul_i32 s53, s52, 3\n"
"s_mul_i32 s54, s52, 6\n"
"s_add_u32 s54, s54, 2\n"
"s_cmp_lt_u32 s52, 2\n"
"s_cselect_b32 s54, s54, 12\n"
"s_cselect_b32 s48, 48, 16\n"
"s_cmp_eq_u32 s47, 0\n"
"s_cselect_b32 s53, s53, s54\n"
"s_cselect_b32 s48, 8, s48\n"
"s_mov_b32 s47, 4\n"
"s_add_u32 s46, s46, s53\n"
"s_lshl_b32 s46, s46, 4\n"
"s_mul_i32 s76, s50, 276480\n"
"s_add_u32 s76, s76, 4682752\n"
"LrA6_%=:\n"
"s_mul_i32 s53, s51, 5120\n"
"s_add_u32 s76, s76, s53\n"
"s_mul_i32 s50, s50, 160\n"
"s_mov_b32 s77, 2147418112\n"
"s_mov_b32 s75, 2147418112\n"
"v_and_b32 v146, 3, v124\n"
"v_add_u32 v146, s46, v146\n"
"v_bfe_u32 v147, v124, 2, 1\n"
"v_mad_u32_u24 v146, v147, s47, v146\n"
"v_bfe_u32 v147, v124, 3, 1\n"
"v_mad_u32_u24 v146, v147, s48, v146\n"
"v_lshrrev_b32 v147, 4, v124\n"
"v_mad_u32_u24 v146, v147, s49, v146\n"
"v_mul_u32_u24 v146, 1280, v146\n"
"v_add3_u32 v176, v146, v130, s50\n"
"v_and_b32 v146, 3, v125\n"
"v_add_u32 v146, s46, v146\n"
"v_bfe_u32 v147, v125, 2, 1\n"
"v_mad_u32_u24 v146, v147, s47, v146\n"
"v_bfe_u32 v147, v125, 3, 1\n"
"v_mad_u32_u24 v146, v147, s48, v146\n"
"v_lshrrev_b32 v147, 4, v125\n"
"v_mad_u32_u24 v146, v147, s49, v146\n"
"v_mul_u32_u24 v146, 1280, v146\n"
"v_add3_u32 v177, v146, v131, s50\n"
"v_and_b32 v146, 3, v126\n"
"v_add_u32 v146, s46, v146\n"
"v_bfe_u32 v147, v126, 2, 1\n"
"v_mad_u32_u24 v146, v147, s47, v146\n"
"v_bfe_u32 v147, v126, 3, 1\n"
"v_mad_u32_u24 v146, v147, s48, v146\n"
"v_lshrrev_b32 v147, 4, v126\n"
"v_mad_u32_u24 v146, v147, s49, v146\n"
"v_mul_u32_u24 v146, 1280, v146\n"
"v_add3_u32 v178, v146, v132, s50\n"
"v_and_b32 v146, 3, v127\n"
"v_add_u32 v146, s46, v146\n"
"v_bfe_u32 v147, v127, 2, 1\n"
"v_mad_u32_u24 v146, v147, s47, v146\n"
"v_bfe_u32 v147, v127, 3, 1\n"
"v_mad_u32_u24 v146, v147, s48, v146\n"
"v_lshrrev_b32 v147, 4, v127\n"
"v_mad_u32_u24 v146, v147, s49, v146\n"
"v_mul_u32_u24 v146, 1280, v146\n"
"v_add3_u32 v179, v146, v133, s50\n"
"v_and_b32 v146, 3, v128\n"
"v_add_u32 v146, s46, v146\n"
"v_bfe_u32 v147, v128, 2, 1\n"
"v_mad_u32_u24 v146, v147, s47, v146\n"
"v_bfe_u32 v147, v128, 3, 1\n"
"v_mad_u32_u24 v146, v147, s48, v146\n"
"v_lshrrev_b32 v147, 4, v128\n"
"v_mad_u32_u24 v146, v147, s49, v146\n"
"v_mul_u32_u24 v146, 1280, v146\n"
"v_add3_u32 v180, v146, v134, s50\n"
"s_branch LrE7_%=\n"
"LrB5_%=:\n"
"s_and_b32 s52, s7, 3\n"
"s_and_b32 s53, s7, 0xfc\n"
"s_add_u32 s54, s53, s22\n"
"s_lshl_b32 s75, s54, 5\n"
"s_cmp_eq_u32 s52, 0\n"
"s_cselect_b32 s75, s75, 2147418112\n"
"s_mul_i32 s54, s54, 21504\n"
"s_add_u32 s54, s54, 2360320\n"
"s_mul_i32 s76, s52, 5120\n"
"s_add_u32 s76, s76, s54\n"
"s_lshl_b32 s77, s52, 8\n"
"s_add_u32 s77, s77, s54\n"
"s_add_u32 s77, s77, 20480\n"
"s_lshl_b32 s53, s53, 5\n"
"s_lshl_b32 s52, s52, 2\n"
"s_add_u32 s52, s52, s22\n"
"s_mul_i32 s52, s52, 138240\n"
"s_add_u32 s53, s53, s52\n"
"v_lshrrev_b32 v146, 3, v4\n"
"v_mul_u32_u24 v146, 3456, v146\n"
"v_and_b32 v147, 0x70, v1\n"
"v_lshlrev_b32 v176, 1, v4\n"
"v_and_b32 v176, 32, v176\n"
"v_xor_b32 v147, v147, v176\n"
"s_lshl_b32 s52, s22, 6\n"
"s_and_b32 s52, s52, 64\n"
"v_xor_b32 v147, s52, v147\n"
"v_add3_u32 v176, v146, v147, s53\n"
"v_xor_b32 v147, 64, v147\n"
"v_add3_u32 v177, v146, v147, s53\n"
"v_add_u32 v180, 110592, v176\n"
"v_add_u32 v179, 82944, v177\n"
"v_add_u32 v178, 55296, v176\n"
"v_add_u32 v177, 27648, v177\n"
"LrE7_%=:\n"
"buffer_load_dword v122, v121, s[80:83], s8 offen\n"
"s_add_u32 m0, s61, 0\n"
"s_nop 0\n"
"buffer_load_dwordx4 v194, s[16:19], s65 offen offset:0 lds\n"
"s_add_u32 m0, s61, 1024\n"
"s_nop 0\n"
"buffer_load_dwordx4 v195, s[16:19], s65 offen offset:0 lds\n"
"s_add_u32 m0, s61, 1920\n"
"s_nop 0\n"
"buffer_load_dwordx4 v194, s[16:19], s65 offen offset:128 lds\n"
"s_add_u32 m0, s61, 2944\n"
"s_nop 0\n"
"buffer_load_dwordx4 v195, s[16:19], s65 offen offset:128 lds\n"
"s_mov_b64 exec, 0\n"
"s_cmp_eq_u32 s22, 0\n"
"s_cbranch_scc1 Lpad5_%=\n"
"s_cmp_eq_u32 s22, 1\n"
"s_cbranch_scc1 Lpad6_%=\n"
"s_cmp_eq_u32 s22, 2\n"
"s_cbranch_scc1 Lpad7_%=\n"
"s_branch Lpad8_%=\n"
"Lpadback1_%=:\n"
"s_mov_b64 exec, -1\n"
"s_add_u32 m0, s61, 3840\n"
"s_nop 0\n"
"buffer_load_dwordx4 v194, s[16:19], s65 offen offset:256 lds\n"
"s_add_u32 m0, s61, 4864\n"
"s_nop 0\n"
"buffer_load_dwordx4 v195, s[16:19], s65 offen offset:256 lds\n"
"s_add_u32 m0, s61, 5760\n"
"s_nop 0\n"
"buffer_load_dwordx4 v194, s[16:19], s65 offen offset:384 lds\n"
"s_add_u32 m0, s61, 6784\n"
"s_nop 0\n"
"buffer_load_dwordx4 v195, s[16:19], s65 offen offset:384 lds\n"
"v_add_u32 v60, 122496, v1\n"
"v_add_u32 v61, 124544, v1\n"
"v_and_b32 v156, 0x1f0, v1\n"
"v_min_u32 v157, 32, v1\n"
"s_add_u32 s46, s78, 127616\n"
"v_add_u32 v164, s46, v157\n"
"v_mov_b32 v63, 127648\n"
"v_mov_b32 v64, 127648\n"
"v_mov_b32 v65, 127648\n"
"v_mov_b32 v66, 127648\n"
"v_mov_b64 v[160:161], 0\n"
"v_mov_b64 v[162:163], 0\n"
"s_mov_b32 exec_lo, 0\n"
"v_mov_b32 v61, 127632\n"
"v_add_u32 v156, 320, v156\n"
"s_mov_b32 exec_lo, 0x11000000\n"
"s_mov_b32 exec_hi, 0x11000000\n"
"v_mov_b32 v63, 127616\n"
"v_mov_b32 v64, 127616\n"
"v_mov_b32 v65, 127616\n"
"v_mov_b32 v66, 127616\n"
"s_mov_b32 exec_lo, 0xfffff\n"
"s_mov_b32 exec_hi, 0xfffff\n"
"v_add_u32 v63, 125056, v156\n"
"v_add_u32 v64, 125696, v156\n"
"v_add_u32 v65, 126336, v156\n"
"v_add_u32 v66, 126976, v156\n"
"s_mov_b64 exec, 3\n"
"v_mov_b32 v160, 0x3f803f80\n"
"v_mov_b32 v161, 0x3f803f80\n"
"v_mov_b32 v162, 0x3f803f80\n"
"v_mov_b32 v163, 0x3f803f80\n"
"s_mov_b64 exec, 2\n"
"v_mov_b32 v161, 0x3f80\n"
"v_mov_b32 v163, 0x3f80\n"
"s_mov_b64 exec, -1\n"
"ds_write_b128 v164, v[160:163] offset:20672\n"
"v_accvgpr_write_b32 a200, v60\n"
"v_accvgpr_write_b32 a201, v61\n"
"v_accvgpr_write_b32 a202, v63\n"
"v_accvgpr_write_b32 a203, v64\n"
"v_accvgpr_write_b32 a204, v65\n"
"v_accvgpr_write_b32 a205, v66\n"
"v_accvgpr_write_b32 a206, v160\n"
"v_accvgpr_write_b32 a207, v161\n"
"v_accvgpr_write_b32 a208, v162\n"
"v_accvgpr_write_b32 a209, v163\n"
"v_accvgpr_write_b32 a210, v164\n"
"s_add_u32 m0, s61, 7680\n"
"s_nop 0\n"
"buffer_load_dwordx4 v194, s[16:19], s65 offen offset:512 lds\n"
"s_add_u32 m0, s61, 8704\n"
"s_nop 0\n"
"buffer_load_dwordx4 v195, s[16:19], s65 offen offset:512 lds\n"
"s_add_u32 m0, s61, 9600\n"
"s_nop 0\n"
"buffer_load_dwordx4 v194, s[16:19], s65 offen offset:640 lds\n"
"s_mul_i32 s73, s22, 5120\n"
"v_and_b32 v144, 15, v4\n"
"v_lshrrev_b32 v145, 2, v144\n"
"v_and_b32 v146, 3, v144\n"
"v_lshrrev_b32 v147, 4, v4\n"
"s_cmp_lt_u32 s7, 108\n"
"s_cbranch_scc1 LaB8_%=\n"
"s_add_u32 s47, s73, 102400\n"
"v_lshl_add_u32 v145, v147, 2, v145\n"
"v_mul_u32_u24 v145, 160, v145\n"
"v_add_u32 v145, s47, v145\n"
"v_lshl_add_u32 v157, v146, 4, v145\n"
"v_and_b32 v147, 1, v146\n"
"v_lshl_add_u32 v145, v147, 4, v145\n"
"v_lshrrev_b32 v147, 1, v146\n"
"v_lshl_add_u32 v174, v147, 3, v145\n"
"s_branch LaE9_%=\n"
"LaB8_%=:\n"
"v_and_b32 v144, 1, v147\n"
"v_lshrrev_b32 v174, 1, v145\n"
"v_lshl_add_u32 v144, v144, 1, v174\n"
"v_xor_b32 v144, s22, v144\n"
"v_lshlrev_b32 v144, 5, v144\n"
"v_lshl_add_u32 v146, v146, 3, v144\n"
"v_add_u32 v146, 102400, v146\n"
"v_lshl_add_u32 v146, v145, 7, v146\n"
"v_lshl_add_u32 v157, v147, 10, v146\n"
"LaE9_%=:\n"
"s_add_u32 s10, s76, 4096\n"
"s_mul_i32 s47, s21, 129024\n"
"s_add_u32 s47, s47, 6894592\n"
"s_lshl_b32 s48, s20, 2\n"
"s_lshl_b32 s49, s20, 1\n"
"s_add_u32 s49, s49, 81\n"
"s_add_u32 s50, s48, s22\n"
"s_add_u32 s50, s50, 1\n"
"s_cmp_lt_u32 s22, 3\n"
"s_cselect_b32 s50, s50, s49\n"
"s_add_u32 s49, s49, 1\n"
"s_lshl_b32 s48, s48, 10\n"
"s_add_u32 s48, s48, s47\n"
"s_lshl_b32 s49, s49, 10\n"
"s_add_u32 s49, s49, s47\n"
"s_lshl_b32 s50, s50, 10\n"
"s_add_u32 s50, s50, s47\n"
"s_cmp_eq_u32 s22, 0\n"
"s_cselect_b32 s49, s49, 2147418112\n"
"s_cmp_eq_u32 s22, 1\n"
"s_cselect_b32 s48, s48, 2147418112\n"
"s_cmp_lt_u32 s20, 20\n"
"s_cselect_b32 s72, s48, 2147418112\n"
"s_cselect_b32 s71, s49, 2147418112\n"
"s_cselect_b32 s11, s50, 2147418112\n"
"s_lshl_b32 s48, s6, 2\n"
"s_add_u32 s48, s48, s22\n"
"s_lshl_b32 s48, s48, 10\n"
"s_add_u32 s84, s48, 164864\n"
"s_add_u32 m0, s61, 10624\n"
"s_nop 0\n"
"buffer_load_dwordx4 v195, s[16:19], s65 offen offset:640 lds\n"
"s_add_u32 m0, s61, 11520\n"
"s_nop 0\n"
"buffer_load_dwordx4 v194, s[16:19], s65 offen offset:768 lds\n"
"s_add_u32 m0, s61, 12544\n"
"s_nop 0\n"
"buffer_load_dwordx4 v195, s[16:19], s65 offen offset:768 lds\n"
"s_add_u32 m0, s61, 13440\n"
"s_nop 0\n"
"buffer_load_dwordx4 v194, s[16:19], s65 offen offset:896 lds\n"
"s_add_u32 m0, s61, 14464\n"
"s_nop 0\n"
"buffer_load_dwordx4 v195, s[16:19], s65 offen offset:896 lds\n"
"s_add_u32 m0, s61, 15360\n"
"s_nop 0\n"
"buffer_load_dwordx4 v194, s[16:19], s65 offen offset:1024 lds\n"
"s_add_u32 m0, s61, 16384\n"
"s_nop 0\n"
"buffer_load_dwordx4 v195, s[16:19], s65 offen offset:1024 lds\n"
"v_and_b32 v162, 15, v4\n"
"v_lshrrev_b32 v163, 4, v4\n"
"v_lshrrev_b32 v164, 2, v162\n"
"v_and_b32 v162, 3, v162\n"
"v_lshl_add_u32 v156, v163, 3, v164\n"
"v_and_b32 v165, 1, v163\n"
"v_mul_u32_u24 v164, 40, v162\n"
"v_mul_u32_u24 v165, 640, v165\n"
"v_mul_u32_u24 v163, 160, v156\n"
"v_add_u32 v163, v163, v164\n"
"v_add_u32 v152, v163, v165\n"
"v_sub_u32 v163, v163, v165\n"
"v_add_u32 v154, 640, v163\n"
"v_add_u32 v153, 51200, v152\n"
"v_add_u32 v155, 51200, v154\n"
"s_lshl_b32 s46, s35, 5\n"
"s_add_u32 s46, s46, 102400\n"
"v_lshl_add_u32 v164, v162, 3, s46\n"
"v_mad_u32_u24 v156, v156, 40, v164\n"
"v_and_b32 v144, 48, v4\n"
"v_lshlrev_b32 v145, 2, v4\n"
"v_cmp_eq_u32 vcc, s34, 1\n"
"v_add_u32 v148, 0, v144\n"
"v_add_u32 v149, 4, v144\n"
"v_add_u32 v150, 8, v144\n"
"v_add_u32 v151, 12, v144\n"
"v_cndmask_b32 v148, v145, v148, vcc\n"
"v_cndmask_b32 v149, v145, v149, vcc\n"
"v_cndmask_b32 v150, v145, v150, vcc\n"
"v_cndmask_b32 v151, v145, v151, vcc\n"
"s_lshl_b32 s63, s26, 2\n"
"s_add_u32 s63, s63, s22\n"
"s_mul_i32 s63, s63, 5120\n"
"s_add_u32 s46, s63, s28\n"
"s_add_u32 s68, s4, s46\n"
"s_addc_u32 s69, s5, 0\n"
"v_mov_b32 v196, 0x3f803f80\n"
"v_mov_b32 v197, 0x3f803f80\n"
"v_mov_b32 v198, 0x3f803f80\n"
"v_mov_b32 v199, 0x3f803f80\n"
"s_add_u32 m0, s61, 17280\n"
"s_nop 0\n"
"buffer_load_dwordx4 v194, s[16:19], s65 offen offset:1152 lds\n"
"s_add_u32 m0, s61, 18304\n"
"s_nop 0\n"
"buffer_load_dwordx4 v195, s[16:19], s65 offen offset:1152 lds\n"
"buffer_load_dword v135, v121, s[80:83], s9 offen\n"
"s_add_u32 s59, s60, 163840\n"
"s_add_u32 m0, s61, 20480\n"
"s_nop 0\n"
"buffer_load_dwordx4 v168, s[80:83], s59 offen offset:0 lds\n"
"s_mul_i32 s52, s20, 5120\n"
"s_add_u32 s52, s52, s28\n"
"s_cmp_lt_u32 s21, 4\n"
"s_cselect_b32 s52, s52, 2147418112\n"
"s_lshl_b32 s53, s20, 2\n"
"s_add_u32 s53, s53, s21\n"
"s_lshl_b32 s53, s53, 7\n"
"s_add_u32 s53, s53, s62\n"
"s_cmp_lt_u32 s21, 4\n"
"s_cselect_b32 s53, s53, 2147418112\n"
"s_cmp_eq_u32 s22, 0\n"
"s_cselect_b32 s53, s53, 2147418112\n"
"v_and_b32 v162, 15, v4\n"
"v_lshlrev_b32 v211, 1, v162\n"
"s_lshl_b32 s49, s22, 8\n"
"s_add_u32 s49, s49, 135168\n"
"v_lshl_add_u32 v163, v162, 4, s49\n"
"v_add_u32 v160, 135168, v1\n"
"v_and_b32 v165, 12, v4\n"
"v_lshl_add_u32 v164, v165, 2, v162\n"
"v_lshlrev_b32 v164, 2, v164\n"
"s_min_u32 s74, s20, 19\n"
"s_mul_i32 s49, s74, 5120\n"
"s_lshl_b32 s50, s22, 3\n"
"s_add_u32 s50, s50, s49\n"
"v_add_u32 v158, s50, v152\n"
"v_add_u32 v159, s50, v154\n"
"s_mul_i32 s51, s74, 1280\n"
"v_add_u32 v161, s51, v156\n"
"s_waitcnt vmcnt(30)\n"
"v_readfirstlane_b32 s40, v214\n"
"v_readfirstlane_b32 s41, v215\n"
"s_mul_i32 s40, s40, 0x9e3779b1\n"
"s_add_u32 s40, s40, 0x7f4a7c15\n"
"s_add_u32 s41, s41, 1\n"
"s_waitcnt vmcnt(23)\n"
"s_barrier\n"
"ds_read_b64_tr_b16 a[0:1], v156 offset:0\n"
"ds_read_b64_tr_b16 a[2:3], v156 offset:160\n"
"ds_read_b64_tr_b16 a[4:5], v156 offset:1280\n"
"ds_read_b64_tr_b16 a[6:7], v156 offset:1440\n"
"ds_read_b64_tr_b16 a[8:9], v156 offset:2560\n"
"ds_read_b64_tr_b16 a[10:11], v156 offset:2720\n"
"ds_read_b64_tr_b16 a[12:13], v156 offset:3840\n"
"ds_read_b64_tr_b16 a[14:15], v156 offset:4000\n"
"ds_read_b64_tr_b16 a[16:17], v156 offset:5120\n"
"ds_read_b64_tr_b16 a[18:19], v156 offset:5280\n"
"s_waitcnt vmcnt(2)\n"
"ds_read_b128 v[200:203], v200\n"
"ds_read_b128 v[204:207], v204\n"
"ds_read_b64 v[208:209], v208\n"
"Lpad1_%=:\n"
"s_cbranch_execz Lpadback0_%=\n"
"ds_read_b128 v[32:35], v212 offset:0\n"
"ds_read_b128 v[36:39], v213 offset:0\n"
"ds_read_b128 v[40:43], v212 offset:2048\n"
"ds_read_b128 v[44:47], v213 offset:2048\n"
"buffer_load_dwordx4 v169, s[80:83], s59 offen offset:1024 lds\n"
"s_waitcnt lgkmcnt(2)\n"
"v_mfma_f32_16x16x32_bf16 v[124:127], v[196:199], v[32:35], 0\n"
"ds_read_b128 v[48:51], v212 offset:4096\n"
"ds_read_b64_tr_b16 a[20:21], v156 offset:6400\n"
"v_mfma_f32_16x16x32_bf16 v[124:127], v[196:199], v[36:39], v[124:127]\n"
"ds_read_b128 v[52:55], v213 offset:4096\n"
"ds_read_b64_tr_b16 a[22:23], v156 offset:6560\n"
"v_mfma_f32_16x16x32_bf16 v[128:131], v[32:35], v[32:35], 0\n"
"ds_read_b64_tr_b16 a[24:25], v156 offset:7680\n"
"ds_read_b64_tr_b16 a[26:27], v156 offset:7840\n"
"v_mfma_f32_16x16x32_bf16 v[128:131], v[36:39], v[36:39], v[128:131]\n"
"s_waitcnt lgkmcnt(6)\n"
"buffer_load_dwordx4 v170, s[80:83], s59 offen offset:2048 lds\n"
"v_mfma_f32_16x16x32_bf16 v[124:127], v[196:199], v[40:43], v[124:127]\n"
"ds_read_b128 v[56:59], v212 offset:6144\n"
"ds_read_b64_tr_b16 a[28:29], v156 offset:8960\n"
"v_mfma_f32_16x16x32_bf16 v[124:127], v[196:199], v[44:47], v[124:127]\n"
"ds_read_b128 v[60:63], v213 offset:6144\n"
"ds_read_b64_tr_b16 a[30:31], v156 offset:9120\n"
"v_mfma_f32_16x16x32_bf16 v[128:131], v[40:43], v[40:43], v[128:131]\n"
"ds_read_b64_tr_b16 a[32:33], v156 offset:10240\n"
"ds_read_b64_tr_b16 a[34:35], v156 offset:10400\n"
"v_mfma_f32_16x16x32_bf16 v[128:131], v[44:47], v[44:47], v[128:131]\n"
"s_waitcnt lgkmcnt(9)\n"
"buffer_load_dwordx4 v171, s[80:83], s59 offen offset:3072 lds\n"
"v_mfma_f32_16x16x32_bf16 v[124:127], v[196:199], v[48:51], v[124:127]\n"
"ds_read_b128 v[64:67], v212 offset:8192\n"
"ds_read_b64_tr_b16 a[36:37], v156 offset:11520\n"
"v_mfma_f32_16x16x32_bf16 v[124:127], v[196:199], v[52:55], v[124:127]\n"
"ds_read_b128 v[68:71], v213 offset:8192\n"
"ds_read_b64_tr_b16 a[38:39], v156 offset:11680\n"
"v_mfma_f32_16x16x32_bf16 v[128:131], v[48:51], v[48:51], v[128:131]\n"
"ds_read_b64_tr_b16 a[40:41], v156 offset:12800\n"
"ds_read_b64_tr_b16 a[42:43], v156 offset:12960\n"
"v_mfma_f32_16x16x32_bf16 v[128:131], v[52:55], v[52:55], v[128:131]\n"
"s_waitcnt lgkmcnt(9)\n"
"ds_read_b64_tr_b16 a[44:45], v156 offset:14080\n"
"ds_read_b64_tr_b16 a[46:47], v156 offset:14240\n"
"v_mfma_f32_16x16x32_bf16 v[124:127], v[196:199], v[56:59], v[124:127]\n"
"ds_read_b128 v[72:75], v212 offset:10240\n"
"ds_read_b64_tr_b16 a[48:49], v156 offset:15360\n"
"v_mfma_f32_16x16x32_bf16 v[124:127], v[196:199], v[60:63], v[124:127]\n"
"ds_read_b128 v[76:79], v213 offset:10240\n"
"ds_read_b64_tr_b16 a[50:51], v156 offset:15520\n"
"v_mfma_f32_16x16x32_bf16 v[128:131], v[56:59], v[56:59], v[128:131]\n"
"ds_read_b64_tr_b16 a[52:53], v156 offset:16640\n"
"ds_read_b64_tr_b16 a[54:55], v156 offset:16800\n"
"v_mfma_f32_16x16x32_bf16 v[128:131], v[60:63], v[60:63], v[128:131]\n"
"s_waitcnt lgkmcnt(11)\n"
"s_add_u32 m0, s61, 24576\n"
"s_nop 0\n"
"buffer_load_dwordx4 v172, s[80:83], s59 offen offset:0 lds\n"
"v_mfma_f32_16x16x32_bf16 v[124:127], v[196:199], v[64:67], v[124:127]\n"
"ds_read_b128 v[80:83], v212 offset:12288\n"
"ds_read_b64_tr_b16 a[56:57], v156 offset:17920\n"
"v_mfma_f32_16x16x32_bf16 v[124:127], v[196:199], v[68:71], v[124:127]\n"
"ds_read_b128 v[84:87], v213 offset:12288\n"
"ds_read_b64_tr_b16 a[58:59], v156 offset:18080\n"
"v_mfma_f32_16x16x32_bf16 v[128:131], v[64:67], v[64:67], v[128:131]\n"
"ds_read_b64_tr_b16 a[60:61], v156 offset:19200\n"
"ds_read_b64_tr_b16 a[62:63], v156 offset:19360\n"
"v_mfma_f32_16x16x32_bf16 v[128:131], v[68:71], v[68:71], v[128:131]\n"
"s_waitcnt lgkmcnt(9)\n"
"s_add_u32 s59, s60, 0\n"
"s_add_u32 m0, s61, 0\n"
"s_nop 0\n"
"buffer_load_dwordx4 v168, s[80:83], s59 offen offset:0 lds\n"
"v_mfma_f32_16x16x32_bf16 v[124:127], v[196:199], v[72:75], v[124:127]\n"
"ds_read_b128 v[88:91], v212 offset:14336\n"
"ds_read_b64_tr_b16 a[64:65], v156 offset:20480\n"
"v_mfma_f32_16x16x32_bf16 v[124:127], v[196:199], v[76:79], v[124:127]\n"
"ds_read_b128 v[92:95], v213 offset:14336\n"
"ds_read_b64_tr_b16 a[66:67], v156 offset:20640\n"
"v_mfma_f32_16x16x32_bf16 v[128:131], v[72:75], v[72:75], v[128:131]\n"
"ds_read_b64_tr_b16 a[68:69], v156 offset:21760\n"
"ds_read_b64_tr_b16 a[70:71], v156 offset:21920\n"
"v_mfma_f32_16x16x32_bf16 v[128:131], v[76:79], v[76:79], v[128:131]\n"
"s_waitcnt lgkmcnt(9)\n"
"ds_read_b64_tr_b16 a[72:73], v156 offset:23040\n"
"ds_read_b64_tr_b16 a[74:75], v156 offset:23200\n"
"v_mfma_f32_16x16x32_bf16 v[124:127], v[196:199], v[80:83], v[124:127]\n"
"ds_read_b128 v[96:99], v212 offset:16384\n"
"ds_read_b64_tr_b16 a[76:77], v156 offset:24320\n"
"v_mfma_f32_16x16x32_bf16 v[124:127], v[196:199], v[84:87], v[124:127]\n"
"ds_read_b128 v[100:103], v213 offset:16384\n"
"ds_read_b64_tr_b16 a[78:79], v156 offset:24480\n"
"v_mfma_f32_16x16x32_bf16 v[128:131], v[80:83], v[80:83], v[128:131]\n"
"ds_read_b64_tr_b16 v[224:225], v161 offset:0\n"
"ds_read_b64_tr_b16 v[226:227], v161 offset:160\n"
"v_mfma_f32_16x16x32_bf16 v[128:131], v[84:87], v[84:87], v[128:131]\n"
"s_waitcnt lgkmcnt(11)\n"
"buffer_load_dwordx4 v169, s[80:83], s59 offen offset:1024 lds\n"
"v_mfma_f32_16x16x32_bf16 v[124:127], v[196:199], v[88:91], v[124:127]\n"
"ds_read_b128 v[104:107], v212 offset:18432\n"
"v_mfma_f32_16x16x32_bf16 v[124:127], v[196:199], v[92:95], v[124:127]\n"
"ds_read_b128 v[108:111], v213 offset:18432\n"
"v_mfma_f32_16x16x32_bf16 v[128:131], v[88:91], v[88:91], v[128:131]\n"
"v_mfma_f32_16x16x32_bf16 v[128:131], v[92:95], v[92:95], v[128:131]\n"
"s_waitcnt lgkmcnt(5)\n"
"buffer_load_dwordx4 v170, s[80:83], s59 offen offset:2048 lds\n"
"v_mfma_f32_16x16x32_bf16 v[124:127], v[196:199], v[96:99], v[124:127]\n"
"v_mfma_f32_16x16x32_bf16 v[124:127], v[196:199], v[100:103], v[124:127]\n"
"v_mfma_f32_16x16x32_bf16 v[128:131], v[96:99], v[96:99], v[128:131]\n"
"v_mfma_f32_16x16x32_bf16 v[128:131], v[100:103], v[100:103], v[128:131]\n"
"s_waitcnt lgkmcnt(0)\n"
"v_mfma_f32_16x16x32_bf16 v[124:127], v[196:199], v[104:107], v[124:127]\n"
"v_mfma_f32_16x16x32_bf16 v[124:127], v[196:199], v[108:111], v[124:127]\n"
"v_mfma_f32_16x16x32_bf16 v[128:131], v[104:107], v[104:107], v[128:131]\n"
"v_mfma_f32_16x16x32_bf16 v[128:131], v[108:111], v[108:111], v[128:131]\n"
"buffer_load_dwordx4 v171, s[80:83], s59 offen offset:3072 lds\n"
"s_waitcnt lgkmcnt(0)\n"
"v_mfma_f32_16x16x32_bf16 v[120:123], v[196:199], a[0:3], 0\n"
"v_mfma_f32_16x16x32_bf16 v[120:123], v[196:199], a[4:7], v[120:123]\n"
"v_mfma_f32_16x16x32_bf16 v[120:123], v[196:199], a[8:11], v[120:123]\n"
"v_mfma_f32_16x16x32_bf16 v[120:123], v[196:199], a[12:15], v[120:123]\n"
"s_add_u32 m0, s61, 4096\n"
"s_nop 0\n"
"buffer_load_dwordx4 v172, s[80:83], s59 offen offset:0 lds\n"
"v_mfma_f32_16x16x32_bf16 v[120:123], v[196:199], a[16:19], v[120:123]\n"
"s_mov_b32 vcc_lo, 0x22222222\n"
"s_mov_b32 vcc_hi, 0x22222222\n"
"v_cndmask_b32 v128, v128, v129, vcc\n"
"v_mfma_f32_16x16x32_bf16 v[120:123], v[196:199], a[20:23], v[120:123]\n"
"s_mov_b32 vcc_lo, 0x44444444\n"
"s_mov_b32 vcc_hi, 0x44444444\n"
"v_cndmask_b32 v128, v128, v130, vcc\n"
"v_mfma_f32_16x16x32_bf16 v[120:123], v[196:199], a[24:27], v[120:123]\n"
"s_mov_b32 vcc_lo, 0x88888888\n"
"s_mov_b32 vcc_hi, 0x88888888\n"
"v_cndmask_b32 v128, v128, v131, vcc\n"
"v_mfma_f32_16x16x32_bf16 v[120:123], v[196:199], a[28:31], v[120:123]\n"
"ds_bpermute_b32 v141, v164, v128\n"
"v_mov_b32 v140, v124\n"
"v_lshlrev_b32 v12, 16, v200\n"
"v_and_b32 v13, 0xffff0000, v200\n"
"v_lshlrev_b32 v14, 16, v201\n"
"v_mfma_f32_16x16x32_bf16 v[120:123], v[196:199], a[32:35], v[120:123]\n"
"v_and_b32 v15, 0xffff0000, v201\n"
"v_lshlrev_b32 v16, 16, v202\n"
"v_mfma_f32_16x16x32_bf16 v[120:123], v[196:199], a[36:39], v[120:123]\n"
"v_and_b32 v17, 0xffff0000, v202\n"
"v_lshlrev_b32 v18, 16, v203\n"
"v_mfma_f32_16x16x32_bf16 v[120:123], v[196:199], a[40:43], v[120:123]\n"
"s_add_u32 s59, s60, 40960\n"
"s_add_u32 m0, s61, 5120\n"
"s_nop 0\n"
"buffer_load_dwordx4 v168, s[80:83], s59 offen offset:0 lds\n"
"v_mfma_f32_16x16x32_bf16 v[120:123], v[196:199], a[44:47], v[120:123]\n"
"v_and_b32 v19, 0xffff0000, v203\n"
"v_lshlrev_b32 v20, 16, v204\n"
"v_mfma_f32_16x16x32_bf16 v[120:123], v[196:199], a[48:51], v[120:123]\n"
"v_and_b32 v21, 0xffff0000, v204\n"
"v_lshlrev_b32 v22, 16, v205\n"
"v_mfma_f32_16x16x32_bf16 v[120:123], v[196:199], a[52:55], v[120:123]\n"
"v_and_b32 v23, 0xffff0000, v205\n"
"v_lshlrev_b32 v24, 16, v206\n"
"v_mfma_f32_16x16x32_bf16 v[120:123], v[196:199], a[56:59], v[120:123]\n"
"v_and_b32 v25, 0xffff0000, v206\n"
"v_lshlrev_b32 v26, 16, v207\n"
"v_mfma_f32_16x16x32_bf16 v[120:123], v[196:199], a[60:63], v[120:123]\n"
"v_and_b32 v27, 0xffff0000, v207\n"
"v_lshlrev_b32 v28, 16, v208\n"
"v_mfma_f32_16x16x32_bf16 v[120:123], v[196:199], a[64:67], v[120:123]\n"
"v_and_b32 v29, 0xffff0000, v208\n"
"v_lshlrev_b32 v30, 16, v209\n"
"v_mfma_f32_16x16x32_bf16 v[120:123], v[196:199], a[68:71], v[120:123]\n"
"buffer_load_dwordx4 v169, s[80:83], s59 offen offset:1024 lds\n"
"v_mfma_f32_16x16x32_bf16 v[120:123], v[196:199], a[72:75], v[120:123]\n"
"v_and_b32 v31, 0xffff0000, v209\n"
"v_mfma_f32_16x16x32_bf16 v[120:123], v[196:199], a[76:79], v[120:123]\n"
"s_cmp_eq_u32 s34, 1\n"
"s_cbranch_scc1 Lva10_%=\n"
"v_mfma_f32_16x16x32_bf16 v[136:139], a[0:3], v[32:35], 0\n"
"v_mul_f32 v140, 0xbacccccd, v140\n"
"s_waitcnt lgkmcnt(0)\n"
"v_mul_f32 v141, 0x3acccccd, v141\n"
"v_mfma_f32_16x16x32_bf16 v[136:139], a[4:7], v[36:39], v[136:139]\n"
"v_fma_f32 v141, -v140, v140, v141\n"
"v_max_f32 v142, 0, v141\n"
"v_mfma_f32_16x16x32_bf16 v[136:139], a[8:11], v[40:43], v[136:139]\n"
"v_fma_f32 v141, v141, 0, v142\n"
"v_add_f32 v142, 0x3727c5ac, v141\n"
"v_mfma_f32_16x16x32_bf16 v[136:139], a[12:15], v[44:47], v[136:139]\n"
"buffer_load_dwordx4 v170, s[80:83], s59 offen offset:2048 lds\n"
"v_mfma_f32_16x16x32_bf16 v[136:139], a[16:19], v[48:51], v[136:139]\n"
"v_rsq_f32 v143, v142\n"
"v_mfma_f32_16x16x32_bf16 v[136:139], a[20:23], v[52:55], v[136:139]\n"
"v_and_b32 v115, 0xffff0000, v140\n"
"v_sub_f32 v114, v140, v115\n"
"v_mfma_f32_16x16x32_bf16 v[136:139], a[24:27], v[56:59], v[136:139]\n"
"v_cvt_pk_bf16_f32 v112, v115, v114\n"
"v_mov_b32 v140, v143\n"
"v_mfma_f32_16x16x32_bf16 v[136:139], a[28:31], v[60:63], v[136:139]\n"
"v_cvt_pk_bf16_f32 v113, v115, v2\n"
"v_mov_b32 v114, 0\n"
"v_mfma_f32_16x16x32_bf16 v[136:139], a[32:35], v[64:67], v[136:139]\n"
"v_mov_b32 v115, 0\n"
"v_mfma_f32_16x16x32_bf16 v[136:139], a[36:39], v[68:71], v[136:139]\n"
"ds_bpermute_b32 v148, v148, v140\n"
"v_mfma_f32_16x16x32_bf16 v[136:139], a[40:43], v[72:75], v[136:139]\n"
"ds_bpermute_b32 v149, v149, v140\n"
"buffer_load_dwordx4 v171, s[80:83], s59 offen offset:3072 lds\n"
"v_mfma_f32_16x16x32_bf16 v[136:139], a[44:47], v[76:79], v[136:139]\n"
"ds_bpermute_b32 v150, v150, v140\n"
"v_mfma_f32_16x16x32_bf16 v[136:139], a[48:51], v[80:83], v[136:139]\n"
"ds_bpermute_b32 v151, v151, v140\n"
"v_mul_f32 v142, 0x3ea52b5c, v140\n"
"v_mfma_f32_16x16x32_bf16 v[136:139], a[52:55], v[84:87], v[136:139]\n"
"s_mov_b32 vcc_lo, 0xffff\n"
"s_mov_b32 vcc_hi, 0\n"
"v_cndmask_b32 v120, 0, v120, vcc\n"
"v_and_b32 v253, 0xffff0000, v120\n"
"v_cvt_pk_bf16_f32 v252, v253, v253\n"
"v_sub_f32 v253, v120, v253\n"
"v_mfma_f32_16x16x32_bf16 v[136:139], a[56:59], v[88:91], v[136:139]\n"
"v_cvt_pk_bf16_f32 v253, v253, v2\n"
"v_mov_b32 v254, 0\n"
"v_mfma_f32_16x16x32_bf16 v[136:139], a[60:63], v[92:95], v[136:139]\n"
"v_mov_b32 v255, 0\n"
"v_mfma_f32_16x16x32_bf16 v[136:139], a[64:67], v[96:99], v[136:139]\n"
"v_mfma_f32_16x16x32_bf16 v[136:139], a[68:71], v[100:103], v[136:139]\n"
"s_add_u32 m0, s61, 9216\n"
"s_nop 0\n"
"buffer_load_dwordx4 v172, s[80:83], s59 offen offset:0 lds\n"
"v_mfma_f32_16x16x32_bf16 v[136:139], a[72:75], v[104:107], v[136:139]\n"
"v_mfma_f32_16x16x32_bf16 v[136:139], a[76:79], v[108:111], v[136:139]\n"
"v_mfma_f32_16x16x32_bf16 v[136:139], v[252:255], v[112:115], v[136:139]\n"
"s_branch Lve11_%=\n"
"Lva10_%=:\n"
"v_mfma_f32_16x16x32_bf16 v[136:139], v[32:35], a[0:3], 0\n"
"v_mul_f32 v140, 0xbacccccd, v140\n"
"s_waitcnt lgkmcnt(0)\n"
"v_mul_f32 v141, 0x3acccccd, v141\n"
"v_mfma_f32_16x16x32_bf16 v[136:139], v[36:39], a[4:7], v[136:139]\n"
"v_fma_f32 v141, -v140, v140, v141\n"
"v_max_f32 v142, 0, v141\n"
"v_mfma_f32_16x16x32_bf16 v[136:139], v[40:43], a[8:11], v[136:139]\n"
"v_fma_f32 v141, v141, 0, v142\n"
"v_add_f32 v142, 0x3727c5ac, v141\n"
"v_mfma_f32_16x16x32_bf16 v[136:139], v[44:47], a[12:15], v[136:139]\n"
"buffer_load_dwordx4 v170, s[80:83], s59 offen offset:2048 lds\n"
"v_mfma_f32_16x16x32_bf16 v[136:139], v[48:51], a[16:19], v[136:139]\n"
"v_rsq_f32 v143, v142\n"
"v_mfma_f32_16x16x32_bf16 v[136:139], v[52:55], a[20:23], v[136:139]\n"
"v_and_b32 v115, 0xffff0000, v140\n"
"v_sub_f32 v114, v140, v115\n"
"v_mfma_f32_16x16x32_bf16 v[136:139], v[56:59], a[24:27], v[136:139]\n"
"v_cvt_pk_bf16_f32 v112, v115, v114\n"
"v_mov_b32 v140, v143\n"
"v_mfma_f32_16x16x32_bf16 v[136:139], v[60:63], a[28:31], v[136:139]\n"
"v_cvt_pk_bf16_f32 v113, v115, v2\n"
"v_mov_b32 v114, 0\n"
"v_mfma_f32_16x16x32_bf16 v[136:139], v[64:67], a[32:35], v[136:139]\n"
"v_mov_b32 v115, 0\n"
"v_mfma_f32_16x16x32_bf16 v[136:139], v[68:71], a[36:39], v[136:139]\n"
"ds_bpermute_b32 v148, v148, v140\n"
"v_mfma_f32_16x16x32_bf16 v[136:139], v[72:75], a[40:43], v[136:139]\n"
"ds_bpermute_b32 v149, v149, v140\n"
"buffer_load_dwordx4 v171, s[80:83], s59 offen offset:3072 lds\n"
"v_mfma_f32_16x16x32_bf16 v[136:139], v[76:79], a[44:47], v[136:139]\n"
"ds_bpermute_b32 v150, v150, v140\n"
"v_mfma_f32_16x16x32_bf16 v[136:139], v[80:83], a[48:51], v[136:139]\n"
"ds_bpermute_b32 v151, v151, v140\n"
"v_mul_f32 v142, 0x3ea52b5c, v140\n"
"v_mfma_f32_16x16x32_bf16 v[136:139], v[84:87], a[52:55], v[136:139]\n"
"s_mov_b32 vcc_lo, 0xffff\n"
"s_mov_b32 vcc_hi, 0\n"
"v_cndmask_b32 v120, 0, v120, vcc\n"
"v_and_b32 v253, 0xffff0000, v120\n"
"v_cvt_pk_bf16_f32 v252, v253, v253\n"
"v_sub_f32 v253, v120, v253\n"
"v_mfma_f32_16x16x32_bf16 v[136:139], v[88:91], a[56:59], v[136:139]\n"
"v_cvt_pk_bf16_f32 v253, v253, v2\n"
"v_mov_b32 v254, 0\n"
"v_mfma_f32_16x16x32_bf16 v[136:139], v[92:95], a[60:63], v[136:139]\n"
"v_mov_b32 v255, 0\n"
"v_mfma_f32_16x16x32_bf16 v[136:139], v[96:99], a[64:67], v[136:139]\n"
"v_mfma_f32_16x16x32_bf16 v[136:139], v[100:103], a[68:71], v[136:139]\n"
"s_add_u32 m0, s61, 9216\n"
"s_nop 0\n"
"buffer_load_dwordx4 v172, s[80:83], s59 offen offset:0 lds\n"
"v_mfma_f32_16x16x32_bf16 v[136:139], v[104:107], a[72:75], v[136:139]\n"
"v_mfma_f32_16x16x32_bf16 v[136:139], v[108:111], a[76:79], v[136:139]\n"
"v_mfma_f32_16x16x32_bf16 v[136:139], v[112:115], v[252:255], v[136:139]\n"
"Lve11_%=:\n"
"s_add_u32 s59, s60, 81920\n"
"s_add_u32 m0, s61, 10240\n"
"s_nop 0\n"
"buffer_load_dwordx4 v168, s[80:83], s59 offen offset:0 lds\n"
"s_waitcnt lgkmcnt(3)\n"
"s_nop 2\n"
"v_mul_f32 v136, v136, v148\n"
"s_waitcnt lgkmcnt(2)\n"
"v_mul_f32 v137, v137, v149\n"
"s_waitcnt lgkmcnt(1)\n"
"v_mul_f32 v138, v138, v150\n"
"s_waitcnt lgkmcnt(0)\n"
"v_mul_f32 v139, v139, v151\n"
"v_cvt_pk_bf16_f32 v136, v136, v137\n"
"v_cvt_pk_bf16_f32 v137, v138, v139\n"
"s_mov_b64 exec, s[38:39]\n"
"buffer_store_dwordx2 v[136:137], v7, s[12:15], s52 offen sc1\n"
"s_mov_b64 exec, -1\n"
"v_add_u32 v212, s61, v152\n"
"v_add_u32 v213, s61, v154\n"
"s_waitcnt vmcnt(12)\n"
"s_barrier\n"
"ds_read_b64_tr_b16 a[0:1], v152 offset:20480\n"
"ds_read_b64_tr_b16 a[2:3], v154 offset:20480\n"
"ds_read_b64_tr_b16 a[4:5], v152 offset:20488\n"
"ds_read_b64_tr_b16 a[6:7], v154 offset:20488\n"
"ds_read_b64_tr_b16 a[8:9], v152 offset:20496\n"
"ds_read_b64_tr_b16 a[10:11], v154 offset:20496\n"
"ds_read_b64_tr_b16 a[12:13], v152 offset:20504\n"
"ds_read_b64_tr_b16 a[14:15], v154 offset:20504\n"
"ds_read_b64_tr_b16 a[16:17], v152 offset:20512\n"
"ds_read_b64_tr_b16 a[18:19], v154 offset:20512\n"
"ds_read_b64_tr_b16 a[20:21], v152 offset:46080\n"
"ds_read_b64_tr_b16 a[22:23], v154 offset:46080\n"
"ds_read_b64_tr_b16 a[24:25], v152 offset:46088\n"
"ds_read_b64_tr_b16 a[26:27], v154 offset:46088\n"
"buffer_load_dwordx4 v169, s[80:83], s59 offen offset:1024 lds\n"
"buffer_load_dwordx4 v170, s[80:83], s59 offen offset:2048 lds\n"
"s_waitcnt lgkmcnt(12)\n"
"v_mfma_f32_16x16x32_bf16 v[116:119], a[0:3], v[48:51], 0\n"
"s_waitcnt lgkmcnt(10)\n"
"ds_read_b64_tr_b16 a[28:29], v152 offset:46096\n"
"ds_read_b64_tr_b16 a[30:31], v154 offset:46096\n"
"v_mfma_f32_16x16x32_bf16 v[120:123], a[4:7], v[48:51], 0\n"
"s_waitcnt lgkmcnt(10)\n"
"ds_read_b64_tr_b16 a[32:33], v152 offset:46104\n"
"ds_read_b64_tr_b16 a[34:35], v154 offset:46104\n"
"buffer_load_dwordx4 v171, s[80:83], s59 offen offset:3072 lds\n"
"ds_read_b64_tr_b16 a[160:161], v212 offset:20480\n"
"ds_read_b64_tr_b16 a[162:163], v213 offset:20480\n"
"ds_read_b64_tr_b16 a[164:165], v212 offset:20488\n"
"ds_read_b64_tr_b16 a[166:167], v213 offset:20488\n"
"ds_read_b64_tr_b16 a[168:169], v212 offset:20496\n"
"ds_read_b64_tr_b16 a[170:171], v213 offset:20496\n"
"ds_read_b64_tr_b16 a[172:173], v212 offset:20504\n"
"ds_read_b64_tr_b16 a[174:175], v213 offset:20504\n"
"ds_read_b64_tr_b16 a[176:177], v212 offset:20512\n"
"ds_read_b64_tr_b16 a[178:179], v213 offset:20512\n"
"v_mfma_f32_16x16x32_bf16 v[124:127], a[8:11], v[48:51], 0\n"
"s_waitcnt lgkmcnt(15)\n"
"ds_read_b64_tr_b16 a[36:37], v152 offset:46112\n"
"ds_read_b64_tr_b16 a[38:39], v154 offset:46112\n"
"v_mfma_f32_16x16x32_bf16 v[128:131], a[12:15], v[48:51], 0\n"
"ds_read_b64_tr_b16 a[40:41], v153 offset:20480\n"
"ds_read_b64_tr_b16 a[42:43], v155 offset:20480\n"
"v_mfma_f32_16x16x32_bf16 v[132:135], a[16:19], v[48:51], 0\n"
"ds_read_b64_tr_b16 a[44:45], v153 offset:20488\n"
"ds_read_b64_tr_b16 a[46:47], v155 offset:20488\n"
"s_add_u32 m0, s61, 14336\n"
"s_nop 0\n"
"buffer_load_dwordx4 v172, s[80:83], s59 offen offset:0 lds\n"
"v_mfma_f32_16x16x32_bf16 v[116:119], a[20:23], v[68:71], v[116:119]\n"
"s_waitcnt lgkmcnt(15)\n"
"ds_read_b64_tr_b16 a[48:49], v153 offset:20496\n"
"ds_read_b64_tr_b16 a[50:51], v155 offset:20496\n"
"v_mfma_f32_16x16x32_bf16 v[120:123], a[24:27], v[68:71], v[120:123]\n"
"ds_read_b64_tr_b16 a[52:53], v153 offset:20504\n"
"ds_read_b64_tr_b16 a[54:55], v155 offset:20504\n"
"v_mfma_f32_16x16x32_bf16 v[124:127], a[28:31], v[68:71], v[124:127]\n"
"ds_read_b64_tr_b16 a[56:57], v153 offset:20512\n"
"ds_read_b64_tr_b16 a[58:59], v155 offset:20512\n"
"s_add_u32 s59, s60, 122880\n"
"s_add_u32 m0, s61, 15360\n"
"s_nop 0\n"
"buffer_load_dwordx4 v168, s[80:83], s59 offen offset:0 lds\n"
"s_waitcnt lgkmcnt(15)\n"
"v_mfma_f32_16x16x32_bf16 v[232:235], v[196:199], a[160:163], 0\n"
"v_mfma_f32_16x16x32_bf16 v[236:239], v[196:199], a[164:167], 0\n"
"v_mfma_f32_16x16x32_bf16 v[240:243], v[196:199], a[168:171], 0\n"
"s_waitcnt lgkmcnt(14)\n"
"v_mfma_f32_16x16x32_bf16 v[244:247], v[196:199], a[172:175], 0\n"
"s_waitcnt lgkmcnt(12)\n"
"v_mfma_f32_16x16x32_bf16 v[248:251], v[196:199], a[176:179], 0\n"
"v_mfma_f32_16x16x32_bf16 v[128:131], a[32:35], v[68:71], v[128:131]\n"
"s_waitcnt lgkmcnt(10)\n"
"ds_read_b64_tr_b16 a[60:61], v153 offset:46080\n"
"ds_read_b64_tr_b16 a[62:63], v155 offset:46080\n"
"v_mfma_f32_16x16x32_bf16 v[132:135], a[36:39], v[68:71], v[132:135]\n"
"s_waitcnt lgkmcnt(10)\n"
"ds_read_b64_tr_b16 a[64:65], v153 offset:46088\n"
"ds_read_b64_tr_b16 a[66:67], v155 offset:46088\n"
"v_mfma_f32_16x16x32_bf16 v[116:119], a[40:43], v[88:91], v[116:119]\n"
"s_waitcnt lgkmcnt(10)\n"
"ds_read_b64_tr_b16 a[68:69], v153 offset:46096\n"
"ds_read_b64_tr_b16 a[70:71], v155 offset:46096\n"
"buffer_load_dwordx4 v169, s[80:83], s59 offen offset:1024 lds\n"
"v_mfma_f32_16x16x32_bf16 v[120:123], a[44:47], v[88:91], v[120:123]\n"
"s_waitcnt lgkmcnt(10)\n"
"ds_read_b64_tr_b16 a[72:73], v153 offset:46104\n"
"ds_read_b64_tr_b16 a[74:75], v155 offset:46104\n"
"v_mfma_f32_16x16x32_bf16 v[124:127], a[48:51], v[88:91], v[124:127]\n"
"s_waitcnt lgkmcnt(10)\n"
"ds_read_b64_tr_b16 a[76:77], v153 offset:46112\n"
"ds_read_b64_tr_b16 a[78:79], v155 offset:46112\n"
"s_waitcnt vmcnt(13)\n"
"ds_read_b64_tr_b16 a[160:161], v212 offset:0\n"
"ds_read_b64_tr_b16 a[162:163], v213 offset:0\n"
"ds_read_b64_tr_b16 a[164:165], v212 offset:8\n"
"ds_read_b64_tr_b16 a[166:167], v213 offset:8\n"
"ds_read_b64_tr_b16 a[168:169], v212 offset:16\n"
"ds_read_b64_tr_b16 a[170:171], v213 offset:16\n"
"ds_read_b64_tr_b16 a[172:173], v212 offset:24\n"
"ds_read_b64_tr_b16 a[174:175], v213 offset:24\n"
"ds_read_b64_tr_b16 a[176:177], v212 offset:32\n"
"ds_read_b64_tr_b16 a[178:179], v213 offset:32\n"
"v_mfma_f32_16x16x32_bf16 v[128:131], a[52:55], v[88:91], v[128:131]\n"
"s_waitcnt lgkmcnt(15)\n"
"s_barrier\n"
"ds_read_b64_tr_b16 a[80:81], v152 offset:0\n"
"ds_read_b64_tr_b16 a[82:83], v154 offset:0\n"
"buffer_load_dwordx4 v170, s[80:83], s59 offen offset:2048 lds\n"
"v_mfma_f32_16x16x32_bf16 v[132:135], a[56:59], v[88:91], v[132:135]\n"
"ds_read_b64_tr_b16 a[84:85], v152 offset:8\n"
"ds_read_b64_tr_b16 a[86:87], v154 offset:8\n"
"v_mfma_f32_16x16x32_bf16 v[116:119], a[60:63], v[108:111], v[116:119]\n"
"ds_read_b64_tr_b16 a[88:89], v152 offset:16\n"
"ds_read_b64_tr_b16 a[90:91], v154 offset:16\n"
"v_mfma_f32_16x16x32_bf16 v[120:123], a[64:67], v[108:111], v[120:123]\n"
"s_waitcnt lgkmcnt(15)\n"
"ds_read_b64_tr_b16 a[92:93], v152 offset:24\n"
"ds_read_b64_tr_b16 a[94:95], v154 offset:24\n"
"buffer_load_dwordx4 v171, s[80:83], s59 offen offset:3072 lds\n"
"v_mfma_f32_16x16x32_bf16 v[124:127], a[68:71], v[108:111], v[124:127]\n"
"ds_read_b64_tr_b16 a[96:97], v152 offset:32\n"
"ds_read_b64_tr_b16 a[98:99], v154 offset:32\n"
"v_mfma_f32_16x16x32_bf16 v[128:131], a[72:75], v[108:111], v[128:131]\n"
"ds_read_b64_tr_b16 a[100:101], v152 offset:25600\n"
"ds_read_b64_tr_b16 a[102:103], v154 offset:25600\n"
"s_waitcnt lgkmcnt(15)\n"
"v_mfma_f32_16x16x32_bf16 v[232:235], v[196:199], a[160:163], v[232:235]\n"
"v_mfma_f32_16x16x32_bf16 v[236:239], v[196:199], a[164:167], v[236:239]\n"
"v_mfma_f32_16x16x32_bf16 v[240:243], v[196:199], a[168:171], v[240:243]\n"
"s_waitcnt lgkmcnt(14)\n"
"v_mfma_f32_16x16x32_bf16 v[244:247], v[196:199], a[172:175], v[244:247]\n"
"s_waitcnt lgkmcnt(12)\n"
"v_mfma_f32_16x16x32_bf16 v[248:251], v[196:199], a[176:179], v[248:251]\n"
"v_mfma_f32_16x16x32_bf16 v[132:135], a[76:79], v[108:111], v[132:135]\n"
"s_waitcnt lgkmcnt(10)\n"
"ds_read_b64_tr_b16 a[104:105], v152 offset:25608\n"
"ds_read_b64_tr_b16 a[106:107], v154 offset:25608\n"
"s_add_u32 m0, s61, 19456\n"
"s_nop 0\n"
"buffer_load_dwordx4 v172, s[80:83], s59 offen offset:0 lds\n"
"v_mfma_f32_16x16x32_bf16 v[116:119], a[80:83], v[32:35], v[116:119]\n"
"s_waitcnt lgkmcnt(10)\n"
"ds_read_b64_tr_b16 a[108:109], v152 offset:25616\n"
"ds_read_b64_tr_b16 a[110:111], v154 offset:25616\n"
"v_mfma_f32_16x16x32_bf16 v[120:123], a[84:87], v[32:35], v[120:123]\n"
"s_waitcnt lgkmcnt(10)\n"
"ds_read_b64_tr_b16 a[112:113], v152 offset:25624\n"
"ds_read_b64_tr_b16 a[114:115], v154 offset:25624\n"
"v_mfma_f32_16x16x32_bf16 v[124:127], a[88:91], v[32:35], v[124:127]\n"
"s_waitcnt lgkmcnt(10)\n"
"ds_read_b64_tr_b16 a[116:117], v152 offset:25632\n"
"ds_read_b64_tr_b16 a[118:119], v154 offset:25632\n"
"buffer_load_ushort v210, v211, s[92:95], s75 offen\n"
"v_mfma_f32_16x16x32_bf16 v[128:131], a[92:95], v[32:35], v[128:131]\n"
"s_waitcnt lgkmcnt(10)\n"
"ds_read_b64_tr_b16 a[120:121], v153 offset:0\n"
"ds_read_b64_tr_b16 a[122:123], v155 offset:0\n"
"v_mfma_f32_16x16x32_bf16 v[132:135], a[96:99], v[32:35], v[132:135]\n"
"s_waitcnt lgkmcnt(10)\n"
"ds_read_b64_tr_b16 a[124:125], v153 offset:8\n"
"ds_read_b64_tr_b16 a[126:127], v155 offset:8\n"
"s_waitcnt vmcnt(12)\n"
"ds_read_b64_tr_b16 a[160:161], v212 offset:5120\n"
"ds_read_b64_tr_b16 a[162:163], v213 offset:5120\n"
"ds_read_b64_tr_b16 a[164:165], v212 offset:5128\n"
"ds_read_b64_tr_b16 a[166:167], v213 offset:5128\n"
"ds_read_b64_tr_b16 a[168:169], v212 offset:5136\n"
"ds_read_b64_tr_b16 a[170:171], v213 offset:5136\n"
"ds_read_b64_tr_b16 a[172:173], v212 offset:5144\n"
"ds_read_b64_tr_b16 a[174:175], v213 offset:5144\n"
"ds_read_b64_tr_b16 a[176:177], v212 offset:5152\n"
"ds_read_b64_tr_b16 a[178:179], v213 offset:5152\n"
"v_mfma_f32_16x16x32_bf16 v[116:119], a[100:103], v[52:55], v[116:119]\n"
"s_waitcnt lgkmcnt(15)\n"
"ds_read_b64_tr_b16 a[128:129], v153 offset:16\n"
"ds_read_b64_tr_b16 a[130:131], v155 offset:16\n"
"s_add_u32 m0, s73, 102400\n"
"s_nop 0\n"
"buffer_load_dwordx4 v176, s[88:91], 0 offen lds\n"
"v_mfma_f32_16x16x32_bf16 v[120:123], a[104:107], v[52:55], v[120:123]\n"
"ds_read_b64_tr_b16 a[132:133], v153 offset:24\n"
"ds_read_b64_tr_b16 a[134:135], v155 offset:24\n"
"v_mfma_f32_16x16x32_bf16 v[124:127], a[108:111], v[52:55], v[124:127]\n"
"ds_read_b64_tr_b16 a[136:137], v153 offset:32\n"
"ds_read_b64_tr_b16 a[138:139], v155 offset:32\n"
"v_mfma_f32_16x16x32_bf16 v[128:131], a[112:115], v[52:55], v[128:131]\n"
"s_waitcnt lgkmcnt(15)\n"
"ds_read_b64_tr_b16 a[140:141], v153 offset:25600\n"
"ds_read_b64_tr_b16 a[142:143], v155 offset:25600\n"
"s_add_u32 m0, s73, 103424\n"
"s_nop 0\n"
"buffer_load_dwordx4 v177, s[88:91], 0 offen lds\n"
"v_mfma_f32_16x16x32_bf16 v[132:135], a[116:119], v[52:55], v[132:135]\n"
"ds_read_b64_tr_b16 a[144:145], v153 offset:25608\n"
"ds_read_b64_tr_b16 a[146:147], v155 offset:25608\n"
"v_mfma_f32_16x16x32_bf16 v[116:119], a[120:123], v[72:75], v[116:119]\n"
"ds_read_b64_tr_b16 a[148:149], v153 offset:25616\n"
"ds_read_b64_tr_b16 a[150:151], v155 offset:25616\n"
"s_waitcnt lgkmcnt(15)\n"
"v_mfma_f32_16x16x32_bf16 v[232:235], v[196:199], a[160:163], v[232:235]\n"
"v_mfma_f32_16x16x32_bf16 v[236:239], v[196:199], a[164:167], v[236:239]\n"
"v_mfma_f32_16x16x32_bf16 v[240:243], v[196:199], a[168:171], v[240:243]\n"
"s_waitcnt lgkmcnt(14)\n"
"v_mfma_f32_16x16x32_bf16 v[244:247], v[196:199], a[172:175], v[244:247]\n"
"s_waitcnt lgkmcnt(12)\n"
"v_mfma_f32_16x16x32_bf16 v[248:251], v[196:199], a[176:179], v[248:251]\n"
"v_mfma_f32_16x16x32_bf16 v[120:123], a[124:127], v[72:75], v[120:123]\n"
"s_waitcnt lgkmcnt(10)\n"
"ds_read_b64_tr_b16 a[152:153], v153 offset:25624\n"
"ds_read_b64_tr_b16 a[154:155], v155 offset:25624\n"
"s_add_u32 m0, s73, 104448\n"
"s_nop 0\n"
"buffer_load_dwordx4 v178, s[88:91], 0 offen lds\n"
"v_mfma_f32_16x16x32_bf16 v[124:127], a[128:131], v[72:75], v[124:127]\n"
"s_waitcnt lgkmcnt(10)\n"
"ds_read_b64_tr_b16 a[156:157], v153 offset:25632\n"
"ds_read_b64_tr_b16 a[158:159], v155 offset:25632\n"
"v_mfma_f32_16x16x32_bf16 v[128:131], a[132:135], v[72:75], v[128:131]\n"
"s_waitcnt lgkmcnt(10)\n"
"s_waitcnt vmcnt(13)\n"
"s_barrier\n"
"s_mov_b64 exec, 1\n"
"v_mov_b32 v166, s40\n"
"v_mov_b32 v167, s41\n"
"buffer_store_dwordx2 v[166:167], v2, s[12:15], s53 offen sc1\n"
"s_mov_b64 exec, -1\n"
"ds_read_b64_tr_b16 a[0:1], v152 offset:5120\n"
"ds_read_b64_tr_b16 a[2:3], v154 offset:5120\n"
"v_mfma_f32_16x16x32_bf16 v[132:135], a[136:139], v[72:75], v[132:135]\n"
"s_waitcnt lgkmcnt(10)\n"
"ds_read_b64_tr_b16 a[4:5], v152 offset:5128\n"
"ds_read_b64_tr_b16 a[6:7], v154 offset:5128\n"
"s_add_u32 m0, s73, 105472\n"
"s_nop 0\n"
"buffer_load_dwordx4 v179, s[88:91], 0 offen lds\n"
"v_mfma_f32_16x16x32_bf16 v[116:119], a[140:143], v[92:95], v[116:119]\n"
"s_waitcnt lgkmcnt(10)\n"
"ds_read_b64_tr_b16 a[8:9], v152 offset:5136\n"
"ds_read_b64_tr_b16 a[10:11], v154 offset:5136\n"
"v_mfma_f32_16x16x32_bf16 v[120:123], a[144:147], v[92:95], v[120:123]\n"
"s_waitcnt lgkmcnt(10)\n"
"ds_read_b64_tr_b16 a[12:13], v152 offset:5144\n"
"ds_read_b64_tr_b16 a[14:15], v154 offset:5144\n"
"s_waitcnt vmcnt(11)\n"
"ds_read_b64_tr_b16 a[160:161], v212 offset:10240\n"
"ds_read_b64_tr_b16 a[162:163], v213 offset:10240\n"
"ds_read_b64_tr_b16 a[164:165], v212 offset:10248\n"
"ds_read_b64_tr_b16 a[166:167], v213 offset:10248\n"
"ds_read_b64_tr_b16 a[168:169], v212 offset:10256\n"
"ds_read_b64_tr_b16 a[170:171], v213 offset:10256\n"
"ds_read_b64_tr_b16 a[172:173], v212 offset:10264\n"
"ds_read_b64_tr_b16 a[174:175], v213 offset:10264\n"
"ds_read_b64_tr_b16 a[176:177], v212 offset:10272\n"
"ds_read_b64_tr_b16 a[178:179], v213 offset:10272\n"
"v_mfma_f32_16x16x32_bf16 v[124:127], a[148:151], v[92:95], v[124:127]\n"
"s_waitcnt lgkmcnt(15)\n"
"ds_read_b64_tr_b16 a[16:17], v152 offset:5152\n"
"ds_read_b64_tr_b16 a[18:19], v154 offset:5152\n"
"s_add_u32 m0, s73, 106496\n"
"s_nop 0\n"
"buffer_load_dwordx4 v180, s[88:91], 0 offen lds\n"
"v_mfma_f32_16x16x32_bf16 v[128:131], a[152:155], v[92:95], v[128:131]\n"
"Lpad2_%=:\n"
"s_cbranch_execz Lpadback0_%=\n"
"ds_read_b64_tr_b16 a[20:21], v152 offset:30720\n"
"ds_read_b64_tr_b16 a[22:23], v154 offset:30720\n"
"v_mfma_f32_16x16x32_bf16 v[132:135], a[156:159], v[92:95], v[132:135]\n"
"ds_read_b64_tr_b16 a[24:25], v152 offset:30728\n"
"ds_read_b64_tr_b16 a[26:27], v154 offset:30728\n"
"v_mfma_f32_16x16x32_bf16 v[116:119], a[0:3], v[36:39], v[116:119]\n"
"s_waitcnt lgkmcnt(15)\n"
"ds_read_b64_tr_b16 a[28:29], v152 offset:30736\n"
"ds_read_b64_tr_b16 a[30:31], v154 offset:30736\n"
"v_mfma_f32_16x16x32_bf16 v[120:123], a[4:7], v[36:39], v[120:123]\n"
"ds_read_b64_tr_b16 a[32:33], v152 offset:30744\n"
"ds_read_b64_tr_b16 a[34:35], v154 offset:30744\n"
"v_mfma_f32_16x16x32_bf16 v[124:127], a[8:11], v[36:39], v[124:127]\n"
"ds_read_b64_tr_b16 a[36:37], v152 offset:30752\n"
"ds_read_b64_tr_b16 a[38:39], v154 offset:30752\n"
"s_waitcnt lgkmcnt(15)\n"
"v_mfma_f32_16x16x32_bf16 v[232:235], v[196:199], a[160:163], v[232:235]\n"
"v_mfma_f32_16x16x32_bf16 v[236:239], v[196:199], a[164:167], v[236:239]\n"
"v_mfma_f32_16x16x32_bf16 v[240:243], v[196:199], a[168:171], v[240:243]\n"
"s_waitcnt lgkmcnt(14)\n"
"v_mfma_f32_16x16x32_bf16 v[244:247], v[196:199], a[172:175], v[244:247]\n"
"s_waitcnt lgkmcnt(12)\n"
"v_mfma_f32_16x16x32_bf16 v[248:251], v[196:199], a[176:179], v[248:251]\n"
"v_mfma_f32_16x16x32_bf16 v[128:131], a[12:15], v[36:39], v[128:131]\n"
"s_waitcnt lgkmcnt(10)\n"
"ds_read_b64_tr_b16 a[40:41], v153 offset:5120\n"
"ds_read_b64_tr_b16 a[42:43], v155 offset:5120\n"
"v_mfma_f32_16x16x32_bf16 v[132:135], a[16:19], v[36:39], v[132:135]\n"
"s_waitcnt lgkmcnt(10)\n"
"ds_read_b64_tr_b16 a[44:45], v153 offset:5128\n"
"ds_read_b64_tr_b16 a[46:47], v155 offset:5128\n"
"v_mfma_f32_16x16x32_bf16 v[116:119], a[20:23], v[56:59], v[116:119]\n"
"s_waitcnt lgkmcnt(10)\n"
"ds_read_b64_tr_b16 a[48:49], v153 offset:5136\n"
"ds_read_b64_tr_b16 a[50:51], v155 offset:5136\n"
"v_mfma_f32_16x16x32_bf16 v[120:123], a[24:27], v[56:59], v[120:123]\n"
"s_waitcnt lgkmcnt(10)\n"
"ds_read_b64_tr_b16 a[52:53], v153 offset:5144\n"
"ds_read_b64_tr_b16 a[54:55], v155 offset:5144\n"
"v_mfma_f32_16x16x32_bf16 v[124:127], a[28:31], v[56:59], v[124:127]\n"
"s_waitcnt lgkmcnt(10)\n"
"ds_read_b64_tr_b16 a[56:57], v153 offset:5152\n"
"ds_read_b64_tr_b16 a[58:59], v155 offset:5152\n"
"v_mfma_f32_16x16x32_bf16 v[128:131], a[32:35], v[56:59], v[128:131]\n"
"s_waitcnt lgkmcnt(10)\n"
"ds_read_b64_tr_b16 a[60:61], v153 offset:30720\n"
"ds_read_b64_tr_b16 a[62:63], v155 offset:30720\n"
"v_mfma_f32_16x16x32_bf16 v[132:135], a[36:39], v[56:59], v[132:135]\n"
"s_waitcnt lgkmcnt(10)\n"
"ds_read_b64_tr_b16 a[64:65], v153 offset:30728\n"
"ds_read_b64_tr_b16 a[66:67], v155 offset:30728\n"
"v_mfma_f32_16x16x32_bf16 v[116:119], a[40:43], v[76:79], v[116:119]\n"
"s_waitcnt lgkmcnt(10)\n"
"ds_read_b64_tr_b16 a[68:69], v153 offset:30736\n"
"ds_read_b64_tr_b16 a[70:71], v155 offset:30736\n"
"s_waitcnt vmcnt(7)\n"
"ds_read_b64_tr_b16 a[160:161], v212 offset:15360\n"
"ds_read_b64_tr_b16 a[162:163], v213 offset:15360\n"
"ds_read_b64_tr_b16 a[164:165], v212 offset:15368\n"
"ds_read_b64_tr_b16 a[166:167], v213 offset:15368\n"
"ds_read_b64_tr_b16 a[168:169], v212 offset:15376\n"
"ds_read_b64_tr_b16 a[170:171], v213 offset:15376\n"
"ds_read_b64_tr_b16 a[172:173], v212 offset:15384\n"
"ds_read_b64_tr_b16 a[174:175], v213 offset:15384\n"
"ds_read_b64_tr_b16 a[176:177], v212 offset:15392\n"
"ds_read_b64_tr_b16 a[178:179], v213 offset:15392\n"
"v_mfma_f32_16x16x32_bf16 v[120:123], a[44:47], v[76:79], v[120:123]\n"
"s_waitcnt lgkmcnt(15)\n"
"ds_read_b64_tr_b16 a[72:73], v153 offset:30744\n"
"ds_read_b64_tr_b16 a[74:75], v155 offset:30744\n"
"v_mfma_f32_16x16x32_bf16 v[124:127], a[48:51], v[76:79], v[124:127]\n"
"ds_read_b64_tr_b16 a[76:77], v153 offset:30752\n"
"ds_read_b64_tr_b16 a[78:79], v155 offset:30752\n"
"v_mfma_f32_16x16x32_bf16 v[128:131], a[52:55], v[76:79], v[128:131]\n"
"s_barrier\n"
"ds_read_b64_tr_b16 a[80:81], v152 offset:10240\n"
"ds_read_b64_tr_b16 a[82:83], v154 offset:10240\n"
"v_mfma_f32_16x16x32_bf16 v[132:135], a[56:59], v[76:79], v[132:135]\n"
"s_waitcnt lgkmcnt(15)\n"
"ds_read_b64_tr_b16 a[84:85], v152 offset:10248\n"
"ds_read_b64_tr_b16 a[86:87], v154 offset:10248\n"
"v_mfma_f32_16x16x32_bf16 v[116:119], a[60:63], v[96:99], v[116:119]\n"
"ds_read_b64_tr_b16 a[88:89], v152 offset:10256\n"
"ds_read_b64_tr_b16 a[90:91], v154 offset:10256\n"
"v_mfma_f32_16x16x32_bf16 v[120:123], a[64:67], v[96:99], v[120:123]\n"
"ds_read_b64_tr_b16 a[92:93], v152 offset:10264\n"
"ds_read_b64_tr_b16 a[94:95], v154 offset:10264\n"
"s_waitcnt lgkmcnt(15)\n"
"v_mfma_f32_16x16x32_bf16 v[232:235], v[196:199], a[160:163], v[232:235]\n"
"v_mfma_f32_16x16x32_bf16 v[236:239], v[196:199], a[164:167], v[236:239]\n"
"v_mfma_f32_16x16x32_bf16 v[240:243], v[196:199], a[168:171], v[240:243]\n"
"s_waitcnt lgkmcnt(14)\n"
"v_mfma_f32_16x16x32_bf16 v[244:247], v[196:199], a[172:175], v[244:247]\n"
"s_waitcnt lgkmcnt(12)\n"
"v_mfma_f32_16x16x32_bf16 v[248:251], v[196:199], a[176:179], v[248:251]\n"
"v_mfma_f32_16x16x32_bf16 v[124:127], a[68:71], v[96:99], v[124:127]\n"
"s_waitcnt lgkmcnt(10)\n"
"ds_read_b64_tr_b16 a[96:97], v152 offset:10272\n"
"ds_read_b64_tr_b16 a[98:99], v154 offset:10272\n"
"v_mfma_f32_16x16x32_bf16 v[128:131], a[72:75], v[96:99], v[128:131]\n"
"s_waitcnt lgkmcnt(10)\n"
"ds_read_b64_tr_b16 a[100:101], v152 offset:35840\n"
"ds_read_b64_tr_b16 a[102:103], v154 offset:35840\n"
"v_mfma_f32_16x16x32_bf16 v[132:135], a[76:79], v[96:99], v[132:135]\n"
"s_waitcnt lgkmcnt(10)\n"
"ds_read_b64_tr_b16 a[104:105], v152 offset:35848\n"
"ds_read_b64_tr_b16 a[106:107], v154 offset:35848\n"
"v_mfma_f32_16x16x32_bf16 v[116:119], a[80:83], v[40:43], v[116:119]\n"
"s_waitcnt lgkmcnt(10)\n"
"ds_read_b64_tr_b16 a[108:109], v152 offset:35856\n"
"ds_read_b64_tr_b16 a[110:111], v154 offset:35856\n"
"v_and_b32 v233, 0xffff0000, v234\n"
"v_cvt_pk_bf16_f32 v232, v233, v233\n"
"v_sub_f32 v233, v234, v233\n"
"v_cvt_pk_bf16_f32 v233, v233, v2\n"
"v_mov_b64 v[234:235], 0\n"
"v_mfma_f32_16x16x32_bf16 v[120:123], a[84:87], v[40:43], v[120:123]\n"
"s_waitcnt lgkmcnt(10)\n"
"ds_read_b64_tr_b16 a[112:113], v152 offset:35864\n"
"ds_read_b64_tr_b16 a[114:115], v154 offset:35864\n"
"v_and_b32 v237, 0xffff0000, v238\n"
"v_cvt_pk_bf16_f32 v236, v237, v237\n"
"v_sub_f32 v237, v238, v237\n"
"v_cvt_pk_bf16_f32 v237, v237, v2\n"
"v_mov_b64 v[238:239], 0\n"
"v_mfma_f32_16x16x32_bf16 v[124:127], a[88:91], v[40:43], v[124:127]\n"
"s_waitcnt lgkmcnt(10)\n"
"ds_read_b64_tr_b16 a[116:117], v152 offset:35872\n"
"ds_read_b64_tr_b16 a[118:119], v154 offset:35872\n"
"v_and_b32 v241, 0xffff0000, v242\n"
"v_cvt_pk_bf16_f32 v240, v241, v241\n"
"v_sub_f32 v241, v242, v241\n"
"v_cvt_pk_bf16_f32 v241, v241, v2\n"
"v_mov_b64 v[242:243], 0\n"
"v_mfma_f32_16x16x32_bf16 v[128:131], a[92:95], v[40:43], v[128:131]\n"
"s_waitcnt lgkmcnt(10)\n"
"ds_write_b64 v163, v[232:233] offset:0\n"
"ds_read_b64_tr_b16 a[120:121], v153 offset:10240\n"
"ds_read_b64_tr_b16 a[122:123], v155 offset:10240\n"
"v_and_b32 v245, 0xffff0000, v246\n"
"v_cvt_pk_bf16_f32 v244, v245, v245\n"
"v_sub_f32 v245, v246, v245\n"
"v_cvt_pk_bf16_f32 v245, v245, v2\n"
"v_mov_b64 v[246:247], 0\n"
"v_mfma_f32_16x16x32_bf16 v[132:135], a[96:99], v[40:43], v[132:135]\n"
"s_waitcnt lgkmcnt(11)\n"
"ds_write_b64 v163, v[236:237] offset:1024\n"
"ds_read_b64_tr_b16 a[124:125], v153 offset:10248\n"
"ds_read_b64_tr_b16 a[126:127], v155 offset:10248\n"
"v_and_b32 v249, 0xffff0000, v250\n"
"v_cvt_pk_bf16_f32 v248, v249, v249\n"
"v_sub_f32 v249, v250, v249\n"
"v_cvt_pk_bf16_f32 v249, v249, v2\n"
"v_mov_b64 v[250:251], 0\n"
"v_mfma_f32_16x16x32_bf16 v[116:119], a[100:103], v[60:63], v[116:119]\n"
"s_waitcnt lgkmcnt(12)\n"
"ds_write_b64 v163, v[240:241] offset:2048\n"
"ds_read_b64_tr_b16 a[128:129], v153 offset:10256\n"
"ds_read_b64_tr_b16 a[130:131], v155 offset:10256\n"
"v_mfma_f32_16x16x32_bf16 v[120:123], a[104:107], v[60:63], v[120:123]\n"
"s_waitcnt lgkmcnt(13)\n"
"ds_write_b64 v163, v[244:245] offset:3072\n"
"ds_read_b64_tr_b16 a[132:133], v153 offset:10264\n"
"ds_read_b64_tr_b16 a[134:135], v155 offset:10264\n"
"v_mfma_f32_16x16x32_bf16 v[124:127], a[108:111], v[60:63], v[124:127]\n"
"s_waitcnt lgkmcnt(14)\n"
"ds_write_b64 v163, v[248:249] offset:4096\n"
"ds_read_b64_tr_b16 a[136:137], v153 offset:10272\n"
"ds_read_b64_tr_b16 a[138:139], v155 offset:10272\n"
"v_mfma_f32_16x16x32_bf16 v[128:131], a[112:115], v[60:63], v[128:131]\n"
"s_waitcnt lgkmcnt(15)\n"
"ds_read_b64_tr_b16 a[140:141], v153 offset:35840\n"
"ds_read_b64_tr_b16 a[142:143], v155 offset:35840\n"
"v_mfma_f32_16x16x32_bf16 v[132:135], a[116:119], v[60:63], v[132:135]\n"
"s_waitcnt lgkmcnt(14)\n"
"ds_read_b64_tr_b16 a[144:145], v153 offset:35848\n"
"ds_read_b64_tr_b16 a[146:147], v155 offset:35848\n"
"v_mfma_f32_16x16x32_bf16 v[116:119], a[120:123], v[80:83], v[116:119]\n"
"s_waitcnt lgkmcnt(13)\n"
"ds_read_b64_tr_b16 a[148:149], v153 offset:35856\n"
"ds_read_b64_tr_b16 a[150:151], v155 offset:35856\n"
"v_mfma_f32_16x16x32_bf16 v[120:123], a[124:127], v[80:83], v[120:123]\n"
"s_waitcnt lgkmcnt(12)\n"
"ds_read_b64_tr_b16 a[152:153], v153 offset:35864\n"
"ds_read_b64_tr_b16 a[154:155], v155 offset:35864\n"
"v_mfma_f32_16x16x32_bf16 v[124:127], a[128:131], v[80:83], v[124:127]\n"
"s_waitcnt lgkmcnt(11)\n"
"ds_read_b64_tr_b16 a[156:157], v153 offset:35872\n"
"ds_read_b64_tr_b16 a[158:159], v155 offset:35872\n"
"v_mfma_f32_16x16x32_bf16 v[128:131], a[132:135], v[80:83], v[128:131]\n"
"s_waitcnt lgkmcnt(10)\n"
"s_barrier\n"
"ds_read_b64_tr_b16 a[0:1], v152 offset:15360\n"
"ds_read_b64_tr_b16 a[2:3], v154 offset:15360\n"
"v_mfma_f32_16x16x32_bf16 v[132:135], a[136:139], v[80:83], v[132:135]\n"
"s_waitcnt lgkmcnt(10)\n"
"ds_read_b64_tr_b16 a[4:5], v152 offset:15368\n"
"ds_read_b64_tr_b16 a[6:7], v154 offset:15368\n"
"v_mfma_f32_16x16x32_bf16 v[116:119], a[140:143], v[100:103], v[116:119]\n"
"s_waitcnt lgkmcnt(10)\n"
"ds_read_b64_tr_b16 a[8:9], v152 offset:15376\n"
"ds_read_b64_tr_b16 a[10:11], v154 offset:15376\n"
"v_mfma_f32_16x16x32_bf16 v[120:123], a[144:147], v[100:103], v[120:123]\n"
"s_waitcnt lgkmcnt(10)\n"
"ds_read_b64_tr_b16 a[12:13], v152 offset:15384\n"
"ds_read_b64_tr_b16 a[14:15], v154 offset:15384\n"
"s_mov_b64 exec, 0xff\n"
"buffer_load_dwordx2 v[166:167], v175, s[12:15], s62 offen sc1\n"
"s_mov_b64 exec, -1\n"
"v_mfma_f32_16x16x32_bf16 v[124:127], a[148:151], v[100:103], v[124:127]\n"
"s_waitcnt lgkmcnt(10)\n"
"ds_read_b64_tr_b16 a[16:17], v152 offset:15392\n"
"ds_read_b64_tr_b16 a[18:19], v154 offset:15392\n"
"v_mfma_f32_16x16x32_bf16 v[128:131], a[152:155], v[100:103], v[128:131]\n"
"s_waitcnt lgkmcnt(10)\n"
"ds_read_b64_tr_b16 a[20:21], v152 offset:40960\n"
"ds_read_b64_tr_b16 a[22:23], v154 offset:40960\n"
"v_mfma_f32_16x16x32_bf16 v[132:135], a[156:159], v[100:103], v[132:135]\n"
"s_waitcnt lgkmcnt(10)\n"
"ds_read_b64_tr_b16 a[24:25], v152 offset:40968\n"
"ds_read_b64_tr_b16 a[26:27], v154 offset:40968\n"
"v_mfma_f32_16x16x32_bf16 v[116:119], a[0:3], v[44:47], v[116:119]\n"
"s_waitcnt lgkmcnt(10)\n"
"ds_read_b64_tr_b16 a[28:29], v152 offset:40976\n"
"ds_read_b64_tr_b16 a[30:31], v154 offset:40976\n"
"v_mfma_f32_16x16x32_bf16 v[120:123], a[4:7], v[44:47], v[120:123]\n"
"s_waitcnt lgkmcnt(10)\n"
"ds_read_b64_tr_b16 a[32:33], v152 offset:40984\n"
"ds_read_b64_tr_b16 a[34:35], v154 offset:40984\n"
"v_mfma_f32_16x16x32_bf16 v[124:127], a[8:11], v[44:47], v[124:127]\n"
"s_waitcnt lgkmcnt(10)\n"
"ds_read_b64_tr_b16 a[36:37], v152 offset:40992\n"
"ds_read_b64_tr_b16 a[38:39], v154 offset:40992\n"
"v_mfma_f32_16x16x32_bf16 v[128:131], a[12:15], v[44:47], v[128:131]\n"
"s_waitcnt lgkmcnt(10)\n"
"ds_read_b64_tr_b16 a[40:41], v153 offset:15360\n"
"ds_read_b64_tr_b16 a[42:43], v155 offset:15360\n"
"v_mfma_f32_16x16x32_bf16 v[132:135], a[16:19], v[44:47], v[132:135]\n"
"s_waitcnt lgkmcnt(10)\n"
"ds_read_b64_tr_b16 a[44:45], v153 offset:15368\n"
"ds_read_b64_tr_b16 a[46:47], v155 offset:15368\n"
"v_mfma_f32_16x16x32_bf16 v[116:119], a[20:23], v[64:67], v[116:119]\n"
"s_waitcnt lgkmcnt(10)\n"
"ds_read_b64_tr_b16 a[48:49], v153 offset:15376\n"
"ds_read_b64_tr_b16 a[50:51], v155 offset:15376\n"
"v_mfma_f32_16x16x32_bf16 v[120:123], a[24:27], v[64:67], v[120:123]\n"
"s_waitcnt lgkmcnt(10)\n"
"ds_read_b64_tr_b16 a[52:53], v153 offset:15384\n"
"ds_read_b64_tr_b16 a[54:55], v155 offset:15384\n"
"v_mfma_f32_16x16x32_bf16 v[124:127], a[28:31], v[64:67], v[124:127]\n"
"s_waitcnt lgkmcnt(10)\n"
"ds_read_b64_tr_b16 a[56:57], v153 offset:15392\n"
"ds_read_b64_tr_b16 a[58:59], v155 offset:15392\n"
"v_mfma_f32_16x16x32_bf16 v[128:131], a[32:35], v[64:67], v[128:131]\n"
"s_waitcnt lgkmcnt(10)\n"
"ds_read_b64_tr_b16 a[60:61], v153 offset:40960\n"
"ds_read_b64_tr_b16 a[62:63], v155 offset:40960\n"
"v_mfma_f32_16x16x32_bf16 v[132:135], a[36:39], v[64:67], v[132:135]\n"
"s_waitcnt lgkmcnt(10)\n"
"ds_read_b64_tr_b16 a[64:65], v153 offset:40968\n"
"ds_read_b64_tr_b16 a[66:67], v155 offset:40968\n"
"v_mfma_f32_16x16x32_bf16 v[116:119], a[40:43], v[84:87], v[116:119]\n"
"s_waitcnt lgkmcnt(10)\n"
"ds_read_b64_tr_b16 a[68:69], v153 offset:40976\n"
"ds_read_b64_tr_b16 a[70:71], v155 offset:40976\n"
"v_mfma_f32_16x16x32_bf16 v[120:123], a[44:47], v[84:87], v[120:123]\n"
"s_waitcnt lgkmcnt(10)\n"
"ds_read_b64_tr_b16 a[72:73], v153 offset:40984\n"
"ds_read_b64_tr_b16 a[74:75], v155 offset:40984\n"
"v_mfma_f32_16x16x32_bf16 v[124:127], a[48:51], v[84:87], v[124:127]\n"
"s_waitcnt lgkmcnt(10)\n"
"ds_read_b64_tr_b16 a[76:77], v153 offset:40992\n"
"ds_read_b64_tr_b16 a[78:79], v155 offset:40992\n"
"v_mfma_f32_16x16x32_bf16 v[128:131], a[52:55], v[84:87], v[128:131]\n"
"s_waitcnt lgkmcnt(10)\n"
"v_mfma_f32_16x16x32_bf16 v[132:135], a[56:59], v[84:87], v[132:135]\n"
"s_waitcnt lgkmcnt(8)\n"
"ds_read_b64 v[232:233], v160 offset:0\n"
"ds_read_b64_tr_b16 v[216:217], v158\n"
"ds_read_b64_tr_b16 v[220:221], v158 offset:32\n"
"v_mfma_f32_16x16x32_bf16 v[116:119], a[60:63], v[104:107], v[116:119]\n"
"s_waitcnt lgkmcnt(9)\n"
"ds_read_b64 v[236:237], v160 offset:1024\n"
"ds_read_b64_tr_b16 v[218:219], v159\n"
"ds_read_b64_tr_b16 v[222:223], v159 offset:32\n"
"v_mfma_f32_16x16x32_bf16 v[120:123], a[64:67], v[104:107], v[120:123]\n"
"s_waitcnt lgkmcnt(10)\n"
"ds_read_b64 v[240:241], v160 offset:2048\n"
"s_mov_b64 exec, 0xff\n"
"s_waitcnt vmcnt(0)\n"
"v_cmp_ne_u64 s[8:9], s[40:41], v[166:167]\n"
"s_mov_b64 exec, -1\n"
"s_cmp_lg_u32 s8, 0\n"
"s_cbranch_scc1 Lkp12_%=\n"
"Lkd13_%=:\n"
"v_mfma_f32_16x16x32_bf16 v[124:127], a[68:71], v[104:107], v[124:127]\n"
"s_waitcnt lgkmcnt(9)\n"
"ds_read_b64 v[244:245], v160 offset:3072\n"
"s_add_u32 m0, s78, 143168\n"
"s_nop 0\n"
"global_load_lds_dwordx4 v1, s[68:69] offset:0 sc1\n"
"v_mfma_f32_16x16x32_bf16 v[128:131], a[72:75], v[104:107], v[128:131]\n"
"s_waitcnt lgkmcnt(8)\n"
"ds_read_b64 v[248:249], v160 offset:4096\n"
"global_load_lds_dwordx4 v1, s[68:69] offset:1024 sc1\n"
"v_mfma_f32_16x16x32_bf16 v[132:135], a[76:79], v[104:107], v[132:135]\n"
"global_load_lds_dwordx4 v1, s[68:69] offset:2048 sc1\n"
"global_load_lds_dwordx4 v1, s[68:69] offset:3072 sc1\n"
"s_add_u32 m0, s78, 147264\n"
"s_add_u32 s68, s68, 4096\n"
"s_addc_u32 s69, s69, 0\n"
"global_load_lds_dwordx4 v1, s[68:69] offset:0 sc1\n"
"s_waitcnt lgkmcnt(8)\n"
"v_mfma_f32_16x16x32_bf16 v[116:119], v[232:235], v[112:115], v[116:119]\n"
"s_waitcnt lgkmcnt(5)\n"
"v_mfma_f32_16x16x32_bf16 v[120:123], v[236:239], v[112:115], v[120:123]\n"
"s_waitcnt lgkmcnt(2)\n"
"v_mfma_f32_16x16x32_bf16 v[124:127], v[240:243], v[112:115], v[124:127]\n"
"s_waitcnt lgkmcnt(1)\n"
"v_mfma_f32_16x16x32_bf16 v[128:131], v[244:247], v[112:115], v[128:131]\n"
"s_waitcnt lgkmcnt(0)\n"
"v_mfma_f32_16x16x32_bf16 v[132:135], v[248:251], v[112:115], v[132:135]\n"
"s_mov_b32 s16, s42\n"
"s_mov_b32 s17, s43\n"
"s_mov_b32 s92, s66\n"
"s_and_b32 s93, s67, 0xffff\n"
"s_mov_b32 s94, 1280\n"
"s_mov_b64 s[80:81], s[12:13]\n"
"s_mov_b32 s82, s14\n"
"v_mov_b32 v205, v157\n"
"v_pk_mul_f32 v[116:117], v[116:117], v[142:143] op_sel_hi:[1,0]\n"
"v_pk_mul_f32 v[118:119], v[118:119], v[142:143] op_sel_hi:[1,0]\n"
"v_pk_mul_f32 v[120:121], v[120:121], v[142:143] op_sel_hi:[1,0]\n"
"v_pk_mul_f32 v[122:123], v[122:123], v[142:143] op_sel_hi:[1,0]\n"
"v_pk_mul_f32 v[124:125], v[124:125], v[142:143] op_sel_hi:[1,0]\n"
"v_pk_mul_f32 v[126:127], v[126:127], v[142:143] op_sel_hi:[1,0]\n"
"v_pk_mul_f32 v[128:129], v[128:129], v[142:143] op_sel_hi:[1,0]\n"
"v_pk_mul_f32 v[130:131], v[130:131], v[142:143] op_sel_hi:[1,0]\n"
"v_pk_mul_f32 v[132:133], v[132:133], v[142:143] op_sel_hi:[1,0]\n"
"v_pk_mul_f32 v[134:135], v[134:135], v[142:143] op_sel_hi:[1,0]\n"
"v_cvt_pk_bf16_f32 v32, v116, v117\n"
"v_cvt_pk_bf16_f32 v33, v118, v119\n"
"v_cvt_pk_bf16_f32 v34, v120, v121\n"
"v_cvt_pk_bf16_f32 v35, v122, v123\n"
"v_cvt_pk_bf16_f32 v36, v124, v125\n"
"v_cvt_pk_bf16_f32 v37, v126, v127\n"
"v_cvt_pk_bf16_f32 v38, v128, v129\n"
"v_cvt_pk_bf16_f32 v39, v130, v131\n"
"v_cvt_pk_bf16_f32 v40, v132, v133\n"
"v_cvt_pk_bf16_f32 v41, v134, v135\n"
"v_accvgpr_read_b32 v60, a200\n"
"v_accvgpr_read_b32 v61, a201\n"
"v_accvgpr_read_b32 v63, a202\n"
"v_accvgpr_read_b32 v64, a203\n"
"v_accvgpr_read_b32 v65, a204\n"
"v_accvgpr_read_b32 v66, a205\n"
"v_accvgpr_read_b32 v160, a206\n"
"v_accvgpr_read_b32 v161, a207\n"
"v_accvgpr_read_b32 v162, a208\n"
"v_accvgpr_read_b32 v163, a209\n"
"v_accvgpr_read_b32 v164, a210\n"
"v_mov_b32 v62, 0xdddddddd\n"
"v_mov_b64 v[116:117], 0\n"
"v_mov_b64 v[118:119], 0\n"
"v_mov_b64 v[120:121], 0\n"
"v_mov_b64 v[122:123], 0\n"
"v_mov_b64 v[124:125], 0\n"
"v_mov_b64 v[126:127], 0\n"
"v_mov_b64 v[128:129], 0\n"
"v_mov_b64 v[130:131], 0\n"
"v_mov_b64 v[132:133], 0\n"
"v_mov_b64 v[134:135], 0\n"
"v_mov_b64 v[136:137], 0\n"
"v_mov_b64 v[138:139], 0\n"
"v_mov_b64 v[140:141], 0\n"
"v_mov_b64 v[142:143], 0\n"
"v_mov_b64 v[144:145], 0\n"
"v_mov_b64 v[146:147], 0\n"
"v_mov_b64 v[42:43], 0\n"
"v_permlane32_swap_b32 v32, v36\n"
"v_permlane32_swap_b32 v33, v37\n"
"v_permlane32_swap_b32 v34, v38\n"
"v_permlane32_swap_b32 v35, v39\n"
"v_permlane32_swap_b32 v40, v42\n"
"v_permlane32_swap_b32 v41, v43\n"
"s_waitcnt vmcnt(0)\n"
"Lhead1_%=:\n"
"s_waitcnt lgkmcnt(0)\n"
"s_barrier\n"
"ds_read_b128 v[44:47], v60 offset:20672\n"
"ds_read_b128 v[48:51], v61 offset:20672\n"
"ds_read_b128 v[52:55], v63 offset:20672\n"
"ds_read_b128 v[56:59], v64 offset:20672\n"
"ds_write_b128 v164, v[160:163]\n"
"buffer_load_dwordx2 v[172:173], v175, s[12:15], s62 offen sc1\n"
"s_waitcnt lgkmcnt(3)\n"
"v_mfma_f32_32x32x8_bf16 v[84:99], v[48:49], v[40:41], 0\n"
"v_mfma_f32_32x32x16_bf16 v[84:99], v[44:47], v[32:35], v[84:99]\n"
"s_cmp_lg_u32 s23, 0\n"
"s_cbranch_scc1 Lrr14_%=\n"
"s_cmp_lt_u32 s7, 108\n"
"s_cbranch_scc1 LtB15_%=\n"
"ds_read_b64_tr_b16 v[176:177], v205 offset:0\n"
"ds_read_b64_tr_b16 v[178:179], v205 offset:2560\n"
"ds_read_b64_tr_b16 v[180:181], v205 offset:8\n"
"ds_read_b64_tr_b16 v[182:183], v205 offset:2568\n"
"ds_read_b64_tr_b16 v[184:185], v205 offset:64\n"
"ds_read_b64_tr_b16 v[186:187], v205 offset:2624\n"
"ds_read_b64_tr_b16 v[188:189], v205 offset:72\n"
"ds_read_b64_tr_b16 v[190:191], v205 offset:2632\n"
"ds_read_b64_tr_b16 v[192:193], v174 offset:128\n"
"ds_read_b64_tr_b16 v[194:195], v174 offset:2688\n"
"s_branch LtE16_%=\n"
"s_waitcnt lgkmcnt(0)\n"
"LtB15_%=:\n"
"ds_read_b64_tr_b16 v[176:177], v205 offset:0\n"
"ds_read_b64_tr_b16 v[178:179], v205 offset:512\n"
"ds_read_b64_tr_b16 v[180:181], v205 offset:4096\n"
"ds_read_b64_tr_b16 v[182:183], v205 offset:4608\n"
"ds_read_b64_tr_b16 v[184:185], v205 offset:8192\n"
"ds_read_b64_tr_b16 v[186:187], v205 offset:8704\n"
"ds_read_b64_tr_b16 v[188:189], v205 offset:12288\n"
"ds_read_b64_tr_b16 v[190:191], v205 offset:12800\n"
"ds_read_b64_tr_b16 v[192:193], v205 offset:16384\n"
"ds_read_b64_tr_b16 v[194:195], v205 offset:16896\n"
"LtE16_%=:\n"
"s_cmp_lg_u32 s24, 80\n"
"s_cbranch_scc1 LtA17_%=\n"
"s_mul_i32 s47, s21, 129024\n"
"s_add_u32 s48, s47, 7018496\n"
"buffer_store_dwordx4 v[232:235], v1, s[12:15], s48 offen sc1\n"
"s_add_u32 s48, s47, 7019520\n"
"buffer_store_dwordx4 v[236:239], v1, s[12:15], s48 offen sc1\n"
"s_add_u32 s48, s47, 7020544\n"
"buffer_store_dwordx4 v[240:243], v1, s[12:15], s48 offen sc1\n"
"s_add_u32 s48, s47, 7021568\n"
"buffer_store_dwordx4 v[244:247], v1, s[12:15], s48 offen sc1\n"
"s_add_u32 s48, s47, 7022592\n"
"buffer_store_dwordx4 v[248:251], v1, s[12:15], s48 offen sc1\n"
"s_add_u32 s48, s47, 6976512\n"
"buffer_store_dwordx4 v[252:255], v1, s[12:15], s48 offen sc1\n"
"LtA17_%=:\n"
"Lrr14_%=:\n"
"v_mfma_f32_32x32x8_bf16 v[100:115], v[48:49], v[42:43], 0\n"
"v_mfma_f32_32x32x16_bf16 v[100:115], v[44:47], v[36:39], v[100:115]\n"
"s_cmp_lt_u32 s21, 6\n"
"s_cselect_b32 s46, 14, 12\n"
"s_cselect_b32 s47, 0, 12\n"
"s_mul_i32 s46, s46, s21\n"
"s_add_u32 s46, s46, s47\n"
"s_mul_i32 s47, s22, 3\n"
"s_add_u32 s47, s47, s46\n"
"s_mul_i32 s47, s47, 21504\n"
"s_add_u32 s64, s47, 2364416\n"
"s_add_u32 s64, s4, s64\n"
"s_addc_u32 s65, s5, 0\n"
"s_lshr_b32 s47, s22, 1\n"
"s_add_u32 s47, s47, s46\n"
"s_mul_i32 s47, s47, 21504\n"
"s_add_u32 s47, s47, 2618368\n"
"s_cmp_lt_u32 s21, 6\n"
"s_cselect_b32 s47, s47, 2147418112\n"
"s_mov_b32 s70, s47\n"
"s_mul_i32 s79, s22, 25600\n"
"s_mul_i32 s47, s21, 102400\n"
"s_add_u32 s47, s47, s79\n"
"s_add_u32 s66, s47, 1541120\n"
"s_add_u32 s66, s4, s66\n"
"s_addc_u32 s67, s5, 0\n"
"v_max3_f32 v156, v84, v85, v86\n"
"v_max_f32 v156, v156, v87\n"
"v_max3_f32 v156, v156, v88, v89\n"
"v_max3_f32 v156, v156, v90, v91\n"
"v_max3_f32 v156, v156, v92, v93\n"
"v_max3_f32 v156, v156, v94, v95\n"
"v_max3_f32 v156, v156, v96, v97\n"
"v_max3_f32 v156, v156, v98, v99\n"
"v_mov_b32 v158, v156\n"
"s_nop 1\n"
"v_permlane32_swap_b32 v156, v158\n"
"v_max_f32 v156, v156, v158\n"
"v_and_b32 v158, 0xffff0000, v156\n"
"v_sub_f32 v156, v156, v158\n"
"v_and_b32 v159, 0xffff0000, v156\n"
"v_sub_f32 v156, v156, v159\n"
"s_mov_b32 exec_lo, 0\n"
"v_cvt_pk_bf16_f32 v40, -v158, -v159\n"
"v_cvt_pk_bf16_f32 v41, -v156, v2\n"
"s_mov_b32 exec_lo, -1\n"
"s_nop 0\n"
"v_mfma_f32_32x32x8_bf16 v[68:83], v[48:49], v[40:41], 0\n"
"v_mfma_f32_32x32x16_bf16 v[68:83], v[44:47], v[32:35], v[68:83]\n"
"v_max3_f32 v157, v100, v101, v102\n"
"v_max_f32 v157, v157, v103\n"
"v_max3_f32 v157, v157, v104, v105\n"
"v_max3_f32 v157, v157, v106, v107\n"
"v_max3_f32 v157, v157, v108, v109\n"
"v_max3_f32 v157, v157, v110, v111\n"
"v_max3_f32 v157, v157, v112, v113\n"
"v_max3_f32 v157, v157, v114, v115\n"
"v_mov_b32 v158, v157\n"
"s_nop 1\n"
"v_permlane32_swap_b32 v157, v158\n"
"v_max_f32 v157, v157, v158\n"
"v_and_b32 v158, 0xffff0000, v157\n"
"v_sub_f32 v157, v157, v158\n"
"v_and_b32 v159, 0xffff0000, v157\n"
"v_sub_f32 v157, v157, v159\n"
"s_mov_b32 exec_lo, 0\n"
"v_cvt_pk_bf16_f32 v42, -v158, -v159\n"
"v_cvt_pk_bf16_f32 v43, -v157, v2\n"
"s_mov_b32 exec_lo, -1\n"
"v_lshlrev_b32 v67, 7, v4\n"
"s_lshl_b32 s53, s22, 13\n"
"s_add_u32 s53, s53, 1024\n"
"v_mfma_f32_32x32x8_bf16 v[84:99], v[48:49], v[42:43], 0\n"
"v_exp_f32 v68, v68\n"
"v_exp_f32 v69, v69\n"
"v_exp_f32 v70, v70\n"
"v_exp_f32 v71, v71\n"
"v_mfma_f32_32x32x16_bf16 v[84:99], v[44:47], v[36:39], v[84:99]\n"
"ds_read_b128 v[44:47], v60 offset:21696\n"
"v_exp_f32 v72, v72\n"
"v_exp_f32 v73, v73\n"
"v_exp_f32 v74, v74\n"
"v_exp_f32 v75, v75\n"
"ds_read_b128 v[148:151], v65 offset:20672\n"
"v_exp_f32 v76, v76\n"
"v_exp_f32 v77, v77\n"
"v_exp_f32 v78, v78\n"
"v_exp_f32 v79, v79\n"
"s_waitcnt lgkmcnt(1)\n"
"ds_read_b128 v[152:155], v66 offset:20672\n"
"v_exp_f32 v80, v80\n"
"v_exp_f32 v81, v81\n"
"v_exp_f32 v82, v82\n"
"v_exp_f32 v83, v83\n"
"v_mfma_f32_32x32x8_bf16 v[100:115], v[50:51], v[40:41], 0\n"
"v_exp_f32 v84, v84\n"
"v_exp_f32 v85, v85\n"
"v_exp_f32 v86, v86\n"
"v_exp_f32 v87, v87\n"
"v_mfma_f32_32x32x16_bf16 v[100:115], v[44:47], v[32:35], v[100:115]\n"
"v_exp_f32 v88, v88\n"
"v_exp_f32 v89, v89\n"
"v_exp_f32 v90, v90\n"
"v_exp_f32 v91, v91\n"
"v_smfmac_f32_32x32x32_bf16 v[116:131], v[52:55], v[68:75], v62\n"
"s_waitcnt lgkmcnt(0)\n"
"v_exp_f32 v92, v92\n"
"v_exp_f32 v93, v93\n"
"v_exp_f32 v94, v94\n"
"v_exp_f32 v95, v95\n"
"v_smfmac_f32_32x32x32_bf16 v[116:131], v[56:59], v[76:83], v62\n"
"v_exp_f32 v96, v96\n"
"v_exp_f32 v97, v97\n"
"v_exp_f32 v98, v98\n"
"v_exp_f32 v99, v99\n"
"v_mfma_f32_32x32x8_bf16 v[68:83], v[50:51], v[42:43], 0\n"
"v_exp_f32 v100, v100\n"
"v_exp_f32 v101, v101\n"
"v_exp_f32 v102, v102\n"
"v_exp_f32 v103, v103\n"
"v_mfma_f32_32x32x16_bf16 v[68:83], v[44:47], v[36:39], v[68:83]\n"
"ds_read_b128 v[48:51], v61 offset:25840\n"
"ds_read_b128 v[44:47], v60 offset:25840\n"
"v_exp_f32 v104, v104\n"
"v_exp_f32 v105, v105\n"
"v_exp_f32 v106, v106\n"
"v_exp_f32 v107, v107\n"
"v_smfmac_f32_32x32x32_bf16 v[132:147], v[52:55], v[84:91], v62\n"
"ds_read_b128 v[52:55], v63 offset:25840\n"
"v_exp_f32 v108, v108\n"
"v_exp_f32 v109, v109\n"
"v_exp_f32 v110, v110\n"
"v_exp_f32 v111, v111\n"
"v_smfmac_f32_32x32x32_bf16 v[132:147], v[56:59], v[92:99], v62\n"
"s_waitcnt lgkmcnt(1)\n"
"ds_read_b128 v[56:59], v64 offset:25840\n"
"v_exp_f32 v112, v112\n"
"v_exp_f32 v113, v113\n"
"v_exp_f32 v114, v114\n"
"v_exp_f32 v115, v115\n"
"v_mfma_f32_32x32x8_bf16 v[84:99], v[48:49], v[40:41], 0\n"
"v_exp_f32 v68, v68\n"
"v_exp_f32 v69, v69\n"
"v_exp_f32 v70, v70\n"
"v_exp_f32 v71, v71\n"
"v_mfma_f32_32x32x16_bf16 v[84:99], v[44:47], v[32:35], v[84:99]\n"
"v_exp_f32 v72, v72\n"
"v_exp_f32 v73, v73\n"
"v_exp_f32 v74, v74\n"
"v_exp_f32 v75, v75\n"
"v_smfmac_f32_32x32x32_bf16 v[116:131], v[148:151], v[100:107], v62\n"
"s_waitcnt lgkmcnt(0)\n"
"v_exp_f32 v76, v76\n"
"v_exp_f32 v77, v77\n"
"v_exp_f32 v78, v78\n"
"v_exp_f32 v79, v79\n"
"v_smfmac_f32_32x32x32_bf16 v[116:131], v[152:155], v[108:115], v62\n"
"v_exp_f32 v80, v80\n"
"v_exp_f32 v81, v81\n"
"v_exp_f32 v82, v82\n"
"v_exp_f32 v83, v83\n"
"v_mfma_f32_32x32x8_bf16 v[100:115], v[48:49], v[42:43], 0\n"
"v_exp_f32 v84, v84\n"
"v_exp_f32 v85, v85\n"
"v_exp_f32 v86, v86\n"
"v_exp_f32 v87, v87\n"
"v_mfma_f32_32x32x16_bf16 v[100:115], v[44:47], v[36:39], v[100:115]\n"
"ds_read_b128 v[44:47], v60 offset:26864\n"
"v_exp_f32 v88, v88\n"
"v_exp_f32 v89, v89\n"
"v_exp_f32 v90, v90\n"
"v_exp_f32 v91, v91\n"
"v_smfmac_f32_32x32x32_bf16 v[132:147], v[148:151], v[68:75], v62\n"
"ds_read_b128 v[148:151], v65 offset:25840\n"
"v_exp_f32 v92, v92\n"
"v_exp_f32 v93, v93\n"
"v_exp_f32 v94, v94\n"
"v_exp_f32 v95, v95\n"
"v_smfmac_f32_32x32x32_bf16 v[132:147], v[152:155], v[76:83], v62\n"
"s_waitcnt lgkmcnt(1)\n"
"ds_read_b128 v[152:155], v66 offset:25840\n"
"v_exp_f32 v96, v96\n"
"v_exp_f32 v97, v97\n"
"v_exp_f32 v98, v98\n"
"v_exp_f32 v99, v99\n"
"v_mfma_f32_32x32x8_bf16 v[68:83], v[50:51], v[40:41], 0\n"
"v_exp_f32 v100, v100\n"
"v_exp_f32 v101, v101\n"
"v_exp_f32 v102, v102\n"
"v_exp_f32 v103, v103\n"
"v_mfma_f32_32x32x16_bf16 v[68:83], v[44:47], v[32:35], v[68:83]\n"
"s_waitcnt vmcnt(0)\n"
"v_cmp_ne_u64 s[8:9], s[40:41], v[172:173]\n"
"v_exp_f32 v104, v104\n"
"v_exp_f32 v105, v105\n"
"v_exp_f32 v106, v106\n"
"v_exp_f32 v107, v107\n"
"v_smfmac_f32_32x32x32_bf16 v[116:131], v[52:55], v[84:91], v62\n"
"s_waitcnt lgkmcnt(0)\n"
"v_exp_f32 v108, v108\n"
"v_exp_f32 v109, v109\n"
"v_exp_f32 v110, v110\n"
"v_exp_f32 v111, v111\n"
"v_smfmac_f32_32x32x32_bf16 v[116:131], v[56:59], v[92:99], v62\n"
"s_cmp_lg_u32 s23, 0\n"
"s_cbranch_scc1 Ll032_%=\n"
"buffer_store_dwordx4 v[176:179], v1, s[12:15], s76 offen offset:0 sc1\n"
"buffer_store_dwordx4 v[180:183], v1, s[12:15], s76 offen offset:1024 sc1\n"
"buffer_store_dwordx4 v[184:187], v1, s[12:15], s76 offen offset:2048 sc1\n"
"Lpad3_%=:\n"
"s_cbranch_execz Lpadback0_%=\n"
"v_mfma_f32_16x16x32_bf16 v[228:231], v[196:199], v[176:179], 0\n"
"v_mfma_f32_16x16x32_bf16 v[228:231], v[196:199], v[180:183], v[228:231]\n"
"v_mfma_f32_16x16x32_bf16 v[228:231], v[196:199], v[184:187], v[228:231]\n"
"v_mfma_f32_16x16x32_bf16 v[228:231], v[196:199], v[188:191], v[228:231]\n"
"v_mfma_f32_16x16x32_bf16 v[228:231], v[196:199], v[192:195], v[228:231]\n"
"s_barrier\n"
"Ll032_%=:\n"
"v_exp_f32 v112, v112\n"
"v_exp_f32 v113, v113\n"
"v_exp_f32 v114, v114\n"
"v_exp_f32 v115, v115\n"
"v_mfma_f32_32x32x8_bf16 v[84:99], v[50:51], v[42:43], 0\n"
"v_exp_f32 v68, v68\n"
"v_exp_f32 v69, v69\n"
"v_exp_f32 v70, v70\n"
"v_exp_f32 v71, v71\n"
"v_mfma_f32_32x32x16_bf16 v[84:99], v[44:47], v[36:39], v[84:99]\n"
"s_bfe_u32 s45, s8, 0x80008\n"
"s_cbranch_scc1 Lcs18_%=\n"
"Lcb19_%=:\n"
"ds_read_b128 v[48:51], v61 offset:31008\n"
"ds_read_b128 v[44:47], v60 offset:31008\n"
"v_exp_f32 v72, v72\n"
"v_exp_f32 v73, v73\n"
"v_exp_f32 v74, v74\n"
"v_exp_f32 v75, v75\n"
"v_smfmac_f32_32x32x32_bf16 v[132:147], v[52:55], v[100:107], v62\n"
"s_add_u32 s63, s63, 20480\n"
"s_cmp_ge_u32 s63, 163840\n"
"s_cselect_b32 s46, 163840, 0\n"
"ds_read_b128 v[52:55], v63 offset:31008\n"
"v_exp_f32 v76, v76\n"
"v_exp_f32 v77, v77\n"
"v_exp_f32 v78, v78\n"
"v_exp_f32 v79, v79\n"
"v_smfmac_f32_32x32x32_bf16 v[132:147], v[56:59], v[108:115], v62\n"
"s_waitcnt lgkmcnt(1)\n"
"s_sub_u32 s63, s63, s46\n"
"ds_read_b128 v[56:59], v64 offset:31008\n"
"v_exp_f32 v80, v80\n"
"v_exp_f32 v81, v81\n"
"v_exp_f32 v82, v82\n"
"v_exp_f32 v83, v83\n"
"v_mfma_f32_32x32x8_bf16 v[100:115], v[48:49], v[40:41], 0\n"
"v_exp_f32 v84, v84\n"
"v_exp_f32 v85, v85\n"
"v_exp_f32 v86, v86\n"
"v_exp_f32 v87, v87\n"
"v_mfma_f32_32x32x16_bf16 v[100:115], v[44:47], v[32:35], v[100:115]\n"
"s_add_u32 s46, s63, s28\n"
"v_exp_f32 v88, v88\n"
"v_exp_f32 v89, v89\n"
"v_exp_f32 v90, v90\n"
"v_exp_f32 v91, v91\n"
"v_smfmac_f32_32x32x32_bf16 v[116:131], v[148:151], v[68:75], v62\n"
"s_waitcnt lgkmcnt(0)\n"
"s_add_u32 s68, s4, s46\n"
"s_addc_u32 s69, s5, 0\n"
"v_exp_f32 v92, v92\n"
"v_exp_f32 v93, v93\n"
"v_exp_f32 v94, v94\n"
"v_exp_f32 v95, v95\n"
"v_smfmac_f32_32x32x32_bf16 v[116:131], v[152:155], v[76:83], v62\n"
"s_add_u32 m0, s78, 122496\n"
"s_nop 0\n"
"global_load_lds_dwordx4 v1, s[68:69] offset:0 sc1\n"
"global_load_lds_dwordx4 v1, s[68:69] offset:1024 sc1\n"
"global_load_lds_dwordx4 v1, s[68:69] offset:2048 sc1\n"
"global_load_lds_dwordx4 v1, s[68:69] offset:3072 sc1\n"
"v_exp_f32 v96, v96\n"
"v_exp_f32 v97, v97\n"
"v_exp_f32 v98, v98\n"
"v_exp_f32 v99, v99\n"
"v_mfma_f32_32x32x8_bf16 v[68:83], v[48:49], v[42:43], 0\n"
"v_exp_f32 v100, v100\n"
"v_exp_f32 v101, v101\n"
"v_exp_f32 v102, v102\n"
"v_exp_f32 v103, v103\n"
"v_mfma_f32_32x32x16_bf16 v[68:83], v[44:47], v[36:39], v[68:83]\n"
"s_add_u32 s68, s68, 4096\n"
"s_addc_u32 s69, s69, 0\n"
"ds_read_b128 v[44:47], v60 offset:32032\n"
"v_exp_f32 v104, v104\n"
"v_exp_f32 v105, v105\n"
"v_exp_f32 v106, v106\n"
"v_exp_f32 v107, v107\n"
"v_smfmac_f32_32x32x32_bf16 v[132:147], v[148:151], v[84:91], v62\n"
"s_add_u32 m0, s78, 126592\n"
"ds_read_b128 v[148:151], v65 offset:31008\n"
"v_exp_f32 v108, v108\n"
"v_exp_f32 v109, v109\n"
"v_exp_f32 v110, v110\n"
"v_exp_f32 v111, v111\n"
"v_smfmac_f32_32x32x32_bf16 v[132:147], v[152:155], v[92:99], v62\n"
"s_waitcnt lgkmcnt(1)\n"
"ds_read_b128 v[152:155], v66 offset:31008\n"
"v_exp_f32 v112, v112\n"
"v_exp_f32 v113, v113\n"
"v_exp_f32 v114, v114\n"
"v_exp_f32 v115, v115\n"
"v_mfma_f32_32x32x8_bf16 v[84:99], v[50:51], v[40:41], 0\n"
"v_exp_f32 v68, v68\n"
"v_exp_f32 v69, v69\n"
"v_exp_f32 v70, v70\n"
"v_exp_f32 v71, v71\n"
"v_mfma_f32_32x32x16_bf16 v[84:99], v[44:47], v[32:35], v[84:99]\n"
"v_exp_f32 v72, v72\n"
"v_exp_f32 v73, v73\n"
"v_exp_f32 v74, v74\n"
"v_exp_f32 v75, v75\n"
"v_smfmac_f32_32x32x32_bf16 v[116:131], v[52:55], v[100:107], v62\n"
"s_waitcnt lgkmcnt(0)\n"
"v_exp_f32 v76, v76\n"
"v_exp_f32 v77, v77\n"
"v_exp_f32 v78, v78\n"
"v_exp_f32 v79, v79\n"
"v_smfmac_f32_32x32x32_bf16 v[116:131], v[56:59], v[108:115], v62\n"
"global_load_lds_dwordx4 v1, s[68:69] offset:0 sc1\n"
"v_exp_f32 v80, v80\n"
"v_exp_f32 v81, v81\n"
"v_exp_f32 v82, v82\n"
"v_exp_f32 v83, v83\n"
"v_mfma_f32_32x32x8_bf16 v[100:115], v[50:51], v[42:43], 0\n"
"v_exp_f32 v84, v84\n"
"v_exp_f32 v85, v85\n"
"v_exp_f32 v86, v86\n"
"v_exp_f32 v87, v87\n"
"v_mfma_f32_32x32x16_bf16 v[100:115], v[44:47], v[36:39], v[100:115]\n"
"s_mul_i32 s54, s34, 5\n"
"ds_read_b128 v[48:51], v61 offset:36176\n"
"ds_read_b128 v[44:47], v60 offset:36176\n"
"v_exp_f32 v88, v88\n"
"v_exp_f32 v89, v89\n"
"v_exp_f32 v90, v90\n"
"v_exp_f32 v91, v91\n"
"v_smfmac_f32_32x32x32_bf16 v[132:147], v[52:55], v[68:75], v62\n"
"s_lshl_b32 s47, s35, 1\n"
"s_add_u32 s54, s54, s47\n"
"ds_read_b128 v[52:55], v63 offset:36176\n"
"v_exp_f32 v92, v92\n"
"v_exp_f32 v93, v93\n"
"v_exp_f32 v94, v94\n"
"v_exp_f32 v95, v95\n"
"v_smfmac_f32_32x32x32_bf16 v[132:147], v[56:59], v[76:83], v62\n"
"s_waitcnt lgkmcnt(1)\n"
"s_lshl_b32 s54, s54, 10\n"
"ds_read_b128 v[56:59], v64 offset:36176\n"
"v_exp_f32 v96, v96\n"
"v_exp_f32 v97, v97\n"
"v_exp_f32 v98, v98\n"
"v_exp_f32 v99, v99\n"
"v_mfma_f32_32x32x8_bf16 v[68:83], v[48:49], v[40:41], 0\n"
"v_exp_f32 v100, v100\n"
"v_exp_f32 v101, v101\n"
"v_exp_f32 v102, v102\n"
"v_exp_f32 v103, v103\n"
"v_mfma_f32_32x32x16_bf16 v[68:83], v[44:47], v[32:35], v[68:83]\n"
"s_add_u32 s54, s54, s29\n"
"v_exp_f32 v104, v104\n"
"v_exp_f32 v105, v105\n"
"v_exp_f32 v106, v106\n"
"v_exp_f32 v107, v107\n"
"v_smfmac_f32_32x32x32_bf16 v[116:131], v[148:151], v[84:91], v62\n"
"s_waitcnt lgkmcnt(0)\n"
"s_mul_i32 s55, s34, 5120\n"
"v_exp_f32 v108, v108\n"
"v_exp_f32 v109, v109\n"
"v_exp_f32 v110, v110\n"
"v_exp_f32 v111, v111\n"
"v_smfmac_f32_32x32x32_bf16 v[116:131], v[152:155], v[92:99], v62\n"
"v_exp_f32 v112, v112\n"
"v_exp_f32 v113, v113\n"
"v_exp_f32 v114, v114\n"
"v_exp_f32 v115, v115\n"
"v_mfma_f32_32x32x8_bf16 v[84:99], v[48:49], v[42:43], 0\n"
"v_exp_f32 v68, v68\n"
"v_exp_f32 v69, v69\n"
"v_exp_f32 v70, v70\n"
"v_exp_f32 v71, v71\n"
"v_mfma_f32_32x32x16_bf16 v[84:99], v[44:47], v[36:39], v[84:99]\n"
"ds_read_b128 v[44:47], v60 offset:37200\n"
"v_exp_f32 v72, v72\n"
"v_exp_f32 v73, v73\n"
"v_exp_f32 v74, v74\n"
"v_exp_f32 v75, v75\n"
"v_smfmac_f32_32x32x32_bf16 v[132:147], v[148:151], v[100:107], v62\n"
"ds_read_b128 v[148:151], v65 offset:36176\n"
"v_exp_f32 v76, v76\n"
"v_exp_f32 v77, v77\n"
"v_exp_f32 v78, v78\n"
"v_exp_f32 v79, v79\n"
"v_smfmac_f32_32x32x32_bf16 v[132:147], v[152:155], v[108:115], v62\n"
"s_waitcnt lgkmcnt(1)\n"
"ds_read_b128 v[152:155], v66 offset:36176\n"
"v_exp_f32 v80, v80\n"
"v_exp_f32 v81, v81\n"
"v_exp_f32 v82, v82\n"
"v_exp_f32 v83, v83\n"
"v_mfma_f32_32x32x8_bf16 v[100:115], v[50:51], v[40:41], 0\n"
"v_exp_f32 v84, v84\n"
"v_exp_f32 v85, v85\n"
"v_exp_f32 v86, v86\n"
"v_exp_f32 v87, v87\n"
"v_mfma_f32_32x32x16_bf16 v[100:115], v[44:47], v[32:35], v[100:115]\n"
"v_exp_f32 v88, v88\n"
"v_exp_f32 v89, v89\n"
"v_exp_f32 v90, v90\n"
"v_exp_f32 v91, v91\n"
"v_smfmac_f32_32x32x32_bf16 v[116:131], v[52:55], v[68:75], v62\n"
"s_waitcnt vmcnt(0) lgkmcnt(0)\n"
"s_barrier\n"
"v_exp_f32 v92, v92\n"
"v_exp_f32 v93, v93\n"
"v_exp_f32 v94, v94\n"
"v_exp_f32 v95, v95\n"
"v_smfmac_f32_32x32x32_bf16 v[116:131], v[56:59], v[76:83], v62\n"
"s_cmp_lg_u32 s23, 0\n"
"s_cbranch_scc1 Ll033_%=\n"
"buffer_store_dwordx4 v[188:191], v1, s[12:15], s76 offen offset:3072 sc1\n"
"buffer_store_dwordx4 v[192:195], v1, s[12:15], s10 offen offset:0 sc1\n"
"buffer_store_dwordx4 v[220:223], v1, s[12:15], s71 offen sc1\n"
"buffer_store_dwordx4 v[224:227], v1, s[12:15], s72 offen sc1\n"
"v_and_b32 v201, 0xffff0000, v228\n"
"v_cvt_pk_bf16_f32 v200, v201, v201\n"
"v_sub_f32 v201, v228, v201\n"
"v_cvt_pk_bf16_f32 v201, v201, v2\n"
"v_lshl_or_b32 v201, v210, 16, v201\n"
"v_mov_b32 v202, v210\n"
"v_mov_b32 v203, 0\n"
"v_and_b32 v204, 0xf0, v1\n"
"v_mov_b64 v[208:209], 0\n"
"v_mov_b64 v[210:211], 0\n"
"Ll033_%=:\n"
"v_exp_f32 v96, v96\n"
"v_exp_f32 v97, v97\n"
"v_exp_f32 v98, v98\n"
"v_exp_f32 v99, v99\n"
"v_mfma_f32_32x32x8_bf16 v[68:83], v[50:51], v[42:43], 0\n"
"v_exp_f32 v100, v100\n"
"v_exp_f32 v101, v101\n"
"v_exp_f32 v102, v102\n"
"v_exp_f32 v103, v103\n"
"v_mfma_f32_32x32x16_bf16 v[68:83], v[44:47], v[36:39], v[68:83]\n"
"s_bfe_u32 s45, s8, 0x80010\n"
"s_cbranch_scc1 Lcs20_%=\n"
"Lcb21_%=:\n"
"ds_read_b128 v[48:51], v61 offset:0\n"
"ds_read_b128 v[44:47], v60 offset:0\n"
"v_exp_f32 v104, v104\n"
"v_exp_f32 v105, v105\n"
"v_exp_f32 v106, v106\n"
"v_exp_f32 v107, v107\n"
"v_smfmac_f32_32x32x32_bf16 v[132:147], v[52:55], v[84:91], v62\n"
"s_add_u32 s63, s63, 20480\n"
"s_cmp_ge_u32 s63, 163840\n"
"s_cselect_b32 s46, 163840, 0\n"
"ds_read_b128 v[52:55], v63 offset:0\n"
"v_exp_f32 v108, v108\n"
"v_exp_f32 v109, v109\n"
"v_exp_f32 v110, v110\n"
"v_exp_f32 v111, v111\n"
"v_smfmac_f32_32x32x32_bf16 v[132:147], v[56:59], v[92:99], v62\n"
"s_waitcnt lgkmcnt(1)\n"
"s_sub_u32 s63, s63, s46\n"
"ds_read_b128 v[56:59], v64 offset:0\n"
"v_exp_f32 v112, v112\n"
"v_exp_f32 v113, v113\n"
"v_exp_f32 v114, v114\n"
"v_exp_f32 v115, v115\n"
"v_mfma_f32_32x32x8_bf16 v[84:99], v[48:49], v[40:41], 0\n"
"v_exp_f32 v68, v68\n"
"v_exp_f32 v69, v69\n"
"v_exp_f32 v70, v70\n"
"v_exp_f32 v71, v71\n"
"v_mfma_f32_32x32x16_bf16 v[84:99], v[44:47], v[32:35], v[84:99]\n"
"s_add_u32 s46, s63, s28\n"
"v_exp_f32 v72, v72\n"
"v_exp_f32 v73, v73\n"
"v_exp_f32 v74, v74\n"
"v_exp_f32 v75, v75\n"
"v_smfmac_f32_32x32x32_bf16 v[116:131], v[148:151], v[100:107], v62\n"
"s_waitcnt lgkmcnt(0)\n"
"s_add_u32 s68, s4, s46\n"
"s_addc_u32 s69, s5, 0\n"
"v_exp_f32 v76, v76\n"
"v_exp_f32 v77, v77\n"
"v_exp_f32 v78, v78\n"
"v_exp_f32 v79, v79\n"
"v_smfmac_f32_32x32x32_bf16 v[116:131], v[152:155], v[108:115], v62\n"
"s_add_u32 m0, s78, 143168\n"
"s_nop 0\n"
"global_load_lds_dwordx4 v1, s[68:69] offset:0 sc1\n"
"global_load_lds_dwordx4 v1, s[68:69] offset:1024 sc1\n"
"global_load_lds_dwordx4 v1, s[68:69] offset:2048 sc1\n"
"global_load_lds_dwordx4 v1, s[68:69] offset:3072 sc1\n"
"v_exp_f32 v80, v80\n"
"v_exp_f32 v81, v81\n"
"v_exp_f32 v82, v82\n"
"v_exp_f32 v83, v83\n"
"v_mfma_f32_32x32x8_bf16 v[100:115], v[48:49], v[42:43], 0\n"
"v_exp_f32 v84, v84\n"
"v_exp_f32 v85, v85\n"
"v_exp_f32 v86, v86\n"
"v_exp_f32 v87, v87\n"
"v_mfma_f32_32x32x16_bf16 v[100:115], v[44:47], v[36:39], v[100:115]\n"
"s_add_u32 s68, s68, 4096\n"
"s_addc_u32 s69, s69, 0\n"
"ds_read_b128 v[44:47], v60 offset:1024\n"
"v_exp_f32 v88, v88\n"
"v_exp_f32 v89, v89\n"
"v_exp_f32 v90, v90\n"
"v_exp_f32 v91, v91\n"
"v_smfmac_f32_32x32x32_bf16 v[132:147], v[148:151], v[68:75], v62\n"
"s_add_u32 m0, s78, 147264\n"
"ds_read_b128 v[148:151], v65 offset:0\n"
"v_exp_f32 v92, v92\n"
"v_exp_f32 v93, v93\n"
"v_exp_f32 v94, v94\n"
"v_exp_f32 v95, v95\n"
"v_smfmac_f32_32x32x32_bf16 v[132:147], v[152:155], v[76:83], v62\n"
"s_waitcnt lgkmcnt(1)\n"
"ds_read_b128 v[152:155], v66 offset:0\n"
"v_exp_f32 v96, v96\n"
"v_exp_f32 v97, v97\n"
"v_exp_f32 v98, v98\n"
"v_exp_f32 v99, v99\n"
"v_mfma_f32_32x32x8_bf16 v[68:83], v[50:51], v[40:41], 0\n"
"v_exp_f32 v100, v100\n"
"v_exp_f32 v101, v101\n"
"v_exp_f32 v102, v102\n"
"v_exp_f32 v103, v103\n"
"v_mfma_f32_32x32x16_bf16 v[68:83], v[44:47], v[32:35], v[68:83]\n"
"v_exp_f32 v104, v104\n"
"v_exp_f32 v105, v105\n"
"v_exp_f32 v106, v106\n"
"v_exp_f32 v107, v107\n"
"v_smfmac_f32_32x32x32_bf16 v[116:131], v[52:55], v[84:91], v62\n"
"s_waitcnt lgkmcnt(0)\n"
"v_exp_f32 v108, v108\n"
"v_exp_f32 v109, v109\n"
"v_exp_f32 v110, v110\n"
"v_exp_f32 v111, v111\n"
"v_smfmac_f32_32x32x32_bf16 v[116:131], v[56:59], v[92:99], v62\n"
"global_load_lds_dwordx4 v1, s[68:69] offset:0 sc1\n"
"v_exp_f32 v112, v112\n"
"v_exp_f32 v113, v113\n"
"v_exp_f32 v114, v114\n"
"v_exp_f32 v115, v115\n"
"v_mfma_f32_32x32x8_bf16 v[84:99], v[50:51], v[42:43], 0\n"
"v_exp_f32 v68, v68\n"
"v_exp_f32 v69, v69\n"
"v_exp_f32 v70, v70\n"
"v_exp_f32 v71, v71\n"
"v_mfma_f32_32x32x16_bf16 v[84:99], v[44:47], v[36:39], v[84:99]\n"
"s_lshl_b32 s47, s35, 9\n"
"ds_read_b128 v[48:51], v61 offset:5168\n"
"ds_read_b128 v[44:47], v60 offset:5168\n"
"v_exp_f32 v72, v72\n"
"v_exp_f32 v73, v73\n"
"v_exp_f32 v74, v74\n"
"v_exp_f32 v75, v75\n"
"v_smfmac_f32_32x32x32_bf16 v[132:147], v[52:55], v[100:107], v62\n"
"s_add_u32 s55, s55, s47\n"
"s_add_u32 s55, s55, s29\n"
"ds_read_b128 v[52:55], v63 offset:5168\n"
"v_exp_f32 v76, v76\n"
"v_exp_f32 v77, v77\n"
"v_exp_f32 v78, v78\n"
"v_exp_f32 v79, v79\n"
"v_smfmac_f32_32x32x32_bf16 v[132:147], v[56:59], v[108:115], v62\n"
"s_waitcnt lgkmcnt(1)\n"
"s_add_u32 s55, s55, 4096\n"
"ds_read_b128 v[56:59], v64 offset:5168\n"
"v_exp_f32 v80, v80\n"
"v_exp_f32 v81, v81\n"
"v_exp_f32 v82, v82\n"
"v_exp_f32 v83, v83\n"
"v_mfma_f32_32x32x8_bf16 v[100:115], v[48:49], v[40:41], 0\n"
"v_exp_f32 v84, v84\n"
"v_exp_f32 v85, v85\n"
"v_exp_f32 v86, v86\n"
"v_exp_f32 v87, v87\n"
"v_mfma_f32_32x32x16_bf16 v[100:115], v[44:47], v[32:35], v[100:115]\n"
"v_exp_f32 v88, v88\n"
"v_exp_f32 v89, v89\n"
"v_exp_f32 v90, v90\n"
"v_exp_f32 v91, v91\n"
"v_smfmac_f32_32x32x32_bf16 v[116:131], v[148:151], v[68:75], v62\n"
"s_waitcnt lgkmcnt(0)\n"
"v_exp_f32 v92, v92\n"
"v_exp_f32 v93, v93\n"
"v_exp_f32 v94, v94\n"
"v_exp_f32 v95, v95\n"
"v_smfmac_f32_32x32x32_bf16 v[116:131], v[152:155], v[76:83], v62\n"
"s_cmp_lg_u32 s23, 0\n"
"s_cbranch_scc1 Ll034_%=\n"
"buffer_store_dwordx4 v[200:203], v204, s[12:15], s77 offen sc1\n"
"buffer_store_dwordx4 v[216:219], v1, s[12:15], s11 offen sc1\n"
"buffer_store_dwordx4 v[208:211], v1, s[12:15], s84 offen sc1\n"
"Ll034_%=:\n"
"v_mov_b32 v160, 0x7fffffff\n"
"v_mov_b32 v161, 0x7fffffff\n"
"v_mov_b32 v162, 0x7fffffff\n"
"v_mov_b32 v163, 0x7fffffff\n"
"v_exp_f32 v96, v96\n"
"v_exp_f32 v97, v97\n"
"v_exp_f32 v98, v98\n"
"v_exp_f32 v99, v99\n"
"v_mfma_f32_32x32x8_bf16 v[68:83], v[48:49], v[42:43], 0\n"
"v_exp_f32 v100, v100\n"
"v_exp_f32 v101, v101\n"
"v_exp_f32 v102, v102\n"
"v_exp_f32 v103, v103\n"
"v_mfma_f32_32x32x16_bf16 v[68:83], v[44:47], v[36:39], v[68:83]\n"
"s_lshl_b32 s44, s20, 2\n"
"ds_read_b128 v[44:47], v60 offset:6192\n"
"v_exp_f32 v104, v104\n"
"v_exp_f32 v105, v105\n"
"v_exp_f32 v106, v106\n"
"v_exp_f32 v107, v107\n"
"v_smfmac_f32_32x32x32_bf16 v[132:147], v[148:151], v[84:91], v62\n"
"s_add_u32 s44, s44, s22\n"
"s_lshl_b32 s44, s44, 3\n"
"ds_read_b128 v[148:151], v65 offset:5168\n"
"v_exp_f32 v108, v108\n"
"v_exp_f32 v109, v109\n"
"v_exp_f32 v110, v110\n"
"v_exp_f32 v111, v111\n"
"v_smfmac_f32_32x32x32_bf16 v[132:147], v[152:155], v[92:99], v62\n"
"s_waitcnt lgkmcnt(1)\n"
"s_lshl_b32 s44, s44, 7\n"
"ds_read_b128 v[152:155], v66 offset:5168\n"
"v_exp_f32 v112, v112\n"
"v_exp_f32 v113, v113\n"
"v_exp_f32 v114, v114\n"
"v_exp_f32 v115, v115\n"
"v_mfma_f32_32x32x8_bf16 v[84:99], v[50:51], v[40:41], 0\n"
"v_exp_f32 v68, v68\n"
"v_exp_f32 v69, v69\n"
"v_exp_f32 v70, v70\n"
"v_exp_f32 v71, v71\n"
"v_mfma_f32_32x32x16_bf16 v[84:99], v[44:47], v[32:35], v[84:99]\n"
"s_add_u32 s44, s44, s27\n"
"v_exp_f32 v72, v72\n"
"v_exp_f32 v73, v73\n"
"v_exp_f32 v74, v74\n"
"v_exp_f32 v75, v75\n"
"v_smfmac_f32_32x32x32_bf16 v[116:131], v[52:55], v[100:107], v62\n"
"s_waitcnt lgkmcnt(0)\n"
"v_exp_f32 v76, v76\n"
"v_exp_f32 v77, v77\n"
"v_exp_f32 v78, v78\n"
"v_exp_f32 v79, v79\n"
"v_smfmac_f32_32x32x32_bf16 v[116:131], v[56:59], v[108:115], v62\n"
"buffer_store_dwordx4 v[160:163], v1, s[12:15], s54 offen offset:0 sc1\n"
"v_lshl_add_u32 v174, v4, 7, s44\n"
"v_exp_f32 v80, v80\n"
"v_exp_f32 v81, v81\n"
"v_exp_f32 v82, v82\n"
"v_exp_f32 v83, v83\n"
"v_mfma_f32_32x32x8_bf16 v[100:115], v[50:51], v[42:43], 0\n"
"v_exp_f32 v84, v84\n"
"v_exp_f32 v85, v85\n"
"v_exp_f32 v86, v86\n"
"v_exp_f32 v87, v87\n"
"v_mfma_f32_32x32x16_bf16 v[100:115], v[44:47], v[36:39], v[100:115]\n"
"s_lshl_b32 s44, s20, 2\n"
"ds_read_b128 v[48:51], v61 offset:10336\n"
"ds_read_b128 v[44:47], v60 offset:10336\n"
"v_exp_f32 v88, v88\n"
"v_exp_f32 v89, v89\n"
"v_exp_f32 v90, v90\n"
"v_exp_f32 v91, v91\n"
"v_smfmac_f32_32x32x32_bf16 v[132:147], v[52:55], v[68:75], v62\n"
"s_add_u32 s44, s44, s22\n"
"s_lshl_b32 s44, s44, 3\n"
"ds_read_b128 v[52:55], v63 offset:10336\n"
"v_exp_f32 v92, v92\n"
"v_exp_f32 v93, v93\n"
"v_exp_f32 v94, v94\n"
"v_exp_f32 v95, v95\n"
"v_smfmac_f32_32x32x32_bf16 v[132:147], v[56:59], v[76:83], v62\n"
"s_waitcnt lgkmcnt(1)\n"
"s_add_u32 s44, s44, s21\n"
"ds_read_b128 v[56:59], v64 offset:10336\n"
"v_exp_f32 v96, v96\n"
"v_exp_f32 v97, v97\n"
"v_exp_f32 v98, v98\n"
"v_exp_f32 v99, v99\n"
"v_mfma_f32_32x32x8_bf16 v[68:83], v[48:49], v[40:41], 0\n"
"v_exp_f32 v100, v100\n"
"v_exp_f32 v101, v101\n"
"v_exp_f32 v102, v102\n"
"v_exp_f32 v103, v103\n"
"v_mfma_f32_32x32x16_bf16 v[68:83], v[44:47], v[32:35], v[68:83]\n"
"s_lshl_b32 s44, s44, 7\n"
"v_exp_f32 v104, v104\n"
"v_exp_f32 v105, v105\n"
"v_exp_f32 v106, v106\n"
"v_exp_f32 v107, v107\n"
"v_smfmac_f32_32x32x32_bf16 v[116:131], v[148:151], v[84:91], v62\n"
"s_waitcnt lgkmcnt(0)\n"
"s_add_u32 s44, s44, s27\n"
"v_exp_f32 v108, v108\n"
"v_exp_f32 v109, v109\n"
"v_exp_f32 v110, v110\n"
"v_exp_f32 v111, v111\n"
"v_smfmac_f32_32x32x32_bf16 v[116:131], v[152:155], v[92:99], v62\n"
"buffer_store_dwordx4 v[160:163], v1, s[12:15], s54 offen offset:1024 sc1\n"
"v_mov_b32 v3, s44\n"
"v_mov_b32 v0, 1\n"
"v_exp_f32 v112, v112\n"
"v_exp_f32 v113, v113\n"
"v_exp_f32 v114, v114\n"
"v_exp_f32 v115, v115\n"
"v_mfma_f32_32x32x8_bf16 v[84:99], v[48:49], v[42:43], 0\n"
"v_exp_f32 v68, v68\n"
"v_exp_f32 v69, v69\n"
"v_exp_f32 v70, v70\n"
"v_exp_f32 v71, v71\n"
"v_mfma_f32_32x32x16_bf16 v[84:99], v[44:47], v[36:39], v[84:99]\n"
"ds_read_b128 v[44:47], v60 offset:11360\n"
"v_exp_f32 v72, v72\n"
"v_exp_f32 v73, v73\n"
"v_exp_f32 v74, v74\n"
"v_exp_f32 v75, v75\n"
"v_smfmac_f32_32x32x32_bf16 v[132:147], v[148:151], v[100:107], v62\n"
"ds_read_b128 v[148:151], v65 offset:10336\n"
"v_exp_f32 v76, v76\n"
"v_exp_f32 v77, v77\n"
"v_exp_f32 v78, v78\n"
"v_exp_f32 v79, v79\n"
"v_smfmac_f32_32x32x32_bf16 v[132:147], v[152:155], v[108:115], v62\n"
"s_waitcnt lgkmcnt(1)\n"
"ds_read_b128 v[152:155], v66 offset:10336\n"
"v_exp_f32 v80, v80\n"
"v_exp_f32 v81, v81\n"
"v_exp_f32 v82, v82\n"
"v_exp_f32 v83, v83\n"
"v_mfma_f32_32x32x8_bf16 v[100:115], v[50:51], v[40:41], 0\n"
"v_exp_f32 v84, v84\n"
"v_exp_f32 v85, v85\n"
"v_exp_f32 v86, v86\n"
"v_exp_f32 v87, v87\n"
"v_mfma_f32_32x32x16_bf16 v[100:115], v[44:47], v[32:35], v[100:115]\n"
"v_exp_f32 v88, v88\n"
"v_exp_f32 v89, v89\n"
"v_exp_f32 v90, v90\n"
"v_exp_f32 v91, v91\n"
"v_smfmac_f32_32x32x32_bf16 v[116:131], v[52:55], v[68:75], v62\n"
"s_waitcnt lgkmcnt(0)\n"
"v_exp_f32 v92, v92\n"
"v_exp_f32 v93, v93\n"
"v_exp_f32 v94, v94\n"
"v_exp_f32 v95, v95\n"
"v_smfmac_f32_32x32x32_bf16 v[116:131], v[56:59], v[76:83], v62\n"
"s_mov_b32 exec_hi, 0\n"
"buffer_store_dwordx4 v[160:163], v1, s[12:15], s55 offen sc1\n"
"s_mov_b32 exec_hi, -1\n"
"v_exp_f32 v96, v96\n"
"v_exp_f32 v97, v97\n"
"v_exp_f32 v98, v98\n"
"v_exp_f32 v99, v99\n"
"v_mfma_f32_32x32x8_bf16 v[68:83], v[50:51], v[42:43], 0\n"
"v_exp_f32 v100, v100\n"
"v_exp_f32 v101, v101\n"
"v_exp_f32 v102, v102\n"
"v_exp_f32 v103, v103\n"
"v_mfma_f32_32x32x16_bf16 v[68:83], v[44:47], v[36:39], v[68:83]\n"
"s_mul_i32 s60, s21, 276480\n"
"ds_read_b128 v[48:51], v61 offset:15504\n"
"ds_read_b128 v[44:47], v60 offset:15504\n"
"v_exp_f32 v104, v104\n"
"v_exp_f32 v105, v105\n"
"v_exp_f32 v106, v106\n"
"v_exp_f32 v107, v107\n"
"v_smfmac_f32_32x32x32_bf16 v[132:147], v[52:55], v[84:91], v62\n"
"s_mul_i32 s61, s22, 71680\n"
"s_add_u32 s60, s60, s61\n"
"ds_read_b128 v[52:55], v63 offset:15504\n"
"v_exp_f32 v108, v108\n"
"v_exp_f32 v109, v109\n"
"v_exp_f32 v110, v110\n"
"v_exp_f32 v111, v111\n"
"v_smfmac_f32_32x32x32_bf16 v[132:147], v[56:59], v[92:99], v62\n"
"s_waitcnt lgkmcnt(1)\n"
"s_add_u32 s60, s60, 4689920\n"
"ds_read_b128 v[56:59], v64 offset:15504\n"
"v_exp_f32 v112, v112\n"
"v_exp_f32 v113, v113\n"
"v_exp_f32 v114, v114\n"
"v_exp_f32 v115, v115\n"
"v_mfma_f32_32x32x8_bf16 v[84:99], v[48:49], v[40:41], 0\n"
"v_exp_f32 v68, v68\n"
"v_exp_f32 v69, v69\n"
"v_exp_f32 v70, v70\n"
"v_exp_f32 v71, v71\n"
"v_mfma_f32_32x32x16_bf16 v[84:99], v[44:47], v[32:35], v[84:99]\n"
"s_add_u32 s60, s4, s60\n"
"s_addc_u32 s61, s5, 0\n"
"v_exp_f32 v72, v72\n"
"v_exp_f32 v73, v73\n"
"v_exp_f32 v74, v74\n"
"v_exp_f32 v75, v75\n"
"v_smfmac_f32_32x32x32_bf16 v[116:131], v[148:151], v[100:107], v62\n"
"s_waitcnt lgkmcnt(0)\n"
"s_lshl_b32 s91, s22, 10\n"
"v_exp_f32 v76, v76\n"
"v_exp_f32 v77, v77\n"
"v_exp_f32 v78, v78\n"
"v_exp_f32 v79, v79\n"
"v_smfmac_f32_32x32x32_bf16 v[116:131], v[152:155], v[108:115], v62\n"
"v_exp_f32 v80, v80\n"
"v_exp_f32 v81, v81\n"
"v_exp_f32 v82, v82\n"
"v_exp_f32 v83, v83\n"
"v_mfma_f32_32x32x8_bf16 v[100:115], v[48:49], v[42:43], 0\n"
"v_exp_f32 v84, v84\n"
"v_exp_f32 v85, v85\n"
"v_exp_f32 v86, v86\n"
"v_exp_f32 v87, v87\n"
"v_mfma_f32_32x32x16_bf16 v[100:115], v[44:47], v[36:39], v[100:115]\n"
"ds_read_b128 v[44:47], v60 offset:16528\n"
"v_exp_f32 v88, v88\n"
"v_exp_f32 v89, v89\n"
"v_exp_f32 v90, v90\n"
"v_exp_f32 v91, v91\n"
"v_smfmac_f32_32x32x32_bf16 v[132:147], v[148:151], v[68:75], v62\n"
"ds_read_b128 v[148:151], v65 offset:15504\n"
"v_exp_f32 v92, v92\n"
"v_exp_f32 v93, v93\n"
"v_exp_f32 v94, v94\n"
"v_exp_f32 v95, v95\n"
"v_smfmac_f32_32x32x32_bf16 v[132:147], v[152:155], v[76:83], v62\n"
"s_waitcnt lgkmcnt(1)\n"
"ds_read_b128 v[152:155], v66 offset:15504\n"
"v_exp_f32 v96, v96\n"
"v_exp_f32 v97, v97\n"
"v_exp_f32 v98, v98\n"
"v_exp_f32 v99, v99\n"
"v_mfma_f32_32x32x8_bf16 v[68:83], v[50:51], v[40:41], 0\n"
"v_exp_f32 v100, v100\n"
"v_exp_f32 v101, v101\n"
"v_exp_f32 v102, v102\n"
"v_exp_f32 v103, v103\n"
"v_mfma_f32_32x32x16_bf16 v[68:83], v[44:47], v[32:35], v[68:83]\n"
"v_exp_f32 v104, v104\n"
"v_exp_f32 v105, v105\n"
"v_exp_f32 v106, v106\n"
"v_exp_f32 v107, v107\n"
"v_smfmac_f32_32x32x32_bf16 v[116:131], v[52:55], v[84:91], v62\n"
"s_waitcnt vmcnt(3) lgkmcnt(0)\n"
"s_barrier\n"
"s_or_b32 s46, s22, s23\n"
"s_cbranch_scc1 Lwr35_%=\n"
"s_lshl_b32 s46, s6, 7\n"
"s_add_u32 s46, s46, 1024\n"
"s_mov_b64 exec, 1\n"
"v_mov_b32 v166, s40\n"
"v_mov_b32 v167, s41\n"
"buffer_store_dwordx2 v[166:167], v2, s[12:15], s46 offen sc1\n"
"s_mov_b64 exec, -1\n"
"Lwr35_%=:\n"
"v_exp_f32 v108, v108\n"
"v_exp_f32 v109, v109\n"
"v_exp_f32 v110, v110\n"
"v_exp_f32 v111, v111\n"
"v_smfmac_f32_32x32x32_bf16 v[116:131], v[56:59], v[92:99], v62\n"
"s_cmp_lg_u32 s23, 0\n"
"s_cbranch_scc1 Ll036_%=\n"
"s_mul_i32 s48, s24, 22528\n"
"s_add_u32 s48, s48, 18936832\n"
"s_mul_i32 s49, s21, 2560\n"
"s_add_u32 s49, s49, s48\n"
"buffer_store_dwordx4 v[160:163], v1, s[12:15], s49 offen offset:0 sc1\n"
"buffer_store_dwordx4 v[160:163], v1, s[12:15], s49 offen offset:1024 sc1\n"
"buffer_store_dwordx4 v[160:163], v5, s[12:15], s48 offen sc1\n"
"s_mul_i32 s48, s20, 5120\n"
"s_add_u32 s48, s48, 1377280\n"
"s_cmp_lt_u32 s21, 4\n"
"s_cselect_b32 s48, s48, 2147418112\n"
"s_mov_b64 exec, s[38:39]\n"
"buffer_store_dwordx2 v[160:161], v7, s[12:15], s48 offen sc1\n"
"s_mov_b64 exec, -1\n"
"Ll036_%=:\n"
"v_exp_f32 v112, v112\n"
"v_exp_f32 v113, v113\n"
"v_exp_f32 v114, v114\n"
"v_exp_f32 v115, v115\n"
"v_mfma_f32_32x32x8_bf16 v[84:99], v[50:51], v[42:43], 0\n"
"v_exp_f32 v68, v68\n"
"v_exp_f32 v69, v69\n"
"v_exp_f32 v70, v70\n"
"v_exp_f32 v71, v71\n"
"v_mfma_f32_32x32x16_bf16 v[84:99], v[44:47], v[36:39], v[84:99]\n"
"s_bfe_u32 s45, s8, 0x80018\n"
"s_cbranch_scc1 Lcs22_%=\n"
"Lcb23_%=:\n"
"ds_read_b128 v[48:51], v61 offset:20672\n"
"ds_read_b128 v[44:47], v60 offset:20672\n"
"v_exp_f32 v72, v72\n"
"v_exp_f32 v73, v73\n"
"v_exp_f32 v74, v74\n"
"v_exp_f32 v75, v75\n"
"v_smfmac_f32_32x32x32_bf16 v[132:147], v[52:55], v[100:107], v62\n"
"s_add_u32 s63, s63, 20480\n"
"s_cmp_ge_u32 s63, 163840\n"
"s_cselect_b32 s46, 163840, 0\n"
"ds_read_b128 v[52:55], v63 offset:20672\n"
"v_exp_f32 v76, v76\n"
"v_exp_f32 v77, v77\n"
"v_exp_f32 v78, v78\n"
"v_exp_f32 v79, v79\n"
"v_smfmac_f32_32x32x32_bf16 v[132:147], v[56:59], v[108:115], v62\n"
"s_waitcnt lgkmcnt(1)\n"
"s_sub_u32 s63, s63, s46\n"
"ds_read_b128 v[56:59], v64 offset:20672\n"
"v_exp_f32 v80, v80\n"
"v_exp_f32 v81, v81\n"
"v_exp_f32 v82, v82\n"
"v_exp_f32 v83, v83\n"
"v_mfma_f32_32x32x8_bf16 v[100:115], v[48:49], v[40:41], 0\n"
"v_exp_f32 v84, v84\n"
"v_exp_f32 v85, v85\n"
"v_exp_f32 v86, v86\n"
"v_exp_f32 v87, v87\n"
"v_mfma_f32_32x32x16_bf16 v[100:115], v[44:47], v[32:35], v[100:115]\n"
"s_add_u32 s46, s63, s28\n"
"v_exp_f32 v88, v88\n"
"v_exp_f32 v89, v89\n"
"v_exp_f32 v90, v90\n"
"v_exp_f32 v91, v91\n"
"v_smfmac_f32_32x32x32_bf16 v[116:131], v[148:151], v[68:75], v62\n"
"s_waitcnt lgkmcnt(0)\n"
"s_add_u32 s68, s4, s46\n"
"s_addc_u32 s69, s5, 0\n"
"v_exp_f32 v92, v92\n"
"v_exp_f32 v93, v93\n"
"v_exp_f32 v94, v94\n"
"v_exp_f32 v95, v95\n"
"v_smfmac_f32_32x32x32_bf16 v[116:131], v[152:155], v[76:83], v62\n"
"s_add_u32 m0, s78, 122496\n"
"s_nop 0\n"
"global_load_lds_dwordx4 v1, s[68:69] offset:0 sc1\n"
"global_load_lds_dwordx4 v1, s[68:69] offset:1024 sc1\n"
"global_load_lds_dwordx4 v1, s[68:69] offset:2048 sc1\n"
"global_load_lds_dwordx4 v1, s[68:69] offset:3072 sc1\n"
"v_exp_f32 v96, v96\n"
"v_exp_f32 v97, v97\n"
"v_exp_f32 v98, v98\n"
"v_exp_f32 v99, v99\n"
"v_mfma_f32_32x32x8_bf16 v[68:83], v[48:49], v[42:43], 0\n"
"v_exp_f32 v100, v100\n"
"v_exp_f32 v101, v101\n"
"v_exp_f32 v102, v102\n"
"v_exp_f32 v103, v103\n"
"v_mfma_f32_32x32x16_bf16 v[68:83], v[44:47], v[36:39], v[68:83]\n"
"s_add_u32 s68, s68, 4096\n"
"s_addc_u32 s69, s69, 0\n"
"ds_read_b128 v[44:47], v60 offset:21696\n"
"v_exp_f32 v104, v104\n"
"v_exp_f32 v105, v105\n"
"v_exp_f32 v106, v106\n"
"v_exp_f32 v107, v107\n"
"v_smfmac_f32_32x32x32_bf16 v[132:147], v[148:151], v[84:91], v62\n"
"s_add_u32 m0, s78, 126592\n"
"ds_read_b128 v[148:151], v65 offset:20672\n"
"v_exp_f32 v108, v108\n"
"v_exp_f32 v109, v109\n"
"v_exp_f32 v110, v110\n"
"v_exp_f32 v111, v111\n"
"v_smfmac_f32_32x32x32_bf16 v[132:147], v[152:155], v[92:99], v62\n"
"s_waitcnt lgkmcnt(1)\n"
"ds_read_b128 v[152:155], v66 offset:20672\n"
"v_exp_f32 v112, v112\n"
"v_exp_f32 v113, v113\n"
"v_exp_f32 v114, v114\n"
"v_exp_f32 v115, v115\n"
"v_mfma_f32_32x32x8_bf16 v[84:99], v[50:51], v[40:41], 0\n"
"v_exp_f32 v68, v68\n"
"v_exp_f32 v69, v69\n"
"v_exp_f32 v70, v70\n"
"v_exp_f32 v71, v71\n"
"v_mfma_f32_32x32x16_bf16 v[84:99], v[44:47], v[32:35], v[84:99]\n"
"Lpad4_%=:\n"
"s_cbranch_execz Lpadback0_%=\n"
"v_exp_f32 v72, v72\n"
"v_exp_f32 v73, v73\n"
"v_exp_f32 v74, v74\n"
"v_exp_f32 v75, v75\n"
"v_smfmac_f32_32x32x32_bf16 v[116:131], v[52:55], v[100:107], v62\n"
"s_waitcnt lgkmcnt(0)\n"
"v_exp_f32 v76, v76\n"
"v_exp_f32 v77, v77\n"
"v_exp_f32 v78, v78\n"
"v_exp_f32 v79, v79\n"
"v_smfmac_f32_32x32x32_bf16 v[116:131], v[56:59], v[108:115], v62\n"
"global_load_lds_dwordx4 v1, s[68:69] offset:0 sc1\n"
"v_exp_f32 v80, v80\n"
"v_exp_f32 v81, v81\n"
"v_exp_f32 v82, v82\n"
"v_exp_f32 v83, v83\n"
"v_mfma_f32_32x32x8_bf16 v[100:115], v[50:51], v[42:43], 0\n"
"v_exp_f32 v84, v84\n"
"v_exp_f32 v85, v85\n"
"v_exp_f32 v86, v86\n"
"v_exp_f32 v87, v87\n"
"v_mfma_f32_32x32x16_bf16 v[100:115], v[44:47], v[36:39], v[100:115]\n"
"s_mul_i32 s49, s21, 2560\n"
"ds_read_b128 v[48:51], v61 offset:25840\n"
"ds_read_b128 v[44:47], v60 offset:25840\n"
"v_exp_f32 v88, v88\n"
"v_exp_f32 v89, v89\n"
"v_exp_f32 v90, v90\n"
"v_exp_f32 v91, v91\n"
"v_smfmac_f32_32x32x32_bf16 v[132:147], v[52:55], v[68:75], v62\n"
"s_add_u32 s49, s49, s30\n"
"ds_read_b128 v[52:55], v63 offset:25840\n"
"v_exp_f32 v92, v92\n"
"v_exp_f32 v93, v93\n"
"v_exp_f32 v94, v94\n"
"v_exp_f32 v95, v95\n"
"v_smfmac_f32_32x32x32_bf16 v[132:147], v[56:59], v[76:83], v62\n"
"s_waitcnt lgkmcnt(1)\n"
"ds_read_b128 v[56:59], v64 offset:25840\n"
"v_exp_f32 v96, v96\n"
"v_exp_f32 v97, v97\n"
"v_exp_f32 v98, v98\n"
"v_exp_f32 v99, v99\n"
"v_mfma_f32_32x32x8_bf16 v[68:83], v[48:49], v[40:41], 0\n"
"v_exp_f32 v100, v100\n"
"v_exp_f32 v101, v101\n"
"v_exp_f32 v102, v102\n"
"v_exp_f32 v103, v103\n"
"v_mfma_f32_32x32x16_bf16 v[68:83], v[44:47], v[32:35], v[68:83]\n"
"v_exp_f32 v104, v104\n"
"v_exp_f32 v105, v105\n"
"v_exp_f32 v106, v106\n"
"v_exp_f32 v107, v107\n"
"v_smfmac_f32_32x32x32_bf16 v[116:131], v[148:151], v[84:91], v62\n"
"s_waitcnt lgkmcnt(0)\n"
"v_exp_f32 v108, v108\n"
"v_exp_f32 v109, v109\n"
"v_exp_f32 v110, v110\n"
"v_exp_f32 v111, v111\n"
"v_smfmac_f32_32x32x32_bf16 v[116:131], v[152:155], v[92:99], v62\n"
"buffer_store_dwordx4 v[160:163], v1, s[12:15], s49 offen offset:0 sc1\n"
"v_exp_f32 v112, v112\n"
"v_exp_f32 v113, v113\n"
"v_exp_f32 v114, v114\n"
"v_exp_f32 v115, v115\n"
"v_mfma_f32_32x32x8_bf16 v[84:99], v[48:49], v[42:43], 0\n"
"v_exp_f32 v68, v68\n"
"v_exp_f32 v69, v69\n"
"v_exp_f32 v70, v70\n"
"v_exp_f32 v71, v71\n"
"v_mfma_f32_32x32x16_bf16 v[84:99], v[44:47], v[36:39], v[84:99]\n"
"ds_read_b128 v[44:47], v60 offset:26864\n"
"v_exp_f32 v72, v72\n"
"v_exp_f32 v73, v73\n"
"v_exp_f32 v74, v74\n"
"v_exp_f32 v75, v75\n"
"v_smfmac_f32_32x32x32_bf16 v[132:147], v[148:151], v[100:107], v62\n"
"ds_read_b128 v[148:151], v65 offset:25840\n"
"v_exp_f32 v76, v76\n"
"v_exp_f32 v77, v77\n"
"v_exp_f32 v78, v78\n"
"v_exp_f32 v79, v79\n"
"v_smfmac_f32_32x32x32_bf16 v[132:147], v[152:155], v[108:115], v62\n"
"s_waitcnt lgkmcnt(1)\n"
"ds_read_b128 v[152:155], v66 offset:25840\n"
"v_exp_f32 v80, v80\n"
"v_exp_f32 v81, v81\n"
"v_exp_f32 v82, v82\n"
"v_exp_f32 v83, v83\n"
"v_mfma_f32_32x32x8_bf16 v[100:115], v[50:51], v[40:41], 0\n"
"v_exp_f32 v84, v84\n"
"v_exp_f32 v85, v85\n"
"v_exp_f32 v86, v86\n"
"v_exp_f32 v87, v87\n"
"v_mfma_f32_32x32x16_bf16 v[100:115], v[44:47], v[32:35], v[100:115]\n"
"v_exp_f32 v88, v88\n"
"v_exp_f32 v89, v89\n"
"v_exp_f32 v90, v90\n"
"v_exp_f32 v91, v91\n"
"v_smfmac_f32_32x32x32_bf16 v[116:131], v[52:55], v[68:75], v62\n"
"s_waitcnt lgkmcnt(0)\n"
"v_exp_f32 v92, v92\n"
"v_exp_f32 v93, v93\n"
"v_exp_f32 v94, v94\n"
"v_exp_f32 v95, v95\n"
"v_smfmac_f32_32x32x32_bf16 v[116:131], v[56:59], v[76:83], v62\n"
"buffer_store_dwordx4 v[160:163], v1, s[12:15], s49 offen offset:1024 sc1\n"
"v_exp_f32 v96, v96\n"
"v_exp_f32 v97, v97\n"
"v_exp_f32 v98, v98\n"
"v_exp_f32 v99, v99\n"
"v_mfma_f32_32x32x8_bf16 v[68:83], v[50:51], v[42:43], 0\n"
"v_exp_f32 v100, v100\n"
"v_exp_f32 v101, v101\n"
"v_exp_f32 v102, v102\n"
"v_exp_f32 v103, v103\n"
"v_mfma_f32_32x32x16_bf16 v[68:83], v[44:47], v[36:39], v[68:83]\n"
"ds_read_b128 v[48:51], v61 offset:31008\n"
"ds_read_b128 v[44:47], v60 offset:31008\n"
"v_exp_f32 v104, v104\n"
"v_exp_f32 v105, v105\n"
"v_exp_f32 v106, v106\n"
"v_exp_f32 v107, v107\n"
"v_smfmac_f32_32x32x32_bf16 v[132:147], v[52:55], v[84:91], v62\n"
"ds_read_b128 v[52:55], v63 offset:31008\n"
"v_exp_f32 v108, v108\n"
"v_exp_f32 v109, v109\n"
"v_exp_f32 v110, v110\n"
"v_exp_f32 v111, v111\n"
"v_smfmac_f32_32x32x32_bf16 v[132:147], v[56:59], v[92:99], v62\n"
"s_waitcnt lgkmcnt(1)\n"
"ds_read_b128 v[56:59], v64 offset:31008\n"
"v_exp_f32 v112, v112\n"
"v_exp_f32 v113, v113\n"
"v_exp_f32 v114, v114\n"
"v_exp_f32 v115, v115\n"
"v_mfma_f32_32x32x8_bf16 v[84:99], v[48:49], v[40:41], 0\n"
"v_exp_f32 v68, v68\n"
"v_exp_f32 v69, v69\n"
"v_exp_f32 v70, v70\n"
"v_exp_f32 v71, v71\n"
"v_mfma_f32_32x32x16_bf16 v[84:99], v[44:47], v[32:35], v[84:99]\n"
"v_exp_f32 v72, v72\n"
"v_exp_f32 v73, v73\n"
"v_exp_f32 v74, v74\n"
"v_exp_f32 v75, v75\n"
"v_smfmac_f32_32x32x32_bf16 v[116:131], v[148:151], v[100:107], v62\n"
"s_waitcnt lgkmcnt(0)\n"
"v_exp_f32 v76, v76\n"
"v_exp_f32 v77, v77\n"
"v_exp_f32 v78, v78\n"
"v_exp_f32 v79, v79\n"
"v_smfmac_f32_32x32x32_bf16 v[116:131], v[152:155], v[108:115], v62\n"
"buffer_load_dwordx2 v[166:167], v67, s[12:15], s53 offen sc1\n"
"v_exp_f32 v80, v80\n"
"v_exp_f32 v81, v81\n"
"v_exp_f32 v82, v82\n"
"v_exp_f32 v83, v83\n"
"v_mfma_f32_32x32x8_bf16 v[100:115], v[48:49], v[42:43], 0\n"
"v_exp_f32 v84, v84\n"
"v_exp_f32 v85, v85\n"
"v_exp_f32 v86, v86\n"
"v_exp_f32 v87, v87\n"
"v_mfma_f32_32x32x16_bf16 v[100:115], v[44:47], v[36:39], v[100:115]\n"
"ds_read_b128 v[44:47], v60 offset:32032\n"
"v_exp_f32 v88, v88\n"
"v_exp_f32 v89, v89\n"
"v_exp_f32 v90, v90\n"
"v_exp_f32 v91, v91\n"
"v_smfmac_f32_32x32x32_bf16 v[132:147], v[148:151], v[68:75], v62\n"
"ds_read_b128 v[148:151], v65 offset:31008\n"
"v_exp_f32 v92, v92\n"
"v_exp_f32 v93, v93\n"
"v_exp_f32 v94, v94\n"
"v_exp_f32 v95, v95\n"
"v_smfmac_f32_32x32x32_bf16 v[132:147], v[152:155], v[76:83], v62\n"
"s_waitcnt lgkmcnt(1)\n"
"ds_read_b128 v[152:155], v66 offset:31008\n"
"v_exp_f32 v96, v96\n"
"v_exp_f32 v97, v97\n"
"v_exp_f32 v98, v98\n"
"v_exp_f32 v99, v99\n"
"v_mfma_f32_32x32x8_bf16 v[68:83], v[50:51], v[40:41], 0\n"
"v_exp_f32 v100, v100\n"
"v_exp_f32 v101, v101\n"
"v_exp_f32 v102, v102\n"
"v_exp_f32 v103, v103\n"
"v_mfma_f32_32x32x16_bf16 v[68:83], v[44:47], v[32:35], v[68:83]\n"
"v_exp_f32 v104, v104\n"
"v_exp_f32 v105, v105\n"
"v_exp_f32 v106, v106\n"
"v_exp_f32 v107, v107\n"
"v_smfmac_f32_32x32x32_bf16 v[116:131], v[52:55], v[84:91], v62\n"
"s_waitcnt lgkmcnt(0)\n"
"v_exp_f32 v108, v108\n"
"v_exp_f32 v109, v109\n"
"v_exp_f32 v110, v110\n"
"v_exp_f32 v111, v111\n"
"v_smfmac_f32_32x32x32_bf16 v[116:131], v[56:59], v[92:99], v62\n"
"buffer_store_dwordx4 v[160:163], v5, s[12:15], s30 offen sc1\n"
"v_exp_f32 v112, v112\n"
"v_exp_f32 v113, v113\n"
"v_exp_f32 v114, v114\n"
"v_exp_f32 v115, v115\n"
"v_mfma_f32_32x32x8_bf16 v[84:99], v[50:51], v[42:43], 0\n"
"v_exp_f32 v68, v68\n"
"v_exp_f32 v69, v69\n"
"v_exp_f32 v70, v70\n"
"v_exp_f32 v71, v71\n"
"v_mfma_f32_32x32x16_bf16 v[84:99], v[44:47], v[36:39], v[84:99]\n"
"ds_read_b128 v[48:51], v61 offset:36176\n"
"ds_read_b128 v[44:47], v60 offset:36176\n"
"v_exp_f32 v72, v72\n"
"v_exp_f32 v73, v73\n"
"v_exp_f32 v74, v74\n"
"v_exp_f32 v75, v75\n"
"v_smfmac_f32_32x32x32_bf16 v[132:147], v[52:55], v[100:107], v62\n"
"ds_read_b128 v[52:55], v63 offset:36176\n"
"v_exp_f32 v76, v76\n"
"v_exp_f32 v77, v77\n"
"v_exp_f32 v78, v78\n"
"v_exp_f32 v79, v79\n"
"v_smfmac_f32_32x32x32_bf16 v[132:147], v[56:59], v[108:115], v62\n"
"s_waitcnt lgkmcnt(1)\n"
"ds_read_b128 v[56:59], v64 offset:36176\n"
"v_exp_f32 v80, v80\n"
"v_exp_f32 v81, v81\n"
"v_exp_f32 v82, v82\n"
"v_exp_f32 v83, v83\n"
"v_mfma_f32_32x32x8_bf16 v[100:115], v[48:49], v[40:41], 0\n"
"v_exp_f32 v84, v84\n"
"v_exp_f32 v85, v85\n"
"v_exp_f32 v86, v86\n"
"v_exp_f32 v87, v87\n"
"v_mfma_f32_32x32x16_bf16 v[100:115], v[44:47], v[32:35], v[100:115]\n"
"v_exp_f32 v88, v88\n"
"v_exp_f32 v89, v89\n"
"v_exp_f32 v90, v90\n"
"v_exp_f32 v91, v91\n"
"v_smfmac_f32_32x32x32_bf16 v[116:131], v[148:151], v[68:75], v62\n"
"s_waitcnt lgkmcnt(0)\n"
"v_exp_f32 v92, v92\n"
"v_exp_f32 v93, v93\n"
"v_exp_f32 v94, v94\n"
"v_exp_f32 v95, v95\n"
"v_smfmac_f32_32x32x32_bf16 v[116:131], v[152:155], v[76:83], v62\n"
"v_exp_f32 v96, v96\n"
"v_exp_f32 v97, v97\n"
"v_exp_f32 v98, v98\n"
"v_exp_f32 v99, v99\n"
"v_mfma_f32_32x32x8_bf16 v[68:83], v[48:49], v[42:43], 0\n"
"v_exp_f32 v100, v100\n"
"v_exp_f32 v101, v101\n"
"v_exp_f32 v102, v102\n"
"v_exp_f32 v103, v103\n"
"v_mfma_f32_32x32x16_bf16 v[68:83], v[44:47], v[36:39], v[68:83]\n"
"ds_read_b128 v[44:47], v60 offset:37200\n"
"v_exp_f32 v104, v104\n"
"v_exp_f32 v105, v105\n"
"v_exp_f32 v106, v106\n"
"v_exp_f32 v107, v107\n"
"v_smfmac_f32_32x32x32_bf16 v[132:147], v[148:151], v[84:91], v62\n"
"ds_read_b128 v[148:151], v65 offset:36176\n"
"v_exp_f32 v108, v108\n"
"v_exp_f32 v109, v109\n"
"v_exp_f32 v110, v110\n"
"v_exp_f32 v111, v111\n"
"v_smfmac_f32_32x32x32_bf16 v[132:147], v[152:155], v[92:99], v62\n"
"s_waitcnt lgkmcnt(1)\n"
"ds_read_b128 v[152:155], v66 offset:36176\n"
"v_exp_f32 v112, v112\n"
"v_exp_f32 v113, v113\n"
"v_exp_f32 v114, v114\n"
"v_exp_f32 v115, v115\n"
"v_mfma_f32_32x32x8_bf16 v[84:99], v[50:51], v[40:41], 0\n"
"v_exp_f32 v68, v68\n"
"v_exp_f32 v69, v69\n"
"v_exp_f32 v70, v70\n"
"v_exp_f32 v71, v71\n"
"v_mfma_f32_32x32x16_bf16 v[84:99], v[44:47], v[32:35], v[84:99]\n"
"s_waitcnt vmcnt(1)\n"
"v_cmp_ne_u64 vcc, s[40:41], v[166:167]\n"
"v_exp_f32 v72, v72\n"
"v_exp_f32 v73, v73\n"
"v_exp_f32 v74, v74\n"
"v_exp_f32 v75, v75\n"
"v_smfmac_f32_32x32x32_bf16 v[116:131], v[52:55], v[100:107], v62\n"
"s_cbranch_vccnz Lws37_%=\n"
"Lwb38_%=:\n"
"s_waitcnt lgkmcnt(0)\n"
"s_barrier\n"
"v_exp_f32 v76, v76\n"
"v_exp_f32 v77, v77\n"
"v_exp_f32 v78, v78\n"
"v_exp_f32 v79, v79\n"
"v_smfmac_f32_32x32x32_bf16 v[116:131], v[56:59], v[108:115], v62\n"
"v_exp_f32 v80, v80\n"
"v_exp_f32 v81, v81\n"
"v_exp_f32 v82, v82\n"
"v_exp_f32 v83, v83\n"
"v_mfma_f32_32x32x8_bf16 v[100:115], v[50:51], v[42:43], 0\n"
"v_exp_f32 v84, v84\n"
"v_exp_f32 v85, v85\n"
"v_exp_f32 v86, v86\n"
"v_exp_f32 v87, v87\n"
"v_mfma_f32_32x32x16_bf16 v[100:115], v[44:47], v[36:39], v[100:115]\n"
"s_bfe_u32 s45, s9, 0x80000\n"
"s_cbranch_scc1 Lcs24_%=\n"
"Lcb25_%=:\n"
"ds_read_b128 v[48:51], v61 offset:0\n"
"ds_read_b128 v[44:47], v60 offset:0\n"
"v_exp_f32 v88, v88\n"
"v_exp_f32 v89, v89\n"
"v_exp_f32 v90, v90\n"
"v_exp_f32 v91, v91\n"
"v_smfmac_f32_32x32x32_bf16 v[132:147], v[52:55], v[68:75], v62\n"
"s_add_u32 s63, s63, 20480\n"
"s_cmp_ge_u32 s63, 163840\n"
"s_cselect_b32 s46, 163840, 0\n"
"ds_read_b128 v[52:55], v63 offset:0\n"
"v_exp_f32 v92, v92\n"
"v_exp_f32 v93, v93\n"
"v_exp_f32 v94, v94\n"
"v_exp_f32 v95, v95\n"
"v_smfmac_f32_32x32x32_bf16 v[132:147], v[56:59], v[76:83], v62\n"
"s_waitcnt lgkmcnt(1)\n"
"s_sub_u32 s63, s63, s46\n"
"ds_read_b128 v[56:59], v64 offset:0\n"
"v_exp_f32 v96, v96\n"
"v_exp_f32 v97, v97\n"
"v_exp_f32 v98, v98\n"
"v_exp_f32 v99, v99\n"
"v_mfma_f32_32x32x8_bf16 v[68:83], v[48:49], v[40:41], 0\n"
"v_exp_f32 v100, v100\n"
"v_exp_f32 v101, v101\n"
"v_exp_f32 v102, v102\n"
"v_exp_f32 v103, v103\n"
"v_mfma_f32_32x32x16_bf16 v[68:83], v[44:47], v[32:35], v[68:83]\n"
"s_add_u32 s46, s63, s28\n"
"v_exp_f32 v104, v104\n"
"v_exp_f32 v105, v105\n"
"v_exp_f32 v106, v106\n"
"v_exp_f32 v107, v107\n"
"v_smfmac_f32_32x32x32_bf16 v[116:131], v[148:151], v[84:91], v62\n"
"s_waitcnt lgkmcnt(0)\n"
"s_add_u32 s68, s4, s46\n"
"s_addc_u32 s69, s5, 0\n"
"v_exp_f32 v108, v108\n"
"v_exp_f32 v109, v109\n"
"v_exp_f32 v110, v110\n"
"v_exp_f32 v111, v111\n"
"v_smfmac_f32_32x32x32_bf16 v[116:131], v[152:155], v[92:99], v62\n"
"s_add_u32 m0, s78, 143168\n"
"s_nop 0\n"
"global_load_lds_dwordx4 v1, s[68:69] offset:0 sc1\n"
"global_load_lds_dwordx4 v1, s[68:69] offset:1024 sc1\n"
"global_load_lds_dwordx4 v1, s[68:69] offset:2048 sc1\n"
"global_load_lds_dwordx4 v1, s[68:69] offset:3072 sc1\n"
"v_exp_f32 v112, v112\n"
"v_exp_f32 v113, v113\n"
"v_exp_f32 v114, v114\n"
"v_exp_f32 v115, v115\n"
"v_mfma_f32_32x32x8_bf16 v[84:99], v[48:49], v[42:43], 0\n"
"v_exp_f32 v68, v68\n"
"v_exp_f32 v69, v69\n"
"v_exp_f32 v70, v70\n"
"v_exp_f32 v71, v71\n"
"v_mfma_f32_32x32x16_bf16 v[84:99], v[44:47], v[36:39], v[84:99]\n"
"s_add_u32 s68, s68, 4096\n"
"s_addc_u32 s69, s69, 0\n"
"ds_read_b128 v[44:47], v60 offset:1024\n"
"v_exp_f32 v72, v72\n"
"v_exp_f32 v73, v73\n"
"v_exp_f32 v74, v74\n"
"v_exp_f32 v75, v75\n"
"v_smfmac_f32_32x32x32_bf16 v[132:147], v[148:151], v[100:107], v62\n"
"s_add_u32 m0, s78, 147264\n"
"ds_read_b128 v[148:151], v65 offset:0\n"
"v_exp_f32 v76, v76\n"
"v_exp_f32 v77, v77\n"
"v_exp_f32 v78, v78\n"
"v_exp_f32 v79, v79\n"
"v_smfmac_f32_32x32x32_bf16 v[132:147], v[152:155], v[108:115], v62\n"
"s_waitcnt lgkmcnt(1)\n"
"ds_read_b128 v[152:155], v66 offset:0\n"
"v_exp_f32 v80, v80\n"
"v_exp_f32 v81, v81\n"
"v_exp_f32 v82, v82\n"
"v_exp_f32 v83, v83\n"
"v_mfma_f32_32x32x8_bf16 v[100:115], v[50:51], v[40:41], 0\n"
"v_exp_f32 v84, v84\n"
"v_exp_f32 v85, v85\n"
"v_exp_f32 v86, v86\n"
"v_exp_f32 v87, v87\n"
"v_mfma_f32_32x32x16_bf16 v[100:115], v[44:47], v[32:35], v[100:115]\n"
"v_exp_f32 v88, v88\n"
"v_exp_f32 v89, v89\n"
"v_exp_f32 v90, v90\n"
"v_exp_f32 v91, v91\n"
"v_smfmac_f32_32x32x32_bf16 v[116:131], v[52:55], v[68:75], v62\n"
"s_waitcnt lgkmcnt(0)\n"
"v_exp_f32 v92, v92\n"
"v_exp_f32 v93, v93\n"
"v_exp_f32 v94, v94\n"
"v_exp_f32 v95, v95\n"
"v_smfmac_f32_32x32x32_bf16 v[116:131], v[56:59], v[76:83], v62\n"
"global_load_lds_dwordx4 v1, s[68:69] offset:0 sc1\n"
"global_load_dwordx4 a[168:171], v1, s[64:65] offset:-4096 sc1\n"
"global_load_dwordx4 a[172:175], v1, s[64:65] offset:-3072 sc1\n"
"global_load_dwordx4 a[176:179], v1, s[64:65] offset:-2048 sc1\n"
"v_exp_f32 v96, v96\n"
"v_exp_f32 v97, v97\n"
"v_exp_f32 v98, v98\n"
"v_exp_f32 v99, v99\n"
"v_mfma_f32_32x32x8_bf16 v[68:83], v[50:51], v[42:43], 0\n"
"v_exp_f32 v100, v100\n"
"v_exp_f32 v101, v101\n"
"v_exp_f32 v102, v102\n"
"v_exp_f32 v103, v103\n"
"v_mfma_f32_32x32x16_bf16 v[68:83], v[44:47], v[36:39], v[68:83]\n"
"s_add_u32 m0, s79, 0\n"
"ds_read_b128 v[48:51], v61 offset:5168\n"
"ds_read_b128 v[44:47], v60 offset:5168\n"
"v_exp_f32 v104, v104\n"
"v_exp_f32 v105, v105\n"
"v_exp_f32 v106, v106\n"
"v_exp_f32 v107, v107\n"
"v_smfmac_f32_32x32x32_bf16 v[132:147], v[52:55], v[84:91], v62\n"
"ds_read_b128 v[52:55], v63 offset:5168\n"
"v_exp_f32 v108, v108\n"
"v_exp_f32 v109, v109\n"
"v_exp_f32 v110, v110\n"
"v_exp_f32 v111, v111\n"
"v_smfmac_f32_32x32x32_bf16 v[132:147], v[56:59], v[92:99], v62\n"
"s_waitcnt lgkmcnt(1)\n"
"ds_read_b128 v[56:59], v64 offset:5168\n"
"v_exp_f32 v112, v112\n"
"v_exp_f32 v113, v113\n"
"v_exp_f32 v114, v114\n"
"v_exp_f32 v115, v115\n"
"v_mfma_f32_32x32x8_bf16 v[84:99], v[48:49], v[40:41], 0\n"
"v_exp_f32 v68, v68\n"
"v_exp_f32 v69, v69\n"
"v_exp_f32 v70, v70\n"
"v_exp_f32 v71, v71\n"
"v_mfma_f32_32x32x16_bf16 v[84:99], v[44:47], v[32:35], v[84:99]\n"
"v_exp_f32 v72, v72\n"
"v_exp_f32 v73, v73\n"
"v_exp_f32 v74, v74\n"
"v_exp_f32 v75, v75\n"
"v_smfmac_f32_32x32x32_bf16 v[116:131], v[148:151], v[100:107], v62\n"
"s_waitcnt lgkmcnt(0)\n"
"v_exp_f32 v76, v76\n"
"v_exp_f32 v77, v77\n"
"v_exp_f32 v78, v78\n"
"v_exp_f32 v79, v79\n"
"v_smfmac_f32_32x32x32_bf16 v[116:131], v[152:155], v[108:115], v62\n"
"global_load_lds_dwordx4 v1, s[66:67] offset:0 sc1\n"
"global_load_lds_dwordx4 v1, s[66:67] offset:1024 sc1\n"
"global_load_lds_dwordx4 v1, s[66:67] offset:2048 sc1\n"
"global_load_lds_dwordx4 v1, s[66:67] offset:3072 sc1\n"
"v_exp_f32 v80, v80\n"
"v_exp_f32 v81, v81\n"
"v_exp_f32 v82, v82\n"
"v_exp_f32 v83, v83\n"
"v_mfma_f32_32x32x8_bf16 v[100:115], v[48:49], v[42:43], 0\n"
"v_exp_f32 v84, v84\n"
"v_exp_f32 v85, v85\n"
"v_exp_f32 v86, v86\n"
"v_exp_f32 v87, v87\n"
"v_mfma_f32_32x32x16_bf16 v[100:115], v[44:47], v[36:39], v[100:115]\n"
"s_add_u32 s66, s66, 4096\n"
"s_addc_u32 s67, s67, 0\n"
"ds_read_b128 v[44:47], v60 offset:6192\n"
"v_exp_f32 v88, v88\n"
"v_exp_f32 v89, v89\n"
"v_exp_f32 v90, v90\n"
"v_exp_f32 v91, v91\n"
"v_smfmac_f32_32x32x32_bf16 v[132:147], v[148:151], v[68:75], v62\n"
"s_add_u32 m0, s79, 4096\n"
"ds_read_b128 v[148:151], v65 offset:5168\n"
"v_exp_f32 v92, v92\n"
"v_exp_f32 v93, v93\n"
"v_exp_f32 v94, v94\n"
"v_exp_f32 v95, v95\n"
"v_smfmac_f32_32x32x32_bf16 v[132:147], v[152:155], v[76:83], v62\n"
"s_waitcnt lgkmcnt(1)\n"
"ds_read_b128 v[152:155], v66 offset:5168\n"
"v_exp_f32 v96, v96\n"
"v_exp_f32 v97, v97\n"
"v_exp_f32 v98, v98\n"
"v_exp_f32 v99, v99\n"
"v_mfma_f32_32x32x8_bf16 v[68:83], v[50:51], v[40:41], 0\n"
"v_exp_f32 v100, v100\n"
"v_exp_f32 v101, v101\n"
"v_exp_f32 v102, v102\n"
"v_exp_f32 v103, v103\n"
"v_mfma_f32_32x32x16_bf16 v[68:83], v[44:47], v[32:35], v[68:83]\n"
"v_exp_f32 v104, v104\n"
"v_exp_f32 v105, v105\n"
"v_exp_f32 v106, v106\n"
"v_exp_f32 v107, v107\n"
"v_smfmac_f32_32x32x32_bf16 v[116:131], v[52:55], v[84:91], v62\n"
"s_waitcnt lgkmcnt(0)\n"
"v_exp_f32 v108, v108\n"
"v_exp_f32 v109, v109\n"
"v_exp_f32 v110, v110\n"
"v_exp_f32 v111, v111\n"
"v_smfmac_f32_32x32x32_bf16 v[116:131], v[56:59], v[92:99], v62\n"
"global_load_lds_dwordx4 v1, s[66:67] offset:0 sc1\n"
"global_load_lds_dwordx4 v1, s[66:67] offset:1024 sc1\n"
"global_load_lds_dwordx4 v1, s[66:67] offset:2048 sc1\n"
"global_load_lds_dwordx4 v1, s[66:67] offset:3072 sc1\n"
"v_exp_f32 v112, v112\n"
"v_exp_f32 v113, v113\n"
"v_exp_f32 v114, v114\n"
"v_exp_f32 v115, v115\n"
"v_mfma_f32_32x32x8_bf16 v[84:99], v[50:51], v[42:43], 0\n"
"v_exp_f32 v68, v68\n"
"v_exp_f32 v69, v69\n"
"v_exp_f32 v70, v70\n"
"v_exp_f32 v71, v71\n"
"v_mfma_f32_32x32x16_bf16 v[84:99], v[44:47], v[36:39], v[84:99]\n"
"s_add_u32 s66, s66, 4096\n"
"s_addc_u32 s67, s67, 0\n"
"ds_read_b128 v[48:51], v61 offset:10336\n"
"ds_read_b128 v[44:47], v60 offset:10336\n"
"v_exp_f32 v72, v72\n"
"v_exp_f32 v73, v73\n"
"v_exp_f32 v74, v74\n"
"v_exp_f32 v75, v75\n"
"v_smfmac_f32_32x32x32_bf16 v[132:147], v[52:55], v[100:107], v62\n"
"s_add_u32 m0, s79, 8192\n"
"ds_read_b128 v[52:55], v63 offset:10336\n"
"v_exp_f32 v76, v76\n"
"v_exp_f32 v77, v77\n"
"v_exp_f32 v78, v78\n"
"v_exp_f32 v79, v79\n"
"v_smfmac_f32_32x32x32_bf16 v[132:147], v[56:59], v[108:115], v62\n"
"s_waitcnt lgkmcnt(1)\n"
"ds_read_b128 v[56:59], v64 offset:10336\n"
"v_exp_f32 v80, v80\n"
"v_exp_f32 v81, v81\n"
"v_exp_f32 v82, v82\n"
"v_exp_f32 v83, v83\n"
"v_mfma_f32_32x32x8_bf16 v[100:115], v[48:49], v[40:41], 0\n"
"v_exp_f32 v84, v84\n"
"v_exp_f32 v85, v85\n"
"v_exp_f32 v86, v86\n"
"v_exp_f32 v87, v87\n"
"v_mfma_f32_32x32x16_bf16 v[100:115], v[44:47], v[32:35], v[100:115]\n"
"v_exp_f32 v88, v88\n"
"v_exp_f32 v89, v89\n"
"v_exp_f32 v90, v90\n"
"v_exp_f32 v91, v91\n"
"v_smfmac_f32_32x32x32_bf16 v[116:131], v[148:151], v[68:75], v62\n"
"s_waitcnt lgkmcnt(0)\n"
"v_exp_f32 v92, v92\n"
"v_exp_f32 v93, v93\n"
"v_exp_f32 v94, v94\n"
"v_exp_f32 v95, v95\n"
"v_smfmac_f32_32x32x32_bf16 v[116:131], v[152:155], v[76:83], v62\n"
"global_load_lds_dwordx4 v1, s[66:67] offset:0 sc1\n"
"global_load_lds_dwordx4 v1, s[66:67] offset:1024 sc1\n"
"global_load_lds_dwordx4 v1, s[66:67] offset:2048 sc1\n"
"global_load_lds_dwordx4 v1, s[66:67] offset:3072 sc1\n"
"v_exp_f32 v96, v96\n"
"v_exp_f32 v97, v97\n"
"v_exp_f32 v98, v98\n"
"v_exp_f32 v99, v99\n"
"v_mfma_f32_32x32x8_bf16 v[68:83], v[48:49], v[42:43], 0\n"
"v_exp_f32 v100, v100\n"
"v_exp_f32 v101, v101\n"
"v_exp_f32 v102, v102\n"
"v_exp_f32 v103, v103\n"
"v_mfma_f32_32x32x16_bf16 v[68:83], v[44:47], v[36:39], v[68:83]\n"
"s_add_u32 s66, s66, 4096\n"
"s_addc_u32 s67, s67, 0\n"
"ds_read_b128 v[44:47], v60 offset:11360\n"
"v_exp_f32 v104, v104\n"
"v_exp_f32 v105, v105\n"
"v_exp_f32 v106, v106\n"
"v_exp_f32 v107, v107\n"
"v_smfmac_f32_32x32x32_bf16 v[132:147], v[148:151], v[84:91], v62\n"
"s_add_u32 m0, s79, 12288\n"
"ds_read_b128 v[148:151], v65 offset:10336\n"
"v_exp_f32 v108, v108\n"
"v_exp_f32 v109, v109\n"
"v_exp_f32 v110, v110\n"
"v_exp_f32 v111, v111\n"
"v_smfmac_f32_32x32x32_bf16 v[132:147], v[152:155], v[92:99], v62\n"
"s_waitcnt lgkmcnt(1)\n"
"ds_read_b128 v[152:155], v66 offset:10336\n"
"v_exp_f32 v112, v112\n"
"v_exp_f32 v113, v113\n"
"v_exp_f32 v114, v114\n"
"v_exp_f32 v115, v115\n"
"v_mfma_f32_32x32x8_bf16 v[84:99], v[50:51], v[40:41], 0\n"
"v_exp_f32 v68, v68\n"
"v_exp_f32 v69, v69\n"
"v_exp_f32 v70, v70\n"
"v_exp_f32 v71, v71\n"
"v_mfma_f32_32x32x16_bf16 v[84:99], v[44:47], v[32:35], v[84:99]\n"
"v_exp_f32 v72, v72\n"
"v_exp_f32 v73, v73\n"
"v_exp_f32 v74, v74\n"
"v_exp_f32 v75, v75\n"
"v_smfmac_f32_32x32x32_bf16 v[116:131], v[52:55], v[100:107], v62\n"
"s_waitcnt lgkmcnt(0)\n"
"v_exp_f32 v76, v76\n"
"v_exp_f32 v77, v77\n"
"v_exp_f32 v78, v78\n"
"v_exp_f32 v79, v79\n"
"v_smfmac_f32_32x32x32_bf16 v[116:131], v[56:59], v[108:115], v62\n"
"global_load_lds_dwordx4 v1, s[66:67] offset:0 sc1\n"
"global_load_lds_dwordx4 v1, s[66:67] offset:1024 sc1\n"
"global_load_lds_dwordx4 v1, s[66:67] offset:2048 sc1\n"
"global_load_lds_dwordx4 v1, s[66:67] offset:3072 sc1\n"
"v_exp_f32 v80, v80\n"
"v_exp_f32 v81, v81\n"
"v_exp_f32 v82, v82\n"
"v_exp_f32 v83, v83\n"
"v_mfma_f32_32x32x8_bf16 v[100:115], v[50:51], v[42:43], 0\n"
"v_exp_f32 v84, v84\n"
"v_exp_f32 v85, v85\n"
"v_exp_f32 v86, v86\n"
"v_exp_f32 v87, v87\n"
"v_mfma_f32_32x32x16_bf16 v[100:115], v[44:47], v[36:39], v[100:115]\n"
"s_add_u32 s66, s66, 4096\n"
"s_addc_u32 s67, s67, 0\n"
"ds_read_b128 v[48:51], v61 offset:15504\n"
"ds_read_b128 v[44:47], v60 offset:15504\n"
"v_exp_f32 v88, v88\n"
"v_exp_f32 v89, v89\n"
"v_exp_f32 v90, v90\n"
"v_exp_f32 v91, v91\n"
"v_smfmac_f32_32x32x32_bf16 v[132:147], v[52:55], v[68:75], v62\n"
"s_add_u32 m0, s79, 16384\n"
"ds_read_b128 v[52:55], v63 offset:15504\n"
"v_exp_f32 v92, v92\n"
"v_exp_f32 v93, v93\n"
"v_exp_f32 v94, v94\n"
"v_exp_f32 v95, v95\n"
"v_smfmac_f32_32x32x32_bf16 v[132:147], v[56:59], v[76:83], v62\n"
"s_waitcnt lgkmcnt(1)\n"
"ds_read_b128 v[56:59], v64 offset:15504\n"
"v_exp_f32 v96, v96\n"
"v_exp_f32 v97, v97\n"
"v_exp_f32 v98, v98\n"
"v_exp_f32 v99, v99\n"
"v_mfma_f32_32x32x8_bf16 v[68:83], v[48:49], v[40:41], 0\n"
"v_exp_f32 v100, v100\n"
"v_exp_f32 v101, v101\n"
"v_exp_f32 v102, v102\n"
"v_exp_f32 v103, v103\n"
"v_mfma_f32_32x32x16_bf16 v[68:83], v[44:47], v[32:35], v[68:83]\n"
"v_exp_f32 v104, v104\n"
"v_exp_f32 v105, v105\n"
"v_exp_f32 v106, v106\n"
"v_exp_f32 v107, v107\n"
"v_smfmac_f32_32x32x32_bf16 v[116:131], v[148:151], v[84:91], v62\n"
"s_waitcnt lgkmcnt(0)\n"
"v_exp_f32 v108, v108\n"
"v_exp_f32 v109, v109\n"
"v_exp_f32 v110, v110\n"
"v_exp_f32 v111, v111\n"
"v_smfmac_f32_32x32x32_bf16 v[116:131], v[152:155], v[92:99], v62\n"
"global_load_lds_dwordx4 v1, s[66:67] offset:0 sc1\n"
"global_load_lds_dwordx4 v1, s[66:67] offset:1024 sc1\n"
"global_load_lds_dwordx4 v1, s[66:67] offset:2048 sc1\n"
"global_load_lds_dwordx4 v1, s[66:67] offset:3072 sc1\n"
"v_exp_f32 v112, v112\n"
"v_exp_f32 v113, v113\n"
"v_exp_f32 v114, v114\n"
"v_exp_f32 v115, v115\n"
"v_mfma_f32_32x32x8_bf16 v[84:99], v[48:49], v[42:43], 0\n"
"v_exp_f32 v68, v68\n"
"v_exp_f32 v69, v69\n"
"v_exp_f32 v70, v70\n"
"v_exp_f32 v71, v71\n"
"v_mfma_f32_32x32x16_bf16 v[84:99], v[44:47], v[36:39], v[84:99]\n"
"s_add_u32 s66, s66, 4096\n"
"s_addc_u32 s67, s67, 0\n"
"ds_read_b128 v[44:47], v60 offset:16528\n"
"v_exp_f32 v72, v72\n"
"v_exp_f32 v73, v73\n"
"v_exp_f32 v74, v74\n"
"v_exp_f32 v75, v75\n"
"v_smfmac_f32_32x32x32_bf16 v[132:147], v[148:151], v[100:107], v62\n"
"s_add_u32 m0, s79, 20480\n"
"ds_read_b128 v[148:151], v65 offset:15504\n"
"v_exp_f32 v76, v76\n"
"v_exp_f32 v77, v77\n"
"v_exp_f32 v78, v78\n"
"v_exp_f32 v79, v79\n"
"v_smfmac_f32_32x32x32_bf16 v[132:147], v[152:155], v[108:115], v62\n"
"s_waitcnt lgkmcnt(1)\n"
"ds_read_b128 v[152:155], v66 offset:15504\n"
"v_exp_f32 v80, v80\n"
"v_exp_f32 v81, v81\n"
"v_exp_f32 v82, v82\n"
"v_exp_f32 v83, v83\n"
"v_mfma_f32_32x32x8_bf16 v[100:115], v[50:51], v[40:41], 0\n"
"v_exp_f32 v84, v84\n"
"v_exp_f32 v85, v85\n"
"v_exp_f32 v86, v86\n"
"v_exp_f32 v87, v87\n"
"v_mfma_f32_32x32x16_bf16 v[100:115], v[44:47], v[32:35], v[100:115]\n"
"v_exp_f32 v88, v88\n"
"v_exp_f32 v89, v89\n"
"v_exp_f32 v90, v90\n"
"v_exp_f32 v91, v91\n"
"v_smfmac_f32_32x32x32_bf16 v[116:131], v[52:55], v[68:75], v62\n"
"s_waitcnt vmcnt(23) lgkmcnt(0)\n"
"s_barrier\n"
"v_exp_f32 v92, v92\n"
"v_exp_f32 v93, v93\n"
"v_exp_f32 v94, v94\n"
"v_exp_f32 v95, v95\n"
"v_smfmac_f32_32x32x32_bf16 v[116:131], v[56:59], v[76:83], v62\n"
"global_load_lds_dwordx4 v1, s[66:67] offset:0 sc1\n"
"global_load_lds_dwordx4 v1, s[66:67] offset:1024 sc1\n"
"global_load_lds_dwordx4 v1, s[66:67] offset:2048 sc1\n"
"global_load_lds_dwordx4 v1, s[66:67] offset:3072 sc1\n"
"v_exp_f32 v96, v96\n"
"v_exp_f32 v97, v97\n"
"v_exp_f32 v98, v98\n"
"v_exp_f32 v99, v99\n"
"v_mfma_f32_32x32x8_bf16 v[68:83], v[50:51], v[42:43], 0\n"
"v_exp_f32 v100, v100\n"
"v_exp_f32 v101, v101\n"
"v_exp_f32 v102, v102\n"
"v_exp_f32 v103, v103\n"
"v_mfma_f32_32x32x16_bf16 v[68:83], v[44:47], v[36:39], v[68:83]\n"
"s_bfe_u32 s45, s9, 0x80008\n"
"s_cbranch_scc1 Lcs26_%=\n"
"Lcb27_%=:\n"
"ds_read_b128 v[48:51], v61 offset:20672\n"
"ds_read_b128 v[44:47], v60 offset:20672\n"
"v_exp_f32 v104, v104\n"
"v_exp_f32 v105, v105\n"
"v_exp_f32 v106, v106\n"
"v_exp_f32 v107, v107\n"
"v_smfmac_f32_32x32x32_bf16 v[132:147], v[52:55], v[84:91], v62\n"
"s_add_u32 s63, s63, 20480\n"
"s_cmp_ge_u32 s63, 163840\n"
"s_cselect_b32 s46, 163840, 0\n"
"ds_read_b128 v[52:55], v63 offset:20672\n"
"v_exp_f32 v108, v108\n"
"v_exp_f32 v109, v109\n"
"v_exp_f32 v110, v110\n"
"v_exp_f32 v111, v111\n"
"v_smfmac_f32_32x32x32_bf16 v[132:147], v[56:59], v[92:99], v62\n"
"s_waitcnt lgkmcnt(1)\n"
"s_sub_u32 s63, s63, s46\n"
"ds_read_b128 v[56:59], v64 offset:20672\n"
"v_exp_f32 v112, v112\n"
"v_exp_f32 v113, v113\n"
"v_exp_f32 v114, v114\n"
"v_exp_f32 v115, v115\n"
"v_mfma_f32_32x32x8_bf16 v[84:99], v[48:49], v[40:41], 0\n"
"v_exp_f32 v68, v68\n"
"v_exp_f32 v69, v69\n"
"v_exp_f32 v70, v70\n"
"v_exp_f32 v71, v71\n"
"v_mfma_f32_32x32x16_bf16 v[84:99], v[44:47], v[32:35], v[84:99]\n"
"s_add_u32 s46, s63, s28\n"
"v_exp_f32 v72, v72\n"
"v_exp_f32 v73, v73\n"
"v_exp_f32 v74, v74\n"
"v_exp_f32 v75, v75\n"
"v_smfmac_f32_32x32x32_bf16 v[116:131], v[148:151], v[100:107], v62\n"
"s_waitcnt lgkmcnt(0)\n"
"s_add_u32 s68, s4, s46\n"
"s_addc_u32 s69, s5, 0\n"
"v_exp_f32 v76, v76\n"
"v_exp_f32 v77, v77\n"
"v_exp_f32 v78, v78\n"
"v_exp_f32 v79, v79\n"
"v_smfmac_f32_32x32x32_bf16 v[116:131], v[152:155], v[108:115], v62\n"
"s_add_u32 m0, s78, 122496\n"
"s_nop 0\n"
"global_load_lds_dwordx4 v1, s[68:69] offset:0 sc1\n"
"global_load_lds_dwordx4 v1, s[68:69] offset:1024 sc1\n"
"global_load_lds_dwordx4 v1, s[68:69] offset:2048 sc1\n"
"global_load_lds_dwordx4 v1, s[68:69] offset:3072 sc1\n"
"v_exp_f32 v80, v80\n"
"v_exp_f32 v81, v81\n"
"v_exp_f32 v82, v82\n"
"v_exp_f32 v83, v83\n"
"v_mfma_f32_32x32x8_bf16 v[100:115], v[48:49], v[42:43], 0\n"
"v_exp_f32 v84, v84\n"
"v_exp_f32 v85, v85\n"
"v_exp_f32 v86, v86\n"
"v_exp_f32 v87, v87\n"
"v_mfma_f32_32x32x16_bf16 v[100:115], v[44:47], v[36:39], v[100:115]\n"
"Lpad5_%=:\n"
"s_cbranch_execz Lpadback1_%=\n"
"s_add_u32 s68, s68, 4096\n"
"s_addc_u32 s69, s69, 0\n"
"ds_read_b128 v[44:47], v60 offset:21696\n"
"v_exp_f32 v88, v88\n"
"v_exp_f32 v89, v89\n"
"v_exp_f32 v90, v90\n"
"v_exp_f32 v91, v91\n"
"v_smfmac_f32_32x32x32_bf16 v[132:147], v[148:151], v[68:75], v62\n"
"s_add_u32 m0, s78, 126592\n"
"ds_read_b128 v[148:151], v65 offset:20672\n"
"v_exp_f32 v92, v92\n"
"v_exp_f32 v93, v93\n"
"v_exp_f32 v94, v94\n"
"v_exp_f32 v95, v95\n"
"v_smfmac_f32_32x32x32_bf16 v[132:147], v[152:155], v[76:83], v62\n"
"s_waitcnt lgkmcnt(1)\n"
"ds_read_b128 v[152:155], v66 offset:20672\n"
"v_exp_f32 v96, v96\n"
"v_exp_f32 v97, v97\n"
"v_exp_f32 v98, v98\n"
"v_exp_f32 v99, v99\n"
"v_mfma_f32_32x32x8_bf16 v[68:83], v[50:51], v[40:41], 0\n"
"v_exp_f32 v100, v100\n"
"v_exp_f32 v101, v101\n"
"v_exp_f32 v102, v102\n"
"v_exp_f32 v103, v103\n"
"v_mfma_f32_32x32x16_bf16 v[68:83], v[44:47], v[32:35], v[68:83]\n"
"v_exp_f32 v104, v104\n"
"v_exp_f32 v105, v105\n"
"v_exp_f32 v106, v106\n"
"v_exp_f32 v107, v107\n"
"v_smfmac_f32_32x32x32_bf16 v[116:131], v[52:55], v[84:91], v62\n"
"s_waitcnt lgkmcnt(0)\n"
"v_exp_f32 v108, v108\n"
"v_exp_f32 v109, v109\n"
"v_exp_f32 v110, v110\n"
"v_exp_f32 v111, v111\n"
"v_smfmac_f32_32x32x32_bf16 v[116:131], v[56:59], v[92:99], v62\n"
"global_load_lds_dwordx4 v1, s[68:69] offset:0 sc1\n"
"global_load_dwordx4 a[180:183], v1, s[64:65] offset:-1024 sc1\n"
"global_load_dwordx4 a[184:187], v1, s[64:65] offset:0 sc1\n"
"global_load_dwordx4 a[188:191], v1, s[64:65] offset:1024 sc1\n"
"v_exp_f32 v112, v112\n"
"v_exp_f32 v113, v113\n"
"v_exp_f32 v114, v114\n"
"v_exp_f32 v115, v115\n"
"v_mfma_f32_32x32x8_bf16 v[84:99], v[50:51], v[42:43], 0\n"
"v_exp_f32 v68, v68\n"
"v_exp_f32 v69, v69\n"
"v_exp_f32 v70, v70\n"
"v_exp_f32 v71, v71\n"
"v_mfma_f32_32x32x16_bf16 v[84:99], v[44:47], v[36:39], v[84:99]\n"
"s_add_u32 s66, s66, 4096\n"
"s_addc_u32 s67, s67, 0\n"
"ds_read_b128 v[48:51], v61 offset:25840\n"
"ds_read_b128 v[44:47], v60 offset:25840\n"
"v_exp_f32 v72, v72\n"
"v_exp_f32 v73, v73\n"
"v_exp_f32 v74, v74\n"
"v_exp_f32 v75, v75\n"
"v_smfmac_f32_32x32x32_bf16 v[132:147], v[52:55], v[100:107], v62\n"
"s_add_u32 m0, s79, 24576\n"
"ds_read_b128 v[52:55], v63 offset:25840\n"
"v_exp_f32 v76, v76\n"
"v_exp_f32 v77, v77\n"
"v_exp_f32 v78, v78\n"
"v_exp_f32 v79, v79\n"
"v_smfmac_f32_32x32x32_bf16 v[132:147], v[56:59], v[108:115], v62\n"
"s_waitcnt lgkmcnt(1)\n"
"ds_read_b128 v[56:59], v64 offset:25840\n"
"v_exp_f32 v80, v80\n"
"v_exp_f32 v81, v81\n"
"v_exp_f32 v82, v82\n"
"v_exp_f32 v83, v83\n"
"v_mfma_f32_32x32x8_bf16 v[100:115], v[48:49], v[40:41], 0\n"
"v_exp_f32 v84, v84\n"
"v_exp_f32 v85, v85\n"
"v_exp_f32 v86, v86\n"
"v_exp_f32 v87, v87\n"
"v_mfma_f32_32x32x16_bf16 v[100:115], v[44:47], v[32:35], v[100:115]\n"
"v_exp_f32 v88, v88\n"
"v_exp_f32 v89, v89\n"
"v_exp_f32 v90, v90\n"
"v_exp_f32 v91, v91\n"
"v_smfmac_f32_32x32x32_bf16 v[116:131], v[148:151], v[68:75], v62\n"
"s_waitcnt lgkmcnt(0)\n"
"v_exp_f32 v92, v92\n"
"v_exp_f32 v93, v93\n"
"v_exp_f32 v94, v94\n"
"v_exp_f32 v95, v95\n"
"v_smfmac_f32_32x32x32_bf16 v[116:131], v[152:155], v[76:83], v62\n"
"global_load_lds_dwordx4 v1, s[66:67] offset:0 sc1\n"
"global_load_dwordx4 a[192:195], v1, s[64:65] offset:2048 sc1\n"
"global_load_dwordx4 a[196:199], v1, s[64:65] offset:3072 sc1\n"
"v_exp_f32 v96, v96\n"
"v_exp_f32 v97, v97\n"
"v_exp_f32 v98, v98\n"
"v_exp_f32 v99, v99\n"
"v_mfma_f32_32x32x8_bf16 v[68:83], v[48:49], v[42:43], 0\n"
"v_exp_f32 v100, v100\n"
"v_exp_f32 v101, v101\n"
"v_exp_f32 v102, v102\n"
"v_exp_f32 v103, v103\n"
"v_mfma_f32_32x32x16_bf16 v[68:83], v[44:47], v[36:39], v[68:83]\n"
"s_add_u32 s64, s64, 8192\n"
"s_addc_u32 s65, s65, 0\n"
"ds_read_b128 v[44:47], v60 offset:26864\n"
"v_exp_f32 v104, v104\n"
"v_exp_f32 v105, v105\n"
"v_exp_f32 v106, v106\n"
"v_exp_f32 v107, v107\n"
"v_smfmac_f32_32x32x32_bf16 v[132:147], v[148:151], v[84:91], v62\n"
"ds_read_b128 v[148:151], v65 offset:25840\n"
"v_exp_f32 v108, v108\n"
"v_exp_f32 v109, v109\n"
"v_exp_f32 v110, v110\n"
"v_exp_f32 v111, v111\n"
"v_smfmac_f32_32x32x32_bf16 v[132:147], v[152:155], v[92:99], v62\n"
"s_waitcnt lgkmcnt(1)\n"
"ds_read_b128 v[152:155], v66 offset:25840\n"
"v_exp_f32 v112, v112\n"
"v_exp_f32 v113, v113\n"
"v_exp_f32 v114, v114\n"
"v_exp_f32 v115, v115\n"
"v_mfma_f32_32x32x8_bf16 v[84:99], v[50:51], v[40:41], 0\n"
"v_exp_f32 v68, v68\n"
"v_exp_f32 v69, v69\n"
"v_exp_f32 v70, v70\n"
"v_exp_f32 v71, v71\n"
"v_mfma_f32_32x32x16_bf16 v[84:99], v[44:47], v[32:35], v[84:99]\n"
"v_exp_f32 v72, v72\n"
"v_exp_f32 v73, v73\n"
"v_exp_f32 v74, v74\n"
"v_exp_f32 v75, v75\n"
"v_smfmac_f32_32x32x32_bf16 v[116:131], v[52:55], v[100:107], v62\n"
"s_waitcnt lgkmcnt(0)\n"
"v_exp_f32 v76, v76\n"
"v_exp_f32 v77, v77\n"
"v_exp_f32 v78, v78\n"
"v_exp_f32 v79, v79\n"
"v_smfmac_f32_32x32x32_bf16 v[116:131], v[56:59], v[108:115], v62\n"
"global_load_dwordx4 a[200:203], v1, s[64:65] offset:-4096 sc1\n"
"global_load_dwordx4 a[204:207], v1, s[64:65] offset:-3072 sc1\n"
"global_load_dwordx4 a[208:211], v1, s[64:65] offset:-2048 sc1\n"
"global_load_dwordx4 a[212:215], v1, s[64:65] offset:-1024 sc1\n"
"v_exp_f32 v80, v80\n"
"v_exp_f32 v81, v81\n"
"v_exp_f32 v82, v82\n"
"v_exp_f32 v83, v83\n"
"v_mfma_f32_32x32x8_bf16 v[100:115], v[50:51], v[42:43], 0\n"
"v_exp_f32 v84, v84\n"
"v_exp_f32 v85, v85\n"
"v_exp_f32 v86, v86\n"
"v_exp_f32 v87, v87\n"
"v_mfma_f32_32x32x16_bf16 v[100:115], v[44:47], v[36:39], v[100:115]\n"
"ds_read_b128 v[48:51], v61 offset:31008\n"
"ds_read_b128 v[44:47], v60 offset:31008\n"
"v_exp_f32 v88, v88\n"
"v_exp_f32 v89, v89\n"
"v_exp_f32 v90, v90\n"
"v_exp_f32 v91, v91\n"
"v_smfmac_f32_32x32x32_bf16 v[132:147], v[52:55], v[68:75], v62\n"
"ds_read_b128 v[52:55], v63 offset:31008\n"
"v_exp_f32 v92, v92\n"
"v_exp_f32 v93, v93\n"
"v_exp_f32 v94, v94\n"
"v_exp_f32 v95, v95\n"
"v_smfmac_f32_32x32x32_bf16 v[132:147], v[56:59], v[76:83], v62\n"
"s_waitcnt lgkmcnt(1)\n"
"ds_read_b128 v[56:59], v64 offset:31008\n"
"v_exp_f32 v96, v96\n"
"v_exp_f32 v97, v97\n"
"v_exp_f32 v98, v98\n"
"v_exp_f32 v99, v99\n"
"v_mfma_f32_32x32x8_bf16 v[68:83], v[48:49], v[40:41], 0\n"
"v_exp_f32 v100, v100\n"
"v_exp_f32 v101, v101\n"
"v_exp_f32 v102, v102\n"
"v_exp_f32 v103, v103\n"
"v_mfma_f32_32x32x16_bf16 v[68:83], v[44:47], v[32:35], v[68:83]\n"
"v_exp_f32 v104, v104\n"
"v_exp_f32 v105, v105\n"
"v_exp_f32 v106, v106\n"
"v_exp_f32 v107, v107\n"
"v_smfmac_f32_32x32x32_bf16 v[116:131], v[148:151], v[84:91], v62\n"
"s_waitcnt lgkmcnt(0)\n"
"v_exp_f32 v108, v108\n"
"v_exp_f32 v109, v109\n"
"v_exp_f32 v110, v110\n"
"v_exp_f32 v111, v111\n"
"v_smfmac_f32_32x32x32_bf16 v[116:131], v[152:155], v[92:99], v62\n"
"global_load_dwordx4 a[216:219], v1, s[64:65] offset:0 sc1\n"
"global_load_dwordx4 a[220:223], v1, s[64:65] offset:1024 sc1\n"
"global_load_dwordx4 a[224:227], v1, s[64:65] offset:2048 sc1\n"
"global_load_dwordx4 a[228:231], v1, s[64:65] offset:3072 sc1\n"
"v_exp_f32 v112, v112\n"
"v_exp_f32 v113, v113\n"
"v_exp_f32 v114, v114\n"
"v_exp_f32 v115, v115\n"
"v_mfma_f32_32x32x8_bf16 v[84:99], v[48:49], v[42:43], 0\n"
"v_exp_f32 v68, v68\n"
"v_exp_f32 v69, v69\n"
"v_exp_f32 v70, v70\n"
"v_exp_f32 v71, v71\n"
"v_mfma_f32_32x32x16_bf16 v[84:99], v[44:47], v[36:39], v[84:99]\n"
"s_add_u32 s64, s64, 8192\n"
"s_addc_u32 s65, s65, 0\n"
"ds_read_b128 v[44:47], v60 offset:32032\n"
"v_exp_f32 v72, v72\n"
"v_exp_f32 v73, v73\n"
"v_exp_f32 v74, v74\n"
"v_exp_f32 v75, v75\n"
"v_smfmac_f32_32x32x32_bf16 v[132:147], v[148:151], v[100:107], v62\n"
"ds_read_b128 v[148:151], v65 offset:31008\n"
"v_exp_f32 v76, v76\n"
"v_exp_f32 v77, v77\n"
"v_exp_f32 v78, v78\n"
"v_exp_f32 v79, v79\n"
"v_smfmac_f32_32x32x32_bf16 v[132:147], v[152:155], v[108:115], v62\n"
"s_waitcnt lgkmcnt(1)\n"
"ds_read_b128 v[152:155], v66 offset:31008\n"
"v_exp_f32 v80, v80\n"
"v_exp_f32 v81, v81\n"
"v_exp_f32 v82, v82\n"
"v_exp_f32 v83, v83\n"
"v_mfma_f32_32x32x8_bf16 v[100:115], v[50:51], v[40:41], 0\n"
"v_exp_f32 v84, v84\n"
"v_exp_f32 v85, v85\n"
"v_exp_f32 v86, v86\n"
"v_exp_f32 v87, v87\n"
"v_mfma_f32_32x32x16_bf16 v[100:115], v[44:47], v[32:35], v[100:115]\n"
"v_exp_f32 v88, v88\n"
"v_exp_f32 v89, v89\n"
"v_exp_f32 v90, v90\n"
"v_exp_f32 v91, v91\n"
"v_smfmac_f32_32x32x32_bf16 v[116:131], v[52:55], v[68:75], v62\n"
"s_waitcnt lgkmcnt(0)\n"
"v_exp_f32 v92, v92\n"
"v_exp_f32 v93, v93\n"
"v_exp_f32 v94, v94\n"
"v_exp_f32 v95, v95\n"
"v_smfmac_f32_32x32x32_bf16 v[116:131], v[56:59], v[76:83], v62\n"
"global_load_dwordx4 a[232:235], v1, s[64:65] offset:-4096 sc1\n"
"global_load_dwordx4 a[236:239], v1, s[64:65] offset:-3072 sc1\n"
"global_load_dwordx4 a[240:243], v1, s[64:65] offset:-2048 sc1\n"
"global_load_dwordx4 a[244:247], v1, s[64:65] offset:-1024 sc1\n"
"v_exp_f32 v96, v96\n"
"v_exp_f32 v97, v97\n"
"v_exp_f32 v98, v98\n"
"v_exp_f32 v99, v99\n"
"v_mfma_f32_32x32x8_bf16 v[68:83], v[50:51], v[42:43], 0\n"
"v_exp_f32 v100, v100\n"
"v_exp_f32 v101, v101\n"
"v_exp_f32 v102, v102\n"
"v_exp_f32 v103, v103\n"
"v_mfma_f32_32x32x16_bf16 v[68:83], v[44:47], v[36:39], v[68:83]\n"
"ds_read_b128 v[48:51], v61 offset:36176\n"
"ds_read_b128 v[44:47], v60 offset:36176\n"
"v_exp_f32 v104, v104\n"
"v_exp_f32 v105, v105\n"
"v_exp_f32 v106, v106\n"
"v_exp_f32 v107, v107\n"
"v_smfmac_f32_32x32x32_bf16 v[132:147], v[52:55], v[84:91], v62\n"
"ds_read_b128 v[52:55], v63 offset:36176\n"
"v_exp_f32 v108, v108\n"
"v_exp_f32 v109, v109\n"
"v_exp_f32 v110, v110\n"
"v_exp_f32 v111, v111\n"
"v_smfmac_f32_32x32x32_bf16 v[132:147], v[56:59], v[92:99], v62\n"
"s_waitcnt lgkmcnt(1)\n"
"ds_read_b128 v[56:59], v64 offset:36176\n"
"v_exp_f32 v112, v112\n"
"v_exp_f32 v113, v113\n"
"v_exp_f32 v114, v114\n"
"v_exp_f32 v115, v115\n"
"v_mfma_f32_32x32x8_bf16 v[84:99], v[48:49], v[40:41], 0\n"
"v_exp_f32 v68, v68\n"
"v_exp_f32 v69, v69\n"
"v_exp_f32 v70, v70\n"
"v_exp_f32 v71, v71\n"
"v_mfma_f32_32x32x16_bf16 v[84:99], v[44:47], v[32:35], v[84:99]\n"
"v_exp_f32 v72, v72\n"
"v_exp_f32 v73, v73\n"
"v_exp_f32 v74, v74\n"
"v_exp_f32 v75, v75\n"
"v_smfmac_f32_32x32x32_bf16 v[116:131], v[148:151], v[100:107], v62\n"
"s_waitcnt lgkmcnt(0)\n"
"v_exp_f32 v76, v76\n"
"v_exp_f32 v77, v77\n"
"v_exp_f32 v78, v78\n"
"v_exp_f32 v79, v79\n"
"v_smfmac_f32_32x32x32_bf16 v[116:131], v[152:155], v[108:115], v62\n"
"global_load_dwordx4 a[248:251], v1, s[64:65] offset:0 sc1\n"
"global_load_dwordx4 a[252:255], v1, s[64:65] offset:1024 sc1\n"
"global_load_dwordx4 v[176:179], v1, s[64:65] offset:2048 sc1\n"
"global_load_dwordx4 v[180:183], v1, s[64:65] offset:3072 sc1\n"
"v_exp_f32 v80, v80\n"
"v_exp_f32 v81, v81\n"
"v_exp_f32 v82, v82\n"
"v_exp_f32 v83, v83\n"
"v_mfma_f32_32x32x8_bf16 v[100:115], v[48:49], v[42:43], 0\n"
"v_exp_f32 v84, v84\n"
"v_exp_f32 v85, v85\n"
"v_exp_f32 v86, v86\n"
"v_exp_f32 v87, v87\n"
"v_mfma_f32_32x32x16_bf16 v[100:115], v[44:47], v[36:39], v[100:115]\n"
"s_add_u32 s64, s64, 8192\n"
"s_addc_u32 s65, s65, 0\n"
"ds_read_b128 v[44:47], v60 offset:37200\n"
"v_exp_f32 v88, v88\n"
"v_exp_f32 v89, v89\n"
"v_exp_f32 v90, v90\n"
"v_exp_f32 v91, v91\n"
"v_smfmac_f32_32x32x32_bf16 v[132:147], v[148:151], v[68:75], v62\n"
"ds_read_b128 v[148:151], v65 offset:36176\n"
"v_exp_f32 v92, v92\n"
"v_exp_f32 v93, v93\n"
"v_exp_f32 v94, v94\n"
"v_exp_f32 v95, v95\n"
"v_smfmac_f32_32x32x32_bf16 v[132:147], v[152:155], v[76:83], v62\n"
"s_waitcnt lgkmcnt(1)\n"
"ds_read_b128 v[152:155], v66 offset:36176\n"
"v_exp_f32 v96, v96\n"
"v_exp_f32 v97, v97\n"
"v_exp_f32 v98, v98\n"
"v_exp_f32 v99, v99\n"
"v_mfma_f32_32x32x8_bf16 v[68:83], v[50:51], v[40:41], 0\n"
"v_exp_f32 v100, v100\n"
"v_exp_f32 v101, v101\n"
"v_exp_f32 v102, v102\n"
"v_exp_f32 v103, v103\n"
"v_mfma_f32_32x32x16_bf16 v[68:83], v[44:47], v[32:35], v[68:83]\n"
"v_exp_f32 v104, v104\n"
"v_exp_f32 v105, v105\n"
"v_exp_f32 v106, v106\n"
"v_exp_f32 v107, v107\n"
"v_smfmac_f32_32x32x32_bf16 v[116:131], v[52:55], v[84:91], v62\n"
"s_waitcnt vmcnt(22) lgkmcnt(0)\n"
"s_barrier\n"
"v_exp_f32 v108, v108\n"
"v_exp_f32 v109, v109\n"
"v_exp_f32 v110, v110\n"
"v_exp_f32 v111, v111\n"
"v_smfmac_f32_32x32x32_bf16 v[116:131], v[56:59], v[92:99], v62\n"
"global_load_dwordx4 v[184:187], v1, s[64:65] offset:-4096 sc1\n"
"global_load_dwordx4 v[188:191], v1, s[64:65] offset:-3072 sc1\n"
"global_load_dwordx4 v[192:195], v1, s[64:65] offset:-2048 sc1\n"
"global_load_dwordx4 v[196:199], v1, s[64:65] offset:-1024 sc1\n"
"v_exp_f32 v112, v112\n"
"v_exp_f32 v113, v113\n"
"v_exp_f32 v114, v114\n"
"v_exp_f32 v115, v115\n"
"v_mfma_f32_32x32x8_bf16 v[84:99], v[50:51], v[42:43], 0\n"
"v_exp_f32 v68, v68\n"
"v_exp_f32 v69, v69\n"
"v_exp_f32 v70, v70\n"
"v_exp_f32 v71, v71\n"
"v_mfma_f32_32x32x16_bf16 v[84:99], v[44:47], v[36:39], v[84:99]\n"
"s_bfe_u32 s45, s9, 0x80010\n"
"s_cbranch_scc1 Lcs28_%=\n"
"Lcb29_%=:\n"
"ds_read_b128 v[48:51], v61 offset:0\n"
"ds_read_b128 v[44:47], v60 offset:0\n"
"v_exp_f32 v72, v72\n"
"v_exp_f32 v73, v73\n"
"v_exp_f32 v74, v74\n"
"v_exp_f32 v75, v75\n"
"v_smfmac_f32_32x32x32_bf16 v[132:147], v[52:55], v[100:107], v62\n"
"s_add_u32 s63, s63, 20480\n"
"s_cmp_ge_u32 s63, 163840\n"
"s_cselect_b32 s46, 163840, 0\n"
"ds_read_b128 v[52:55], v63 offset:0\n"
"v_exp_f32 v76, v76\n"
"v_exp_f32 v77, v77\n"
"v_exp_f32 v78, v78\n"
"v_exp_f32 v79, v79\n"
"v_smfmac_f32_32x32x32_bf16 v[132:147], v[56:59], v[108:115], v62\n"
"s_waitcnt lgkmcnt(1)\n"
"s_sub_u32 s63, s63, s46\n"
"ds_read_b128 v[56:59], v64 offset:0\n"
"v_exp_f32 v80, v80\n"
"v_exp_f32 v81, v81\n"
"v_exp_f32 v82, v82\n"
"v_exp_f32 v83, v83\n"
"v_mfma_f32_32x32x8_bf16 v[100:115], v[48:49], v[40:41], 0\n"
"v_exp_f32 v84, v84\n"
"v_exp_f32 v85, v85\n"
"v_exp_f32 v86, v86\n"
"v_exp_f32 v87, v87\n"
"v_mfma_f32_32x32x16_bf16 v[100:115], v[44:47], v[32:35], v[100:115]\n"
"s_add_u32 s46, s63, s28\n"
"v_exp_f32 v88, v88\n"
"v_exp_f32 v89, v89\n"
"v_exp_f32 v90, v90\n"
"v_exp_f32 v91, v91\n"
"v_smfmac_f32_32x32x32_bf16 v[116:131], v[148:151], v[68:75], v62\n"
"s_waitcnt lgkmcnt(0)\n"
"s_add_u32 s68, s4, s46\n"
"s_addc_u32 s69, s5, 0\n"
"v_exp_f32 v92, v92\n"
"v_exp_f32 v93, v93\n"
"v_exp_f32 v94, v94\n"
"v_exp_f32 v95, v95\n"
"v_smfmac_f32_32x32x32_bf16 v[116:131], v[152:155], v[76:83], v62\n"
"s_add_u32 m0, s78, 143168\n"
"s_nop 0\n"
"global_load_lds_dwordx4 v1, s[68:69] offset:0 sc1\n"
"global_load_lds_dwordx4 v1, s[68:69] offset:1024 sc1\n"
"global_load_lds_dwordx4 v1, s[68:69] offset:2048 sc1\n"
"global_load_lds_dwordx4 v1, s[68:69] offset:3072 sc1\n"
"v_exp_f32 v96, v96\n"
"v_exp_f32 v97, v97\n"
"v_exp_f32 v98, v98\n"
"v_exp_f32 v99, v99\n"
"v_mfma_f32_32x32x8_bf16 v[68:83], v[48:49], v[42:43], 0\n"
"v_exp_f32 v100, v100\n"
"v_exp_f32 v101, v101\n"
"v_exp_f32 v102, v102\n"
"v_exp_f32 v103, v103\n"
"v_mfma_f32_32x32x16_bf16 v[68:83], v[44:47], v[36:39], v[68:83]\n"
"s_add_u32 s68, s68, 4096\n"
"s_addc_u32 s69, s69, 0\n"
"ds_read_b128 v[44:47], v60 offset:1024\n"
"v_exp_f32 v104, v104\n"
"v_exp_f32 v105, v105\n"
"v_exp_f32 v106, v106\n"
"v_exp_f32 v107, v107\n"
"v_smfmac_f32_32x32x32_bf16 v[132:147], v[148:151], v[84:91], v62\n"
"s_add_u32 m0, s78, 147264\n"
"ds_read_b128 v[148:151], v65 offset:0\n"
"v_exp_f32 v108, v108\n"
"v_exp_f32 v109, v109\n"
"v_exp_f32 v110, v110\n"
"v_exp_f32 v111, v111\n"
"v_smfmac_f32_32x32x32_bf16 v[132:147], v[152:155], v[92:99], v62\n"
"s_waitcnt lgkmcnt(1)\n"
"ds_read_b128 v[152:155], v66 offset:0\n"
"v_exp_f32 v112, v112\n"
"v_exp_f32 v113, v113\n"
"v_exp_f32 v114, v114\n"
"v_exp_f32 v115, v115\n"
"v_mfma_f32_32x32x8_bf16 v[84:99], v[50:51], v[40:41], 0\n"
"v_exp_f32 v68, v68\n"
"v_exp_f32 v69, v69\n"
"v_exp_f32 v70, v70\n"
"v_exp_f32 v71, v71\n"
"v_mfma_f32_32x32x16_bf16 v[84:99], v[44:47], v[32:35], v[84:99]\n"
"v_exp_f32 v72, v72\n"
"v_exp_f32 v73, v73\n"
"v_exp_f32 v74, v74\n"
"v_exp_f32 v75, v75\n"
"v_smfmac_f32_32x32x32_bf16 v[116:131], v[52:55], v[100:107], v62\n"
"s_waitcnt lgkmcnt(0)\n"
"v_exp_f32 v76, v76\n"
"v_exp_f32 v77, v77\n"
"v_exp_f32 v78, v78\n"
"v_exp_f32 v79, v79\n"
"v_smfmac_f32_32x32x32_bf16 v[116:131], v[56:59], v[108:115], v62\n"
"global_load_lds_dwordx4 v1, s[68:69] offset:0 sc1\n"
"global_load_dwordx4 v[200:203], v1, s[64:65] offset:0 sc1\n"
"global_load_dwordx4 v[204:207], v1, s[64:65] offset:1024 sc1\n"
"global_load_dwordx4 v[208:211], v1, s[64:65] offset:2048 sc1\n"
"v_exp_f32 v80, v80\n"
"v_exp_f32 v81, v81\n"
"v_exp_f32 v82, v82\n"
"v_exp_f32 v83, v83\n"
"v_mfma_f32_32x32x8_bf16 v[100:115], v[50:51], v[42:43], 0\n"
"v_exp_f32 v84, v84\n"
"v_exp_f32 v85, v85\n"
"v_exp_f32 v86, v86\n"
"v_exp_f32 v87, v87\n"
"v_mfma_f32_32x32x16_bf16 v[100:115], v[44:47], v[36:39], v[100:115]\n"
"ds_read_b128 v[48:51], v61 offset:5168\n"
"ds_read_b128 v[44:47], v60 offset:5168\n"
"v_exp_f32 v88, v88\n"
"v_exp_f32 v89, v89\n"
"v_exp_f32 v90, v90\n"
"v_exp_f32 v91, v91\n"
"v_smfmac_f32_32x32x32_bf16 v[132:147], v[52:55], v[68:75], v62\n"
"ds_read_b128 v[52:55], v63 offset:5168\n"
"v_exp_f32 v92, v92\n"
"v_exp_f32 v93, v93\n"
"v_exp_f32 v94, v94\n"
"v_exp_f32 v95, v95\n"
"v_smfmac_f32_32x32x32_bf16 v[132:147], v[56:59], v[76:83], v62\n"
"s_waitcnt lgkmcnt(1)\n"
"ds_read_b128 v[56:59], v64 offset:5168\n"
"v_exp_f32 v96, v96\n"
"v_exp_f32 v97, v97\n"
"v_exp_f32 v98, v98\n"
"v_exp_f32 v99, v99\n"
"v_mfma_f32_32x32x8_bf16 v[68:83], v[48:49], v[40:41], 0\n"
"v_exp_f32 v100, v100\n"
"v_exp_f32 v101, v101\n"
"v_exp_f32 v102, v102\n"
"v_exp_f32 v103, v103\n"
"v_mfma_f32_32x32x16_bf16 v[68:83], v[44:47], v[32:35], v[68:83]\n"
"v_exp_f32 v104, v104\n"
"v_exp_f32 v105, v105\n"
"v_exp_f32 v106, v106\n"
"v_exp_f32 v107, v107\n"
"v_smfmac_f32_32x32x32_bf16 v[116:131], v[148:151], v[84:91], v62\n"
"s_waitcnt lgkmcnt(0)\n"
"v_exp_f32 v108, v108\n"
"v_exp_f32 v109, v109\n"
"v_exp_f32 v110, v110\n"
"v_exp_f32 v111, v111\n"
"v_smfmac_f32_32x32x32_bf16 v[116:131], v[152:155], v[92:99], v62\n"
"global_load_dwordx4 v[212:215], v1, s[64:65] offset:3072 sc1\n"
"v_exp_f32 v112, v112\n"
"v_exp_f32 v113, v113\n"
"v_exp_f32 v114, v114\n"
"v_exp_f32 v115, v115\n"
"v_mfma_f32_32x32x8_bf16 v[84:99], v[48:49], v[42:43], 0\n"
"v_exp_f32 v68, v68\n"
"v_exp_f32 v69, v69\n"
"v_exp_f32 v70, v70\n"
"v_exp_f32 v71, v71\n"
"v_mfma_f32_32x32x16_bf16 v[84:99], v[44:47], v[36:39], v[84:99]\n"
"s_add_u32 s64, s64, 8192\n"
"s_addc_u32 s65, s65, 0\n"
"ds_read_b128 v[44:47], v60 offset:6192\n"
"v_exp_f32 v72, v72\n"
"v_exp_f32 v73, v73\n"
"v_exp_f32 v74, v74\n"
"v_exp_f32 v75, v75\n"
"v_smfmac_f32_32x32x32_bf16 v[132:147], v[148:151], v[100:107], v62\n"
"ds_read_b128 v[148:151], v65 offset:5168\n"
"v_exp_f32 v76, v76\n"
"v_exp_f32 v77, v77\n"
"v_exp_f32 v78, v78\n"
"v_exp_f32 v79, v79\n"
"v_smfmac_f32_32x32x32_bf16 v[132:147], v[152:155], v[108:115], v62\n"
"s_waitcnt lgkmcnt(1)\n"
"ds_read_b128 v[152:155], v66 offset:5168\n"
"v_exp_f32 v80, v80\n"
"v_exp_f32 v81, v81\n"
"v_exp_f32 v82, v82\n"
"v_exp_f32 v83, v83\n"
"v_mfma_f32_32x32x8_bf16 v[100:115], v[50:51], v[40:41], 0\n"
"v_exp_f32 v84, v84\n"
"v_exp_f32 v85, v85\n"
"v_exp_f32 v86, v86\n"
"v_exp_f32 v87, v87\n"
"v_mfma_f32_32x32x16_bf16 v[100:115], v[44:47], v[32:35], v[100:115]\n"
"v_exp_f32 v88, v88\n"
"v_exp_f32 v89, v89\n"
"v_exp_f32 v90, v90\n"
"v_exp_f32 v91, v91\n"
"v_smfmac_f32_32x32x32_bf16 v[116:131], v[52:55], v[68:75], v62\n"
"s_waitcnt lgkmcnt(0)\n"
"v_exp_f32 v92, v92\n"
"v_exp_f32 v93, v93\n"
"v_exp_f32 v94, v94\n"
"v_exp_f32 v95, v95\n"
"v_smfmac_f32_32x32x32_bf16 v[116:131], v[56:59], v[76:83], v62\n"
"global_load_dwordx4 v[216:219], v1, s[64:65] offset:-4096 sc1\n"
"global_load_dwordx4 v[220:223], v1, s[64:65] offset:-3072 sc1\n"
"global_load_dwordx4 v[224:227], v1, s[64:65] offset:-2048 sc1\n"
"global_load_dwordx4 v[228:231], v1, s[64:65] offset:-1024 sc1\n"
"v_exp_f32 v96, v96\n"
"v_exp_f32 v97, v97\n"
"v_exp_f32 v98, v98\n"
"v_exp_f32 v99, v99\n"
"v_mfma_f32_32x32x8_bf16 v[68:83], v[50:51], v[42:43], 0\n"
"v_exp_f32 v100, v100\n"
"v_exp_f32 v101, v101\n"
"v_exp_f32 v102, v102\n"
"v_exp_f32 v103, v103\n"
"v_mfma_f32_32x32x16_bf16 v[68:83], v[44:47], v[36:39], v[68:83]\n"
"ds_read_b128 v[48:51], v61 offset:10336\n"
"ds_read_b128 v[44:47], v60 offset:10336\n"
"v_exp_f32 v104, v104\n"
"v_exp_f32 v105, v105\n"
"v_exp_f32 v106, v106\n"
"v_exp_f32 v107, v107\n"
"v_smfmac_f32_32x32x32_bf16 v[132:147], v[52:55], v[84:91], v62\n"
"ds_read_b128 v[52:55], v63 offset:10336\n"
"v_exp_f32 v108, v108\n"
"v_exp_f32 v109, v109\n"
"v_exp_f32 v110, v110\n"
"v_exp_f32 v111, v111\n"
"v_smfmac_f32_32x32x32_bf16 v[132:147], v[56:59], v[92:99], v62\n"
"s_waitcnt lgkmcnt(1)\n"
"ds_read_b128 v[56:59], v64 offset:10336\n"
"v_exp_f32 v112, v112\n"
"v_exp_f32 v113, v113\n"
"v_exp_f32 v114, v114\n"
"v_exp_f32 v115, v115\n"
"v_mfma_f32_32x32x8_bf16 v[84:99], v[48:49], v[40:41], 0\n"
"v_exp_f32 v68, v68\n"
"v_exp_f32 v69, v69\n"
"v_exp_f32 v70, v70\n"
"v_exp_f32 v71, v71\n"
"v_mfma_f32_32x32x16_bf16 v[84:99], v[44:47], v[32:35], v[84:99]\n"
"v_exp_f32 v72, v72\n"
"v_exp_f32 v73, v73\n"
"v_exp_f32 v74, v74\n"
"v_exp_f32 v75, v75\n"
"v_smfmac_f32_32x32x32_bf16 v[116:131], v[148:151], v[100:107], v62\n"
"s_waitcnt lgkmcnt(0)\n"
"v_exp_f32 v76, v76\n"
"v_exp_f32 v77, v77\n"
"v_exp_f32 v78, v78\n"
"v_exp_f32 v79, v79\n"
"v_smfmac_f32_32x32x32_bf16 v[116:131], v[152:155], v[108:115], v62\n"
"global_load_dwordx4 v[232:235], v1, s[64:65] offset:0 sc1\n"
"global_load_dwordx4 v[236:239], v1, s[64:65] offset:1024 sc1\n"
"global_load_dwordx4 v[240:243], v1, s[64:65] offset:2048 sc1\n"
"global_load_dwordx4 v[244:247], v1, s[64:65] offset:3072 sc1\n"
"v_exp_f32 v80, v80\n"
"v_exp_f32 v81, v81\n"
"v_exp_f32 v82, v82\n"
"v_exp_f32 v83, v83\n"
"v_mfma_f32_32x32x8_bf16 v[100:115], v[48:49], v[42:43], 0\n"
"v_exp_f32 v84, v84\n"
"v_exp_f32 v85, v85\n"
"v_exp_f32 v86, v86\n"
"v_exp_f32 v87, v87\n"
"v_mfma_f32_32x32x16_bf16 v[100:115], v[44:47], v[36:39], v[100:115]\n"
"s_add_u32 s64, s64, 8192\n"
"s_addc_u32 s65, s65, 0\n"
"ds_read_b128 v[44:47], v60 offset:11360\n"
"v_exp_f32 v88, v88\n"
"v_exp_f32 v89, v89\n"
"v_exp_f32 v90, v90\n"
"v_exp_f32 v91, v91\n"
"v_smfmac_f32_32x32x32_bf16 v[132:147], v[148:151], v[68:75], v62\n"
"ds_read_b128 v[148:151], v65 offset:10336\n"
"v_exp_f32 v92, v92\n"
"v_exp_f32 v93, v93\n"
"v_exp_f32 v94, v94\n"
"v_exp_f32 v95, v95\n"
"v_smfmac_f32_32x32x32_bf16 v[132:147], v[152:155], v[76:83], v62\n"
"s_waitcnt lgkmcnt(1)\n"
"ds_read_b128 v[152:155], v66 offset:10336\n"
"v_exp_f32 v96, v96\n"
"v_exp_f32 v97, v97\n"
"v_exp_f32 v98, v98\n"
"v_exp_f32 v99, v99\n"
"v_mfma_f32_32x32x8_bf16 v[68:83], v[50:51], v[40:41], 0\n"
"v_exp_f32 v100, v100\n"
"v_exp_f32 v101, v101\n"
"v_exp_f32 v102, v102\n"
"v_exp_f32 v103, v103\n"
"v_mfma_f32_32x32x16_bf16 v[68:83], v[44:47], v[32:35], v[68:83]\n"
"v_exp_f32 v104, v104\n"
"v_exp_f32 v105, v105\n"
"v_exp_f32 v106, v106\n"
"v_exp_f32 v107, v107\n"
"v_smfmac_f32_32x32x32_bf16 v[116:131], v[52:55], v[84:91], v62\n"
"s_waitcnt lgkmcnt(0)\n"
"v_exp_f32 v108, v108\n"
"v_exp_f32 v109, v109\n"
"v_exp_f32 v110, v110\n"
"v_exp_f32 v111, v111\n"
"v_smfmac_f32_32x32x32_bf16 v[116:131], v[56:59], v[92:99], v62\n"
"global_load_dwordx4 v[248:251], v1, s[64:65] offset:-4096 sc1\n"
"global_load_dwordx4 v[252:255], v1, s[64:65] offset:-3072 sc1\n"
"global_load_dwordx4 a[0:3], v1, s[64:65] offset:-2048 sc1\n"
"global_load_dwordx4 a[4:7], v1, s[64:65] offset:-1024 sc1\n"
"v_exp_f32 v112, v112\n"
"v_exp_f32 v113, v113\n"
"v_exp_f32 v114, v114\n"
"v_exp_f32 v115, v115\n"
"v_mfma_f32_32x32x8_bf16 v[84:99], v[50:51], v[42:43], 0\n"
"v_exp_f32 v68, v68\n"
"v_exp_f32 v69, v69\n"
"v_exp_f32 v70, v70\n"
"v_exp_f32 v71, v71\n"
"v_mfma_f32_32x32x16_bf16 v[84:99], v[44:47], v[36:39], v[84:99]\n"
"ds_read_b128 v[48:51], v61 offset:15504\n"
"ds_read_b128 v[44:47], v60 offset:15504\n"
"v_exp_f32 v72, v72\n"
"v_exp_f32 v73, v73\n"
"v_exp_f32 v74, v74\n"
"v_exp_f32 v75, v75\n"
"v_smfmac_f32_32x32x32_bf16 v[132:147], v[52:55], v[100:107], v62\n"
"ds_read_b128 v[52:55], v63 offset:15504\n"
"v_exp_f32 v76, v76\n"
"v_exp_f32 v77, v77\n"
"v_exp_f32 v78, v78\n"
"v_exp_f32 v79, v79\n"
"v_smfmac_f32_32x32x32_bf16 v[132:147], v[56:59], v[108:115], v62\n"
"s_waitcnt lgkmcnt(1)\n"
"ds_read_b128 v[56:59], v64 offset:15504\n"
"v_exp_f32 v80, v80\n"
"v_exp_f32 v81, v81\n"
"v_exp_f32 v82, v82\n"
"v_exp_f32 v83, v83\n"
"v_mfma_f32_32x32x8_bf16 v[100:115], v[48:49], v[40:41], 0\n"
"v_exp_f32 v84, v84\n"
"v_exp_f32 v85, v85\n"
"v_exp_f32 v86, v86\n"
"v_exp_f32 v87, v87\n"
"v_mfma_f32_32x32x16_bf16 v[100:115], v[44:47], v[32:35], v[100:115]\n"
"v_exp_f32 v88, v88\n"
"v_exp_f32 v89, v89\n"
"v_exp_f32 v90, v90\n"
"v_exp_f32 v91, v91\n"
"v_smfmac_f32_32x32x32_bf16 v[116:131], v[148:151], v[68:75], v62\n"
"s_waitcnt lgkmcnt(0)\n"
"v_exp_f32 v92, v92\n"
"v_exp_f32 v93, v93\n"
"v_exp_f32 v94, v94\n"
"v_exp_f32 v95, v95\n"
"v_smfmac_f32_32x32x32_bf16 v[116:131], v[152:155], v[76:83], v62\n"
"global_load_dwordx4 a[8:11], v1, s[64:65] offset:0 sc1\n"
"global_load_dwordx4 a[12:15], v1, s[64:65] offset:1024 sc1\n"
"global_load_dwordx4 a[16:19], v1, s[64:65] offset:2048 sc1\n"
"global_load_dwordx4 a[20:23], v1, s[64:65] offset:3072 sc1\n"
"v_exp_f32 v96, v96\n"
"v_exp_f32 v97, v97\n"
"v_exp_f32 v98, v98\n"
"v_exp_f32 v99, v99\n"
"v_mfma_f32_32x32x8_bf16 v[68:83], v[48:49], v[42:43], 0\n"
"v_exp_f32 v100, v100\n"
"v_exp_f32 v101, v101\n"
"v_exp_f32 v102, v102\n"
"v_exp_f32 v103, v103\n"
"v_mfma_f32_32x32x16_bf16 v[68:83], v[44:47], v[36:39], v[68:83]\n"
"s_add_u32 s64, s64, 8192\n"
"s_addc_u32 s65, s65, 0\n"
"ds_read_b128 v[44:47], v60 offset:16528\n"
"v_exp_f32 v104, v104\n"
"v_exp_f32 v105, v105\n"
"v_exp_f32 v106, v106\n"
"v_exp_f32 v107, v107\n"
"v_smfmac_f32_32x32x32_bf16 v[132:147], v[148:151], v[84:91], v62\n"
"ds_read_b128 v[148:151], v65 offset:15504\n"
"v_exp_f32 v108, v108\n"
"v_exp_f32 v109, v109\n"
"v_exp_f32 v110, v110\n"
"v_exp_f32 v111, v111\n"
"v_smfmac_f32_32x32x32_bf16 v[132:147], v[152:155], v[92:99], v62\n"
"s_waitcnt lgkmcnt(1)\n"
"ds_read_b128 v[152:155], v66 offset:15504\n"
"v_exp_f32 v112, v112\n"
"v_exp_f32 v113, v113\n"
"v_exp_f32 v114, v114\n"
"v_exp_f32 v115, v115\n"
"v_mfma_f32_32x32x8_bf16 v[84:99], v[50:51], v[40:41], 0\n"
"v_exp_f32 v68, v68\n"
"v_exp_f32 v69, v69\n"
"v_exp_f32 v70, v70\n"
"v_exp_f32 v71, v71\n"
"v_mfma_f32_32x32x16_bf16 v[84:99], v[44:47], v[32:35], v[84:99]\n"
"v_exp_f32 v72, v72\n"
"v_exp_f32 v73, v73\n"
"v_exp_f32 v74, v74\n"
"v_exp_f32 v75, v75\n"
"v_smfmac_f32_32x32x32_bf16 v[116:131], v[52:55], v[100:107], v62\n"
"s_waitcnt vmcnt(20) lgkmcnt(0)\n"
"s_barrier\n"
"v_exp_f32 v76, v76\n"
"v_exp_f32 v77, v77\n"
"v_exp_f32 v78, v78\n"
"v_exp_f32 v79, v79\n"
"v_smfmac_f32_32x32x32_bf16 v[116:131], v[56:59], v[108:115], v62\n"
"global_load_dwordx4 a[24:27], v1, s[64:65] offset:-4096 sc1\n"
"global_load_dwordx4 a[28:31], v1, s[64:65] offset:-3072 sc1\n"
"global_load_dwordx4 a[32:35], v1, s[64:65] offset:-2048 sc1\n"
"global_load_dwordx4 a[36:39], v1, s[64:65] offset:-1024 sc1\n"
"v_exp_f32 v80, v80\n"
"v_exp_f32 v81, v81\n"
"v_exp_f32 v82, v82\n"
"v_exp_f32 v83, v83\n"
"v_mfma_f32_32x32x8_bf16 v[100:115], v[50:51], v[42:43], 0\n"
"v_exp_f32 v84, v84\n"
"v_exp_f32 v85, v85\n"
"v_exp_f32 v86, v86\n"
"v_exp_f32 v87, v87\n"
"v_mfma_f32_32x32x16_bf16 v[100:115], v[44:47], v[36:39], v[100:115]\n"
"s_bfe_u32 s45, s9, 0x80018\n"
"s_cbranch_scc1 Lcs30_%=\n"
"Lcb31_%=:\n"
"ds_read_b128 v[48:51], v61 offset:20672\n"
"Lpad6_%=:\n"
"s_cbranch_execz Lpadback1_%=\n"
"ds_read_b128 v[44:47], v60 offset:20672\n"
"v_exp_f32 v88, v88\n"
"v_exp_f32 v89, v89\n"
"v_exp_f32 v90, v90\n"
"v_exp_f32 v91, v91\n"
"v_smfmac_f32_32x32x32_bf16 v[132:147], v[52:55], v[68:75], v62\n"
"s_add_u32 s63, s63, 20480\n"
"s_cmp_ge_u32 s63, 163840\n"
"s_cselect_b32 s46, 163840, 0\n"
"ds_read_b128 v[52:55], v63 offset:20672\n"
"v_exp_f32 v92, v92\n"
"v_exp_f32 v93, v93\n"
"v_exp_f32 v94, v94\n"
"v_exp_f32 v95, v95\n"
"v_smfmac_f32_32x32x32_bf16 v[132:147], v[56:59], v[76:83], v62\n"
"s_waitcnt lgkmcnt(1)\n"
"s_sub_u32 s63, s63, s46\n"
"ds_read_b128 v[56:59], v64 offset:20672\n"
"v_exp_f32 v96, v96\n"
"v_exp_f32 v97, v97\n"
"v_exp_f32 v98, v98\n"
"v_exp_f32 v99, v99\n"
"v_mfma_f32_32x32x8_bf16 v[68:83], v[48:49], v[40:41], 0\n"
"v_exp_f32 v100, v100\n"
"v_exp_f32 v101, v101\n"
"v_exp_f32 v102, v102\n"
"v_exp_f32 v103, v103\n"
"v_mfma_f32_32x32x16_bf16 v[68:83], v[44:47], v[32:35], v[68:83]\n"
"s_add_u32 s46, s63, s28\n"
"v_exp_f32 v104, v104\n"
"v_exp_f32 v105, v105\n"
"v_exp_f32 v106, v106\n"
"v_exp_f32 v107, v107\n"
"v_smfmac_f32_32x32x32_bf16 v[116:131], v[148:151], v[84:91], v62\n"
"s_waitcnt lgkmcnt(0)\n"
"s_add_u32 s68, s4, s46\n"
"s_addc_u32 s69, s5, 0\n"
"v_exp_f32 v108, v108\n"
"v_exp_f32 v109, v109\n"
"v_exp_f32 v110, v110\n"
"v_exp_f32 v111, v111\n"
"v_smfmac_f32_32x32x32_bf16 v[116:131], v[152:155], v[92:99], v62\n"
"s_add_u32 m0, s78, 122496\n"
"s_nop 0\n"
"global_load_lds_dwordx4 v1, s[68:69] offset:0 sc1\n"
"global_load_lds_dwordx4 v1, s[68:69] offset:1024 sc1\n"
"global_load_lds_dwordx4 v1, s[68:69] offset:2048 sc1\n"
"global_load_lds_dwordx4 v1, s[68:69] offset:3072 sc1\n"
"v_exp_f32 v112, v112\n"
"v_exp_f32 v113, v113\n"
"v_exp_f32 v114, v114\n"
"v_exp_f32 v115, v115\n"
"v_mfma_f32_32x32x8_bf16 v[84:99], v[48:49], v[42:43], 0\n"
"v_exp_f32 v68, v68\n"
"v_exp_f32 v69, v69\n"
"v_exp_f32 v70, v70\n"
"v_exp_f32 v71, v71\n"
"v_mfma_f32_32x32x16_bf16 v[84:99], v[44:47], v[36:39], v[84:99]\n"
"s_add_u32 s68, s68, 4096\n"
"s_addc_u32 s69, s69, 0\n"
"ds_read_b128 v[44:47], v60 offset:21696\n"
"v_exp_f32 v72, v72\n"
"v_exp_f32 v73, v73\n"
"v_exp_f32 v74, v74\n"
"v_exp_f32 v75, v75\n"
"v_smfmac_f32_32x32x32_bf16 v[132:147], v[148:151], v[100:107], v62\n"
"s_add_u32 m0, s78, 126592\n"
"ds_read_b128 v[148:151], v65 offset:20672\n"
"v_exp_f32 v76, v76\n"
"v_exp_f32 v77, v77\n"
"v_exp_f32 v78, v78\n"
"v_exp_f32 v79, v79\n"
"v_smfmac_f32_32x32x32_bf16 v[132:147], v[152:155], v[108:115], v62\n"
"s_waitcnt lgkmcnt(1)\n"
"ds_read_b128 v[152:155], v66 offset:20672\n"
"v_exp_f32 v80, v80\n"
"v_exp_f32 v81, v81\n"
"v_exp_f32 v82, v82\n"
"v_exp_f32 v83, v83\n"
"v_mfma_f32_32x32x8_bf16 v[100:115], v[50:51], v[40:41], 0\n"
"v_exp_f32 v84, v84\n"
"v_exp_f32 v85, v85\n"
"v_exp_f32 v86, v86\n"
"v_exp_f32 v87, v87\n"
"v_mfma_f32_32x32x16_bf16 v[100:115], v[44:47], v[32:35], v[100:115]\n"
"v_exp_f32 v88, v88\n"
"v_exp_f32 v89, v89\n"
"v_exp_f32 v90, v90\n"
"v_exp_f32 v91, v91\n"
"v_smfmac_f32_32x32x32_bf16 v[116:131], v[52:55], v[68:75], v62\n"
"s_waitcnt lgkmcnt(0)\n"
"v_exp_f32 v92, v92\n"
"v_exp_f32 v93, v93\n"
"v_exp_f32 v94, v94\n"
"v_exp_f32 v95, v95\n"
"v_smfmac_f32_32x32x32_bf16 v[116:131], v[56:59], v[76:83], v62\n"
"global_load_lds_dwordx4 v1, s[68:69] offset:0 sc1\n"
"global_load_dwordx4 a[40:43], v1, s[64:65] offset:0 sc1\n"
"global_load_dwordx4 a[44:47], v1, s[64:65] offset:1024 sc1\n"
"global_load_dwordx4 a[48:51], v1, s[64:65] offset:2048 sc1\n"
"v_exp_f32 v96, v96\n"
"v_exp_f32 v97, v97\n"
"v_exp_f32 v98, v98\n"
"v_exp_f32 v99, v99\n"
"v_mfma_f32_32x32x8_bf16 v[68:83], v[50:51], v[42:43], 0\n"
"v_exp_f32 v100, v100\n"
"v_exp_f32 v101, v101\n"
"v_exp_f32 v102, v102\n"
"v_exp_f32 v103, v103\n"
"v_mfma_f32_32x32x16_bf16 v[68:83], v[44:47], v[36:39], v[68:83]\n"
"s_add_u32 m0, s91, 102400\n"
"ds_read_b128 v[48:51], v61 offset:25840\n"
"ds_read_b128 v[44:47], v60 offset:25840\n"
"v_exp_f32 v104, v104\n"
"v_exp_f32 v105, v105\n"
"v_exp_f32 v106, v106\n"
"v_exp_f32 v107, v107\n"
"v_smfmac_f32_32x32x32_bf16 v[132:147], v[52:55], v[84:91], v62\n"
"ds_read_b128 v[52:55], v63 offset:25840\n"
"v_exp_f32 v108, v108\n"
"v_exp_f32 v109, v109\n"
"v_exp_f32 v110, v110\n"
"v_exp_f32 v111, v111\n"
"v_smfmac_f32_32x32x32_bf16 v[132:147], v[56:59], v[92:99], v62\n"
"s_waitcnt lgkmcnt(1)\n"
"ds_read_b128 v[56:59], v64 offset:25840\n"
"v_exp_f32 v112, v112\n"
"v_exp_f32 v113, v113\n"
"v_exp_f32 v114, v114\n"
"v_exp_f32 v115, v115\n"
"v_mfma_f32_32x32x8_bf16 v[84:99], v[48:49], v[40:41], 0\n"
"v_exp_f32 v68, v68\n"
"v_exp_f32 v69, v69\n"
"v_exp_f32 v70, v70\n"
"v_exp_f32 v71, v71\n"
"v_mfma_f32_32x32x16_bf16 v[84:99], v[44:47], v[32:35], v[84:99]\n"
"v_exp_f32 v72, v72\n"
"v_exp_f32 v73, v73\n"
"v_exp_f32 v74, v74\n"
"v_exp_f32 v75, v75\n"
"v_smfmac_f32_32x32x32_bf16 v[116:131], v[148:151], v[100:107], v62\n"
"s_waitcnt lgkmcnt(0)\n"
"v_exp_f32 v76, v76\n"
"v_exp_f32 v77, v77\n"
"v_exp_f32 v78, v78\n"
"v_exp_f32 v79, v79\n"
"v_smfmac_f32_32x32x32_bf16 v[116:131], v[152:155], v[108:115], v62\n"
"global_load_dwordx4 a[52:55], v1, s[64:65] offset:3072 sc1\n"
"global_load_lds_dwordx4 v1, s[60:61] offset:0 sc1\n"
"v_exp_f32 v80, v80\n"
"v_exp_f32 v81, v81\n"
"v_exp_f32 v82, v82\n"
"v_exp_f32 v83, v83\n"
"v_mfma_f32_32x32x8_bf16 v[100:115], v[48:49], v[42:43], 0\n"
"v_exp_f32 v84, v84\n"
"v_exp_f32 v85, v85\n"
"v_exp_f32 v86, v86\n"
"v_exp_f32 v87, v87\n"
"v_mfma_f32_32x32x16_bf16 v[100:115], v[44:47], v[36:39], v[100:115]\n"
"s_add_u32 s64, s64, 8192\n"
"s_addc_u32 s65, s65, 0\n"
"ds_read_b128 v[44:47], v60 offset:26864\n"
"v_exp_f32 v88, v88\n"
"v_exp_f32 v89, v89\n"
"v_exp_f32 v90, v90\n"
"v_exp_f32 v91, v91\n"
"v_smfmac_f32_32x32x32_bf16 v[132:147], v[148:151], v[68:75], v62\n"
"ds_read_b128 v[148:151], v65 offset:25840\n"
"v_exp_f32 v92, v92\n"
"v_exp_f32 v93, v93\n"
"v_exp_f32 v94, v94\n"
"v_exp_f32 v95, v95\n"
"v_smfmac_f32_32x32x32_bf16 v[132:147], v[152:155], v[76:83], v62\n"
"s_waitcnt lgkmcnt(1)\n"
"ds_read_b128 v[152:155], v66 offset:25840\n"
"v_exp_f32 v96, v96\n"
"v_exp_f32 v97, v97\n"
"v_exp_f32 v98, v98\n"
"v_exp_f32 v99, v99\n"
"v_mfma_f32_32x32x8_bf16 v[68:83], v[50:51], v[40:41], 0\n"
"v_exp_f32 v100, v100\n"
"v_exp_f32 v101, v101\n"
"v_exp_f32 v102, v102\n"
"v_exp_f32 v103, v103\n"
"v_mfma_f32_32x32x16_bf16 v[68:83], v[44:47], v[32:35], v[68:83]\n"
"v_exp_f32 v104, v104\n"
"v_exp_f32 v105, v105\n"
"v_exp_f32 v106, v106\n"
"v_exp_f32 v107, v107\n"
"v_smfmac_f32_32x32x32_bf16 v[116:131], v[52:55], v[84:91], v62\n"
"s_waitcnt lgkmcnt(0)\n"
"v_exp_f32 v108, v108\n"
"v_exp_f32 v109, v109\n"
"v_exp_f32 v110, v110\n"
"v_exp_f32 v111, v111\n"
"v_smfmac_f32_32x32x32_bf16 v[116:131], v[56:59], v[92:99], v62\n"
"global_load_dwordx4 a[56:59], v1, s[64:65] offset:-4096 sc1\n"
"global_load_dwordx4 a[60:63], v1, s[64:65] offset:-3072 sc1\n"
"global_load_dwordx4 a[64:67], v1, s[64:65] offset:-2048 sc1\n"
"global_load_dwordx4 a[68:71], v1, s[64:65] offset:-1024 sc1\n"
"v_exp_f32 v112, v112\n"
"v_exp_f32 v113, v113\n"
"v_exp_f32 v114, v114\n"
"v_exp_f32 v115, v115\n"
"v_mfma_f32_32x32x8_bf16 v[84:99], v[50:51], v[42:43], 0\n"
"v_exp_f32 v68, v68\n"
"v_exp_f32 v69, v69\n"
"v_exp_f32 v70, v70\n"
"v_exp_f32 v71, v71\n"
"v_mfma_f32_32x32x16_bf16 v[84:99], v[44:47], v[36:39], v[84:99]\n"
"ds_read_b128 v[48:51], v61 offset:31008\n"
"ds_read_b128 v[44:47], v60 offset:31008\n"
"v_exp_f32 v72, v72\n"
"v_exp_f32 v73, v73\n"
"v_exp_f32 v74, v74\n"
"v_exp_f32 v75, v75\n"
"v_smfmac_f32_32x32x32_bf16 v[132:147], v[52:55], v[100:107], v62\n"
"ds_read_b128 v[52:55], v63 offset:31008\n"
"v_exp_f32 v76, v76\n"
"v_exp_f32 v77, v77\n"
"v_exp_f32 v78, v78\n"
"v_exp_f32 v79, v79\n"
"v_smfmac_f32_32x32x32_bf16 v[132:147], v[56:59], v[108:115], v62\n"
"s_waitcnt lgkmcnt(1)\n"
"ds_read_b128 v[56:59], v64 offset:31008\n"
"v_exp_f32 v80, v80\n"
"v_exp_f32 v81, v81\n"
"v_exp_f32 v82, v82\n"
"v_exp_f32 v83, v83\n"
"v_mfma_f32_32x32x8_bf16 v[100:115], v[48:49], v[40:41], 0\n"
"v_exp_f32 v84, v84\n"
"v_exp_f32 v85, v85\n"
"v_exp_f32 v86, v86\n"
"v_exp_f32 v87, v87\n"
"v_mfma_f32_32x32x16_bf16 v[100:115], v[44:47], v[32:35], v[100:115]\n"
"v_exp_f32 v88, v88\n"
"v_exp_f32 v89, v89\n"
"v_exp_f32 v90, v90\n"
"v_exp_f32 v91, v91\n"
"v_smfmac_f32_32x32x32_bf16 v[116:131], v[148:151], v[68:75], v62\n"
"s_waitcnt lgkmcnt(0)\n"
"v_exp_f32 v92, v92\n"
"v_exp_f32 v93, v93\n"
"v_exp_f32 v94, v94\n"
"v_exp_f32 v95, v95\n"
"v_smfmac_f32_32x32x32_bf16 v[116:131], v[152:155], v[76:83], v62\n"
"global_load_dwordx4 a[72:75], v1, s[64:65] offset:0 sc1\n"
"global_load_dwordx4 a[76:79], v1, s[64:65] offset:1024 sc1\n"
"global_load_dwordx4 a[80:83], v1, s[64:65] offset:2048 sc1\n"
"buffer_load_dwordx4 a[84:87], v1, s[12:15], s70 offen offset:0 sc1\n"
"v_exp_f32 v96, v96\n"
"v_exp_f32 v97, v97\n"
"v_exp_f32 v98, v98\n"
"v_exp_f32 v99, v99\n"
"v_mfma_f32_32x32x8_bf16 v[68:83], v[48:49], v[42:43], 0\n"
"v_exp_f32 v100, v100\n"
"v_exp_f32 v101, v101\n"
"v_exp_f32 v102, v102\n"
"v_exp_f32 v103, v103\n"
"v_mfma_f32_32x32x16_bf16 v[68:83], v[44:47], v[36:39], v[68:83]\n"
"ds_read_b128 v[44:47], v60 offset:32032\n"
"v_exp_f32 v104, v104\n"
"v_exp_f32 v105, v105\n"
"v_exp_f32 v106, v106\n"
"v_exp_f32 v107, v107\n"
"v_smfmac_f32_32x32x32_bf16 v[132:147], v[148:151], v[84:91], v62\n"
"ds_read_b128 v[148:151], v65 offset:31008\n"
"v_exp_f32 v108, v108\n"
"v_exp_f32 v109, v109\n"
"v_exp_f32 v110, v110\n"
"v_exp_f32 v111, v111\n"
"v_smfmac_f32_32x32x32_bf16 v[132:147], v[152:155], v[92:99], v62\n"
"s_waitcnt lgkmcnt(1)\n"
"ds_read_b128 v[152:155], v66 offset:31008\n"
"v_exp_f32 v112, v112\n"
"v_exp_f32 v113, v113\n"
"v_exp_f32 v114, v114\n"
"v_exp_f32 v115, v115\n"
"v_mfma_f32_32x32x8_bf16 v[84:99], v[50:51], v[40:41], 0\n"
"v_exp_f32 v68, v68\n"
"v_exp_f32 v69, v69\n"
"v_exp_f32 v70, v70\n"
"v_exp_f32 v71, v71\n"
"v_mfma_f32_32x32x16_bf16 v[84:99], v[44:47], v[32:35], v[84:99]\n"
"v_exp_f32 v72, v72\n"
"v_exp_f32 v73, v73\n"
"v_exp_f32 v74, v74\n"
"v_exp_f32 v75, v75\n"
"v_smfmac_f32_32x32x32_bf16 v[116:131], v[52:55], v[100:107], v62\n"
"s_waitcnt lgkmcnt(0)\n"
"v_exp_f32 v76, v76\n"
"v_exp_f32 v77, v77\n"
"v_exp_f32 v78, v78\n"
"v_exp_f32 v79, v79\n"
"v_smfmac_f32_32x32x32_bf16 v[116:131], v[56:59], v[108:115], v62\n"
"buffer_load_dwordx4 a[88:91], v1, s[12:15], s70 offen offset:1024 sc1\n"
"buffer_load_dwordx4 a[92:95], v1, s[12:15], s70 offen offset:2048 sc1\n"
"buffer_load_dwordx4 a[96:99], v1, s[12:15], s70 offen offset:3072 sc1\n"
"v_exp_f32 v80, v80\n"
"v_exp_f32 v81, v81\n"
"v_exp_f32 v82, v82\n"
"v_exp_f32 v83, v83\n"
"v_mfma_f32_32x32x8_bf16 v[100:115], v[50:51], v[42:43], 0\n"
"v_exp_f32 v84, v84\n"
"v_exp_f32 v85, v85\n"
"v_exp_f32 v86, v86\n"
"v_exp_f32 v87, v87\n"
"v_mfma_f32_32x32x16_bf16 v[100:115], v[44:47], v[36:39], v[100:115]\n"
"s_add_u32 s70, s70, 4096\n"
"ds_read_b128 v[48:51], v61 offset:36176\n"
"ds_read_b128 v[44:47], v60 offset:36176\n"
"v_exp_f32 v88, v88\n"
"v_exp_f32 v89, v89\n"
"v_exp_f32 v90, v90\n"
"v_exp_f32 v91, v91\n"
"v_smfmac_f32_32x32x32_bf16 v[132:147], v[52:55], v[68:75], v62\n"
"ds_read_b128 v[52:55], v63 offset:36176\n"
"v_exp_f32 v92, v92\n"
"v_exp_f32 v93, v93\n"
"v_exp_f32 v94, v94\n"
"v_exp_f32 v95, v95\n"
"v_smfmac_f32_32x32x32_bf16 v[132:147], v[56:59], v[76:83], v62\n"
"s_waitcnt lgkmcnt(1)\n"
"ds_read_b128 v[56:59], v64 offset:36176\n"
"v_exp_f32 v96, v96\n"
"v_exp_f32 v97, v97\n"
"v_exp_f32 v98, v98\n"
"v_exp_f32 v99, v99\n"
"v_mfma_f32_32x32x8_bf16 v[68:83], v[48:49], v[40:41], 0\n"
"v_exp_f32 v100, v100\n"
"v_exp_f32 v101, v101\n"
"v_exp_f32 v102, v102\n"
"v_exp_f32 v103, v103\n"
"v_mfma_f32_32x32x16_bf16 v[68:83], v[44:47], v[32:35], v[68:83]\n"
"v_exp_f32 v104, v104\n"
"v_exp_f32 v105, v105\n"
"v_exp_f32 v106, v106\n"
"v_exp_f32 v107, v107\n"
"v_smfmac_f32_32x32x32_bf16 v[116:131], v[148:151], v[84:91], v62\n"
"s_waitcnt lgkmcnt(0)\n"
"v_exp_f32 v108, v108\n"
"v_exp_f32 v109, v109\n"
"v_exp_f32 v110, v110\n"
"v_exp_f32 v111, v111\n"
"v_smfmac_f32_32x32x32_bf16 v[116:131], v[152:155], v[92:99], v62\n"
"buffer_load_dwordx4 a[100:103], v1, s[12:15], s70 offen offset:0 sc1\n"
"buffer_load_dwordx4 a[104:107], v1, s[12:15], s70 offen offset:1024 sc1\n"
"buffer_load_dwordx4 a[108:111], v1, s[12:15], s70 offen offset:2048 sc1\n"
"buffer_load_dwordx4 a[112:115], v1, s[12:15], s70 offen offset:3072 sc1\n"
"v_exp_f32 v112, v112\n"
"v_exp_f32 v113, v113\n"
"v_exp_f32 v114, v114\n"
"v_exp_f32 v115, v115\n"
"v_mfma_f32_32x32x8_bf16 v[84:99], v[48:49], v[42:43], 0\n"
"v_exp_f32 v68, v68\n"
"v_exp_f32 v69, v69\n"
"v_exp_f32 v70, v70\n"
"v_exp_f32 v71, v71\n"
"v_mfma_f32_32x32x16_bf16 v[84:99], v[44:47], v[36:39], v[84:99]\n"
"s_add_u32 s70, s70, 4096\n"
"ds_read_b128 v[44:47], v60 offset:37200\n"
"v_exp_f32 v72, v72\n"
"v_exp_f32 v73, v73\n"
"v_exp_f32 v74, v74\n"
"v_exp_f32 v75, v75\n"
"v_smfmac_f32_32x32x32_bf16 v[132:147], v[148:151], v[100:107], v62\n"
"ds_read_b128 v[148:151], v65 offset:36176\n"
"v_exp_f32 v76, v76\n"
"v_exp_f32 v77, v77\n"
"v_exp_f32 v78, v78\n"
"v_exp_f32 v79, v79\n"
"v_smfmac_f32_32x32x32_bf16 v[132:147], v[152:155], v[108:115], v62\n"
"s_waitcnt lgkmcnt(1)\n"
"ds_read_b128 v[152:155], v66 offset:36176\n"
"v_exp_f32 v80, v80\n"
"v_exp_f32 v81, v81\n"
"v_exp_f32 v82, v82\n"
"v_exp_f32 v83, v83\n"
"v_mfma_f32_32x32x8_bf16 v[100:115], v[50:51], v[40:41], 0\n"
"v_exp_f32 v84, v84\n"
"v_exp_f32 v85, v85\n"
"v_exp_f32 v86, v86\n"
"v_exp_f32 v87, v87\n"
"v_mfma_f32_32x32x16_bf16 v[100:115], v[44:47], v[32:35], v[100:115]\n"
"v_exp_f32 v88, v88\n"
"v_exp_f32 v89, v89\n"
"v_exp_f32 v90, v90\n"
"v_exp_f32 v91, v91\n"
"v_smfmac_f32_32x32x32_bf16 v[116:131], v[52:55], v[68:75], v62\n"
"s_waitcnt vmcnt(20) lgkmcnt(0)\n"
"s_barrier\n"
"v_exp_f32 v92, v92\n"
"v_exp_f32 v93, v93\n"
"v_exp_f32 v94, v94\n"
"v_exp_f32 v95, v95\n"
"v_smfmac_f32_32x32x32_bf16 v[116:131], v[56:59], v[76:83], v62\n"
"buffer_load_dwordx4 a[116:119], v1, s[12:15], s70 offen offset:0 sc1\n"
"buffer_load_dwordx4 a[120:123], v1, s[12:15], s70 offen offset:1024 sc1\n"
"buffer_load_dwordx4 a[124:127], v1, s[12:15], s70 offen offset:2048 sc1\n"
"buffer_load_dwordx4 a[128:131], v1, s[12:15], s70 offen offset:3072 sc1\n"
"v_exp_f32 v96, v96\n"
"v_exp_f32 v97, v97\n"
"v_exp_f32 v98, v98\n"
"v_exp_f32 v99, v99\n"
"v_mfma_f32_32x32x8_bf16 v[68:83], v[50:51], v[42:43], 0\n"
"v_exp_f32 v100, v100\n"
"v_exp_f32 v101, v101\n"
"v_exp_f32 v102, v102\n"
"v_exp_f32 v103, v103\n"
"v_mfma_f32_32x32x16_bf16 v[68:83], v[44:47], v[36:39], v[68:83]\n"
"s_add_u32 s70, s70, 4096\n"
"ds_read_b128 v[48:51], v61 offset:0\n"
"ds_read_b128 v[44:47], v60 offset:0\n"
"v_exp_f32 v104, v104\n"
"v_exp_f32 v105, v105\n"
"v_exp_f32 v106, v106\n"
"v_exp_f32 v107, v107\n"
"v_smfmac_f32_32x32x32_bf16 v[132:147], v[52:55], v[84:91], v62\n"
"ds_read_b128 v[52:55], v63 offset:0\n"
"v_exp_f32 v108, v108\n"
"v_exp_f32 v109, v109\n"
"v_exp_f32 v110, v110\n"
"v_exp_f32 v111, v111\n"
"v_smfmac_f32_32x32x32_bf16 v[132:147], v[56:59], v[92:99], v62\n"
"s_waitcnt lgkmcnt(1)\n"
"ds_read_b128 v[56:59], v64 offset:0\n"
"v_exp_f32 v112, v112\n"
"v_exp_f32 v113, v113\n"
"v_exp_f32 v114, v114\n"
"v_exp_f32 v115, v115\n"
"v_mfma_f32_32x32x8_bf16 v[84:99], v[48:49], v[40:41], 0\n"
"v_exp_f32 v68, v68\n"
"v_exp_f32 v69, v69\n"
"v_exp_f32 v70, v70\n"
"v_exp_f32 v71, v71\n"
"v_mfma_f32_32x32x16_bf16 v[84:99], v[44:47], v[32:35], v[84:99]\n"
"v_exp_f32 v72, v72\n"
"v_exp_f32 v73, v73\n"
"v_exp_f32 v74, v74\n"
"v_exp_f32 v75, v75\n"
"v_smfmac_f32_32x32x32_bf16 v[116:131], v[148:151], v[100:107], v62\n"
"s_waitcnt lgkmcnt(0)\n"
"v_exp_f32 v76, v76\n"
"v_exp_f32 v77, v77\n"
"v_exp_f32 v78, v78\n"
"v_exp_f32 v79, v79\n"
"v_smfmac_f32_32x32x32_bf16 v[116:131], v[152:155], v[108:115], v62\n"
"buffer_load_dwordx4 a[132:135], v1, s[12:15], s70 offen offset:0 sc1\n"
"buffer_load_dwordx4 a[136:139], v1, s[12:15], s70 offen offset:1024 sc1\n"
"buffer_load_dwordx4 a[140:143], v1, s[12:15], s70 offen offset:2048 sc1\n"
"buffer_load_dwordx4 a[144:147], v1, s[12:15], s70 offen offset:3072 sc1\n"
"v_exp_f32 v80, v80\n"
"v_exp_f32 v81, v81\n"
"v_exp_f32 v82, v82\n"
"v_exp_f32 v83, v83\n"
"v_mfma_f32_32x32x8_bf16 v[100:115], v[48:49], v[42:43], 0\n"
"v_exp_f32 v84, v84\n"
"v_exp_f32 v85, v85\n"
"v_exp_f32 v86, v86\n"
"v_exp_f32 v87, v87\n"
"v_mfma_f32_32x32x16_bf16 v[100:115], v[44:47], v[36:39], v[100:115]\n"
"s_add_u32 s70, s70, 4096\n"
"ds_read_b128 v[44:47], v60 offset:1024\n"
"v_exp_f32 v88, v88\n"
"v_exp_f32 v89, v89\n"
"v_exp_f32 v90, v90\n"
"v_exp_f32 v91, v91\n"
"v_smfmac_f32_32x32x32_bf16 v[132:147], v[148:151], v[68:75], v62\n"
"ds_read_b128 v[148:151], v65 offset:0\n"
"v_exp_f32 v92, v92\n"
"v_exp_f32 v93, v93\n"
"v_exp_f32 v94, v94\n"
"v_exp_f32 v95, v95\n"
"v_smfmac_f32_32x32x32_bf16 v[132:147], v[152:155], v[76:83], v62\n"
"s_waitcnt lgkmcnt(1)\n"
"ds_read_b128 v[152:155], v66 offset:0\n"
"v_exp_f32 v96, v96\n"
"v_exp_f32 v97, v97\n"
"v_exp_f32 v98, v98\n"
"v_exp_f32 v99, v99\n"
"v_mfma_f32_32x32x8_bf16 v[68:83], v[50:51], v[40:41], 0\n"
"v_exp_f32 v100, v100\n"
"v_exp_f32 v101, v101\n"
"v_exp_f32 v102, v102\n"
"v_exp_f32 v103, v103\n"
"v_mfma_f32_32x32x16_bf16 v[68:83], v[44:47], v[32:35], v[68:83]\n"
"v_exp_f32 v104, v104\n"
"v_exp_f32 v105, v105\n"
"v_exp_f32 v106, v106\n"
"v_exp_f32 v107, v107\n"
"v_smfmac_f32_32x32x32_bf16 v[116:131], v[52:55], v[84:91], v62\n"
"s_waitcnt lgkmcnt(0)\n"
"v_exp_f32 v108, v108\n"
"v_exp_f32 v109, v109\n"
"v_exp_f32 v110, v110\n"
"v_exp_f32 v111, v111\n"
"v_smfmac_f32_32x32x32_bf16 v[116:131], v[56:59], v[92:99], v62\n"
"buffer_load_dwordx4 a[148:151], v1, s[12:15], s70 offen offset:0 sc1\n"
"buffer_load_dwordx4 a[152:155], v1, s[12:15], s70 offen offset:1024 sc1\n"
"buffer_load_dwordx4 a[156:159], v1, s[12:15], s70 offen offset:2048 sc1\n"
"buffer_load_dwordx4 a[160:163], v1, s[12:15], s70 offen offset:3072 sc1\n"
"v_exp_f32 v112, v112\n"
"v_exp_f32 v113, v113\n"
"v_exp_f32 v114, v114\n"
"v_exp_f32 v115, v115\n"
"v_mfma_f32_32x32x8_bf16 v[84:99], v[50:51], v[42:43], 0\n"
"v_exp_f32 v68, v68\n"
"v_exp_f32 v69, v69\n"
"v_exp_f32 v70, v70\n"
"v_exp_f32 v71, v71\n"
"v_mfma_f32_32x32x16_bf16 v[84:99], v[44:47], v[36:39], v[84:99]\n"
"s_add_u32 s70, s70, 4096\n"
"ds_read_b128 v[48:51], v61 offset:5168\n"
"ds_read_b128 v[44:47], v60 offset:5168\n"
"v_exp_f32 v72, v72\n"
"v_exp_f32 v73, v73\n"
"v_exp_f32 v74, v74\n"
"v_exp_f32 v75, v75\n"
"v_smfmac_f32_32x32x32_bf16 v[132:147], v[52:55], v[100:107], v62\n"
"s_add_u32 m0, s91, 105472\n"
"ds_read_b128 v[52:55], v63 offset:5168\n"
"v_exp_f32 v76, v76\n"
"v_exp_f32 v77, v77\n"
"v_exp_f32 v78, v78\n"
"v_exp_f32 v79, v79\n"
"v_smfmac_f32_32x32x32_bf16 v[132:147], v[56:59], v[108:115], v62\n"
"s_waitcnt lgkmcnt(1)\n"
"ds_read_b128 v[56:59], v64 offset:5168\n"
"v_exp_f32 v80, v80\n"
"v_exp_f32 v81, v81\n"
"v_exp_f32 v82, v82\n"
"v_exp_f32 v83, v83\n"
"v_mfma_f32_32x32x8_bf16 v[100:115], v[48:49], v[40:41], 0\n"
"v_exp_f32 v84, v84\n"
"v_exp_f32 v85, v85\n"
"v_exp_f32 v86, v86\n"
"v_exp_f32 v87, v87\n"
"v_mfma_f32_32x32x16_bf16 v[100:115], v[44:47], v[32:35], v[100:115]\n"
"v_exp_f32 v88, v88\n"
"v_exp_f32 v89, v89\n"
"v_exp_f32 v90, v90\n"
"v_exp_f32 v91, v91\n"
"v_smfmac_f32_32x32x32_bf16 v[116:131], v[148:151], v[68:75], v62\n"
"s_waitcnt lgkmcnt(0)\n"
"v_exp_f32 v92, v92\n"
"v_exp_f32 v93, v93\n"
"v_exp_f32 v94, v94\n"
"v_exp_f32 v95, v95\n"
"v_smfmac_f32_32x32x32_bf16 v[116:131], v[152:155], v[76:83], v62\n"
"buffer_load_dwordx4 a[164:167], v1, s[12:15], s70 offen offset:0 sc1\n"
"global_load_lds_dwordx4 v1, s[60:61] offset:1024 sc1\n"
"v_exp_f32 v96, v96\n"
"v_exp_f32 v97, v97\n"
"v_exp_f32 v98, v98\n"
"v_exp_f32 v99, v99\n"
"v_mfma_f32_32x32x8_bf16 v[68:83], v[48:49], v[42:43], 0\n"
"v_exp_f32 v100, v100\n"
"v_exp_f32 v101, v101\n"
"v_exp_f32 v102, v102\n"
"v_exp_f32 v103, v103\n"
"v_mfma_f32_32x32x16_bf16 v[68:83], v[44:47], v[36:39], v[68:83]\n"
"s_add_u32 m0, s91, 108544\n"
"ds_read_b128 v[44:47], v60 offset:6192\n"
"v_exp_f32 v104, v104\n"
"v_exp_f32 v105, v105\n"
"v_exp_f32 v106, v106\n"
"v_exp_f32 v107, v107\n"
"v_smfmac_f32_32x32x32_bf16 v[132:147], v[148:151], v[84:91], v62\n"
"ds_read_b128 v[148:151], v65 offset:5168\n"
"v_exp_f32 v108, v108\n"
"v_exp_f32 v109, v109\n"
"v_exp_f32 v110, v110\n"
"v_exp_f32 v111, v111\n"
"v_smfmac_f32_32x32x32_bf16 v[132:147], v[152:155], v[92:99], v62\n"
"s_waitcnt lgkmcnt(1)\n"
"ds_read_b128 v[152:155], v66 offset:5168\n"
"v_exp_f32 v112, v112\n"
"v_exp_f32 v113, v113\n"
"v_exp_f32 v114, v114\n"
"v_exp_f32 v115, v115\n"
"v_mfma_f32_32x32x8_bf16 v[84:99], v[50:51], v[40:41], 0\n"
"v_exp_f32 v68, v68\n"
"v_exp_f32 v69, v69\n"
"v_exp_f32 v70, v70\n"
"v_exp_f32 v71, v71\n"
"v_mfma_f32_32x32x16_bf16 v[84:99], v[44:47], v[32:35], v[84:99]\n"
"v_exp_f32 v72, v72\n"
"v_exp_f32 v73, v73\n"
"v_exp_f32 v74, v74\n"
"v_exp_f32 v75, v75\n"
"v_smfmac_f32_32x32x32_bf16 v[116:131], v[52:55], v[100:107], v62\n"
"s_waitcnt lgkmcnt(0)\n"
"v_exp_f32 v76, v76\n"
"v_exp_f32 v77, v77\n"
"v_exp_f32 v78, v78\n"
"v_exp_f32 v79, v79\n"
"v_smfmac_f32_32x32x32_bf16 v[116:131], v[56:59], v[108:115], v62\n"
"global_load_lds_dwordx4 v1, s[60:61] offset:2048 sc1\n"
"v_exp_f32 v80, v80\n"
"v_exp_f32 v81, v81\n"
"v_exp_f32 v82, v82\n"
"v_exp_f32 v83, v83\n"
"v_mfma_f32_32x32x8_bf16 v[100:115], v[50:51], v[42:43], 0\n"
"v_exp_f32 v84, v84\n"
"v_exp_f32 v85, v85\n"
"v_exp_f32 v86, v86\n"
"v_exp_f32 v87, v87\n"
"v_mfma_f32_32x32x16_bf16 v[100:115], v[44:47], v[36:39], v[100:115]\n"
"s_add_u32 m0, s91, 111616\n"
"ds_read_b128 v[48:51], v61 offset:10336\n"
"ds_read_b128 v[44:47], v60 offset:10336\n"
"v_exp_f32 v88, v88\n"
"v_exp_f32 v89, v89\n"
"v_exp_f32 v90, v90\n"
"v_exp_f32 v91, v91\n"
"v_smfmac_f32_32x32x32_bf16 v[132:147], v[52:55], v[68:75], v62\n"
"ds_read_b128 v[52:55], v63 offset:10336\n"
"v_exp_f32 v92, v92\n"
"v_exp_f32 v93, v93\n"
"v_exp_f32 v94, v94\n"
"v_exp_f32 v95, v95\n"
"v_smfmac_f32_32x32x32_bf16 v[132:147], v[56:59], v[76:83], v62\n"
"s_waitcnt lgkmcnt(1)\n"
"ds_read_b128 v[56:59], v64 offset:10336\n"
"v_exp_f32 v96, v96\n"
"v_exp_f32 v97, v97\n"
"v_exp_f32 v98, v98\n"
"v_exp_f32 v99, v99\n"
"v_mfma_f32_32x32x8_bf16 v[68:83], v[48:49], v[40:41], 0\n"
"v_exp_f32 v100, v100\n"
"v_exp_f32 v101, v101\n"
"v_exp_f32 v102, v102\n"
"v_exp_f32 v103, v103\n"
"v_mfma_f32_32x32x16_bf16 v[68:83], v[44:47], v[32:35], v[68:83]\n"
"v_exp_f32 v104, v104\n"
"v_exp_f32 v105, v105\n"
"v_exp_f32 v106, v106\n"
"v_exp_f32 v107, v107\n"
"v_smfmac_f32_32x32x32_bf16 v[116:131], v[148:151], v[84:91], v62\n"
"s_waitcnt lgkmcnt(0)\n"
"v_exp_f32 v108, v108\n"
"v_exp_f32 v109, v109\n"
"v_exp_f32 v110, v110\n"
"v_exp_f32 v111, v111\n"
"v_smfmac_f32_32x32x32_bf16 v[116:131], v[152:155], v[92:99], v62\n"
"global_load_lds_dwordx4 v1, s[60:61] offset:3072 sc1\n"
"v_exp_f32 v112, v112\n"
"v_exp_f32 v113, v113\n"
"v_exp_f32 v114, v114\n"
"v_exp_f32 v115, v115\n"
"v_mfma_f32_32x32x8_bf16 v[84:99], v[48:49], v[42:43], 0\n"
"v_exp_f32 v68, v68\n"
"v_exp_f32 v69, v69\n"
"v_exp_f32 v70, v70\n"
"v_exp_f32 v71, v71\n"
"v_mfma_f32_32x32x16_bf16 v[84:99], v[44:47], v[36:39], v[84:99]\n"
"ds_read_b128 v[44:47], v60 offset:11360\n"
"v_exp_f32 v72, v72\n"
"v_exp_f32 v73, v73\n"
"v_exp_f32 v74, v74\n"
"v_exp_f32 v75, v75\n"
"v_smfmac_f32_32x32x32_bf16 v[132:147], v[148:151], v[100:107], v62\n"
"ds_read_b128 v[148:151], v65 offset:10336\n"
"v_exp_f32 v76, v76\n"
"v_exp_f32 v77, v77\n"
"v_exp_f32 v78, v78\n"
"v_exp_f32 v79, v79\n"
"v_smfmac_f32_32x32x32_bf16 v[132:147], v[152:155], v[108:115], v62\n"
"s_waitcnt lgkmcnt(1)\n"
"ds_read_b128 v[152:155], v66 offset:10336\n"
"v_exp_f32 v80, v80\n"
"v_exp_f32 v81, v81\n"
"v_exp_f32 v82, v82\n"
"v_exp_f32 v83, v83\n"
"v_mfma_f32_32x32x8_bf16 v[100:115], v[50:51], v[40:41], 0\n"
"v_exp_f32 v84, v84\n"
"v_exp_f32 v85, v85\n"
"v_exp_f32 v86, v86\n"
"v_exp_f32 v87, v87\n"
"v_mfma_f32_32x32x16_bf16 v[100:115], v[44:47], v[32:35], v[100:115]\n"
"v_exp_f32 v88, v88\n"
"v_exp_f32 v89, v89\n"
"v_exp_f32 v90, v90\n"
"v_exp_f32 v91, v91\n"
"v_smfmac_f32_32x32x32_bf16 v[116:131], v[52:55], v[68:75], v62\n"
"s_waitcnt lgkmcnt(0)\n"
"v_exp_f32 v92, v92\n"
"v_exp_f32 v93, v93\n"
"v_exp_f32 v94, v94\n"
"v_exp_f32 v95, v95\n"
"v_smfmac_f32_32x32x32_bf16 v[116:131], v[56:59], v[76:83], v62\n"
"v_exp_f32 v96, v96\n"
"v_exp_f32 v97, v97\n"
"v_exp_f32 v98, v98\n"
"v_exp_f32 v99, v99\n"
"v_mfma_f32_32x32x8_bf16 v[68:83], v[50:51], v[42:43], 0\n"
"v_exp_f32 v100, v100\n"
"v_exp_f32 v101, v101\n"
"v_exp_f32 v102, v102\n"
"v_exp_f32 v103, v103\n"
"v_mfma_f32_32x32x16_bf16 v[68:83], v[44:47], v[36:39], v[68:83]\n"
"ds_read_b128 v[48:51], v61 offset:15504\n"
"ds_read_b128 v[44:47], v60 offset:15504\n"
"v_exp_f32 v104, v104\n"
"v_exp_f32 v105, v105\n"
"v_exp_f32 v106, v106\n"
"v_exp_f32 v107, v107\n"
"v_smfmac_f32_32x32x32_bf16 v[132:147], v[52:55], v[84:91], v62\n"
"ds_read_b128 v[52:55], v63 offset:15504\n"
"v_exp_f32 v108, v108\n"
"v_exp_f32 v109, v109\n"
"v_exp_f32 v110, v110\n"
"v_exp_f32 v111, v111\n"
"v_smfmac_f32_32x32x32_bf16 v[132:147], v[56:59], v[92:99], v62\n"
"s_waitcnt lgkmcnt(1)\n"
"ds_read_b128 v[56:59], v64 offset:15504\n"
"v_exp_f32 v112, v112\n"
"v_exp_f32 v113, v113\n"
"v_exp_f32 v114, v114\n"
"v_exp_f32 v115, v115\n"
"v_mfma_f32_32x32x8_bf16 v[84:99], v[48:49], v[40:41], 0\n"
"v_exp_f32 v68, v68\n"
"v_exp_f32 v69, v69\n"
"v_exp_f32 v70, v70\n"
"v_exp_f32 v71, v71\n"
"v_mfma_f32_32x32x16_bf16 v[84:99], v[44:47], v[32:35], v[84:99]\n"
"v_exp_f32 v72, v72\n"
"v_exp_f32 v73, v73\n"
"v_exp_f32 v74, v74\n"
"v_exp_f32 v75, v75\n"
"v_smfmac_f32_32x32x32_bf16 v[116:131], v[148:151], v[100:107], v62\n"
"s_waitcnt lgkmcnt(0)\n"
"v_exp_f32 v76, v76\n"
"v_exp_f32 v77, v77\n"
"v_exp_f32 v78, v78\n"
"v_exp_f32 v79, v79\n"
"v_smfmac_f32_32x32x32_bf16 v[116:131], v[152:155], v[108:115], v62\n"
"v_exp_f32 v80, v80\n"
"v_exp_f32 v81, v81\n"
"v_exp_f32 v82, v82\n"
"v_exp_f32 v83, v83\n"
"v_mfma_f32_32x32x8_bf16 v[100:115], v[48:49], v[42:43], 0\n"
"v_exp_f32 v84, v84\n"
"v_exp_f32 v85, v85\n"
"v_exp_f32 v86, v86\n"
"v_exp_f32 v87, v87\n"
"v_mfma_f32_32x32x16_bf16 v[100:115], v[44:47], v[36:39], v[100:115]\n"
"ds_read_b128 v[44:47], v60 offset:16528\n"
"v_exp_f32 v88, v88\n"
"v_exp_f32 v89, v89\n"
"v_exp_f32 v90, v90\n"
"v_exp_f32 v91, v91\n"
"v_smfmac_f32_32x32x32_bf16 v[132:147], v[148:151], v[68:75], v62\n"
"ds_read_b128 v[148:151], v65 offset:15504\n"
"v_exp_f32 v92, v92\n"
"v_exp_f32 v93, v93\n"
"v_exp_f32 v94, v94\n"
"v_exp_f32 v95, v95\n"
"v_smfmac_f32_32x32x32_bf16 v[132:147], v[152:155], v[76:83], v62\n"
"s_waitcnt lgkmcnt(1)\n"
"ds_read_b128 v[152:155], v66 offset:15504\n"
"v_exp_f32 v96, v96\n"
"v_exp_f32 v97, v97\n"
"v_exp_f32 v98, v98\n"
"v_exp_f32 v99, v99\n"
"v_mfma_f32_32x32x8_bf16 v[68:83], v[50:51], v[40:41], 0\n"
"v_exp_f32 v100, v100\n"
"v_exp_f32 v101, v101\n"
"v_exp_f32 v102, v102\n"
"v_exp_f32 v103, v103\n"
"v_mfma_f32_32x32x16_bf16 v[68:83], v[44:47], v[32:35], v[68:83]\n"
"v_exp_f32 v104, v104\n"
"v_exp_f32 v105, v105\n"
"v_exp_f32 v106, v106\n"
"v_exp_f32 v107, v107\n"
"v_smfmac_f32_32x32x32_bf16 v[116:131], v[52:55], v[84:91], v62\n"
"s_waitcnt lgkmcnt(0)\n"
"v_exp_f32 v108, v108\n"
"v_exp_f32 v109, v109\n"
"v_exp_f32 v110, v110\n"
"v_exp_f32 v111, v111\n"
"v_smfmac_f32_32x32x32_bf16 v[116:131], v[56:59], v[92:99], v62\n"
"Lpad7_%=:\n"
"s_cbranch_execz Lpadback1_%=\n"
"v_exp_f32 v112, v112\n"
"v_exp_f32 v113, v113\n"
"v_exp_f32 v114, v114\n"
"v_exp_f32 v115, v115\n"
"v_mfma_f32_32x32x8_bf16 v[84:99], v[50:51], v[42:43], 0\n"
"v_exp_f32 v68, v68\n"
"v_exp_f32 v69, v69\n"
"v_exp_f32 v70, v70\n"
"v_exp_f32 v71, v71\n"
"v_mfma_f32_32x32x16_bf16 v[84:99], v[44:47], v[36:39], v[84:99]\n"
"v_exp_f32 v72, v72\n"
"v_exp_f32 v73, v73\n"
"v_exp_f32 v74, v74\n"
"v_exp_f32 v75, v75\n"
"v_smfmac_f32_32x32x32_bf16 v[132:147], v[52:55], v[100:107], v62\n"
"v_exp_f32 v76, v76\n"
"v_exp_f32 v77, v77\n"
"v_exp_f32 v78, v78\n"
"v_exp_f32 v79, v79\n"
"v_smfmac_f32_32x32x32_bf16 v[132:147], v[56:59], v[108:115], v62\n"
"v_exp_f32 v80, v80\n"
"v_exp_f32 v81, v81\n"
"v_exp_f32 v82, v82\n"
"v_exp_f32 v83, v83\n"
"v_exp_f32 v84, v84\n"
"v_exp_f32 v85, v85\n"
"v_exp_f32 v86, v86\n"
"v_exp_f32 v87, v87\n"
"v_exp_f32 v88, v88\n"
"v_exp_f32 v89, v89\n"
"v_exp_f32 v90, v90\n"
"v_exp_f32 v91, v91\n"
"v_smfmac_f32_32x32x32_bf16 v[116:131], v[148:151], v[68:75], v62\n"
"v_exp_f32 v92, v92\n"
"v_exp_f32 v93, v93\n"
"v_exp_f32 v94, v94\n"
"v_exp_f32 v95, v95\n"
"v_smfmac_f32_32x32x32_bf16 v[116:131], v[152:155], v[76:83], v62\n"
"v_exp_f32 v96, v96\n"
"v_exp_f32 v97, v97\n"
"v_exp_f32 v98, v98\n"
"v_exp_f32 v99, v99\n"
"v_smfmac_f32_32x32x32_bf16 v[132:147], v[148:151], v[84:91], v62\n"
"s_mov_b32 s46, 0x53800000\n"
"v_smfmac_f32_32x32x32_bf16 v[132:147], v[152:155], v[92:99], v62\n"
"s_nop 4\n"
"v_add_f32 v156, v116, v117\n"
"v_add_f32 v156, v156, v118\n"
"v_add_f32 v156, v156, v119\n"
"v_add_f32 v156, v156, v120\n"
"v_add_f32 v156, v156, v121\n"
"v_add_f32 v156, v156, v122\n"
"v_add_f32 v156, v156, v123\n"
"v_add_f32 v156, v156, v124\n"
"v_add_f32 v156, v156, v125\n"
"v_add_f32 v156, v156, v126\n"
"v_add_f32 v156, v156, v127\n"
"v_fma_f32 v156, v156, 0, v128\n"
"v_cmp_nge_f32 s[48:49], s46, v156\n"
"v_cmp_nge_f32 vcc, s46, v144\n"
"s_nop 0\n"
"s_or_b64 s[48:49], vcc, s[48:49]\n"
"s_cbranch_scc1 Lfb39_%=\n"
"Lback40_%=:\n"
"v_rcp_f32 v128, v128\n"
"v_rcp_f32 v144, v144\n"
"v_pk_mul_f32 v[116:117], v[116:117], v[128:129] op_sel_hi:[1,0]\n"
"v_pk_mul_f32 v[118:119], v[118:119], v[128:129] op_sel_hi:[1,0]\n"
"v_pk_mul_f32 v[120:121], v[120:121], v[128:129] op_sel_hi:[1,0]\n"
"v_pk_mul_f32 v[122:123], v[122:123], v[128:129] op_sel_hi:[1,0]\n"
"v_pk_mul_f32 v[124:125], v[124:125], v[128:129] op_sel_hi:[1,0]\n"
"v_pk_mul_f32 v[126:127], v[126:127], v[128:129] op_sel_hi:[1,0]\n"
"v_cvt_pk_bf16_f32 v156, v116, v117\n"
"v_cvt_pk_bf16_f32 v157, v118, v119\n"
"v_cvt_pk_bf16_f32 v158, v120, v121\n"
"v_cvt_pk_bf16_f32 v159, v122, v123\n"
"v_cvt_pk_bf16_f32 v164, v124, v125\n"
"v_cvt_pk_bf16_f32 v165, v126, v127\n"
"buffer_store_dwordx4 v[156:159], v1, s[12:15], s54 offen offset:0 sc1\n"
"v_pk_mul_f32 v[132:133], v[132:133], v[144:145] op_sel_hi:[1,0]\n"
"v_pk_mul_f32 v[134:135], v[134:135], v[144:145] op_sel_hi:[1,0]\n"
"v_pk_mul_f32 v[136:137], v[136:137], v[144:145] op_sel_hi:[1,0]\n"
"v_pk_mul_f32 v[138:139], v[138:139], v[144:145] op_sel_hi:[1,0]\n"
"v_pk_mul_f32 v[140:141], v[140:141], v[144:145] op_sel_hi:[1,0]\n"
"v_pk_mul_f32 v[142:143], v[142:143], v[144:145] op_sel_hi:[1,0]\n"
"v_cvt_pk_bf16_f32 v160, v132, v133\n"
"v_cvt_pk_bf16_f32 v161, v134, v135\n"
"v_cvt_pk_bf16_f32 v162, v136, v137\n"
"v_cvt_pk_bf16_f32 v163, v138, v139\n"
"v_cvt_pk_bf16_f32 v166, v140, v141\n"
"v_cvt_pk_bf16_f32 v167, v142, v143\n"
"buffer_store_dwordx4 v[160:163], v1, s[12:15], s54 offen offset:1024 sc1\n"
"s_mov_b32 exec_hi, 0\n"
"buffer_store_dwordx4 v[164:167], v1, s[12:15], s55 offen sc1\n"
"s_mov_b32 exec_hi, -1\n"
"s_mov_b64 exec, 0xff\n"
"global_atomic_add v174, v0, s[4:5]\n"
"s_mov_b64 exec, 1\n"
"global_atomic_add v108, v3, v2, s[4:5] sc0\n"
"s_mov_b64 exec, -1\n"
"ds_read_b128 v[112:115], v1 offset:0\n"
"ds_read_b128 v[116:119], v1 offset:5120\n"
"ds_read_b128 v[120:123], v1 offset:1024\n"
"ds_read_b128 v[124:127], v1 offset:6144\n"
"ds_read_b128 v[128:131], v1 offset:2048\n"
"ds_read_b128 v[132:135], v1 offset:7168\n"
"ds_read_b128 v[136:139], v1 offset:3072\n"
"ds_read_b128 v[140:143], v1 offset:8192\n"
"s_add_u32 s64, s29, 4096\n"
"s_add_u32 s64, s4, s64\n"
"s_addc_u32 s65, s5, 0\n"
"s_mul_i32 s54, s21, 2560\n"
"s_add_u32 s54, s54, s30\n"
"s_mul_i32 s46, s21, 276480\n"
"s_mul_i32 s47, s22, 71680\n"
"s_add_u32 s46, s46, s47\n"
"s_add_u32 s66, s46, 4686848\n"
"s_add_u32 s66, s4, s66\n"
"s_addc_u32 s67, s5, 0\n"
"s_add_u32 s46, s46, 4744192\n"
"s_cmp_lt_u32 s22, 3\n"
"s_cselect_b32 s46, s46, 2147418112\n"
"s_mov_b32 s68, s46\n"
"global_load_dwordx4 v[144:147], v1, s[66:67] offset:-4096 sc1\n"
"s_mov_b64 exec, 1\n"
"global_atomic_add v109, v3, v2, s[4:5] sc0\n"
"s_mov_b64 exec, -1\n"
"global_load_dwordx4 v[148:151], v1, s[66:67] offset:-3072 sc1\n"
"global_load_dwordx4 v[152:155], v1, s[66:67] offset:-2048 sc1\n"
"global_load_dwordx4 v[156:159], v1, s[66:67] offset:-1024 sc1\n"
"s_mov_b64 exec, 0\n"
"s_cmp_eq_u32 s22, 0\n"
"s_cbranch_scc1 Lpad9_%=\n"
"s_cmp_eq_u32 s22, 1\n"
"s_cbranch_scc1 Lpad10_%=\n"
"s_cmp_eq_u32 s22, 2\n"
"s_cbranch_scc1 Lpad11_%=\n"
"s_branch Lpad12_%=\n"
"Lpadback2_%=:\n"
"s_mov_b64 exec, -1\n"
"s_mov_b64 exec, 1\n"
"global_atomic_add v110, v3, v2, s[4:5] sc0\n"
"s_mov_b64 exec, -1\n"
"global_load_dwordx4 v[160:163], v1, s[66:67] offset:0 sc1\n"
"global_load_dwordx4 v[164:167], v1, s[66:67] offset:1024 sc1\n"
"global_load_dwordx4 v[168:171], v1, s[66:67] offset:2048 sc1\n"
"s_mov_b64 exec, 1\n"
"global_atomic_add v111, v3, v2, s[4:5] sc0\n"
"s_mov_b64 exec, -1\n"
"s_add_u32 s46, s91, 143360\n"
"v_add_u32 v173, s46, v1\n"
"ds_write_b128 v173, v[12:15] offset:0\n"
"ds_write_b128 v173, v[16:19] offset:4096\n"
"ds_write_b128 v173, v[20:23] offset:8192\n"
"ds_write_b128 v173, v[24:27] offset:12288\n"
"ds_write_b128 v173, v[28:31] offset:16384\n"
"v_mov_b32 v0, 0x100\n"
"v_mov_b32 v172, 3\n"
"s_barrier\n"
"s_or_b32 s46, s22, s23\n"
"s_or_b32 s46, s46, s6\n"
"s_cbranch_scc1 Lep43_%=\n"
"s_mov_b64 exec, 1\n"
"v_mov_b32 v32, s40\n"
"v_mov_b32 v33, s41\n"
"buffer_store_dwordx2 v[32:33], v2, s[12:15], 0 offen sc1\n"
"s_mov_b64 exec, -1\n"
"Lep43_%=:\n"
"s_mov_b64 exec, 1\n"
"s_waitcnt vmcnt(10)\n"
"v_readfirstlane_b32 s45, v108\n"
"s_nop 0\n"
"s_and_b32 s45, s45, 255\n"
"s_cmp_ge_u32 s45, 8\n"
"s_cbranch_scc1 Ldone42_%=\n"
"global_atomic_add v108, v3, v2, s[4:5] sc0\n"
"s_waitcnt vmcnt(9)\n"
"v_readfirstlane_b32 s45, v109\n"
"s_nop 0\n"
"s_and_b32 s45, s45, 255\n"
"s_cmp_ge_u32 s45, 8\n"
"s_cbranch_scc1 Ldone42_%=\n"
"global_atomic_add v109, v3, v2, s[4:5] sc0\n"
"s_waitcnt vmcnt(6)\n"
"v_readfirstlane_b32 s45, v110\n"
"s_nop 0\n"
"s_and_b32 s45, s45, 255\n"
"s_cmp_ge_u32 s45, 8\n"
"s_cbranch_scc1 Ldone42_%=\n"
"global_atomic_add v110, v3, v2, s[4:5] sc0\n"
"s_waitcnt vmcnt(3)\n"
"v_readfirstlane_b32 s45, v111\n"
"s_nop 0\n"
"s_and_b32 s45, s45, 255\n"
"s_cmp_ge_u32 s45, 8\n"
"s_cbranch_scc1 Ldone42_%=\n"
"global_atomic_add v111, v3, v2, s[4:5] sc0\n"
"Lpoll41_%=:\n"
"s_waitcnt vmcnt(3)\n"
"v_readfirstlane_b32 s45, v108\n"
"s_nop 0\n"
"s_and_b32 s45, s45, 255\n"
"s_cmp_ge_u32 s45, 8\n"
"s_cbranch_scc1 Ldone42_%=\n"
"global_atomic_add v108, v3, v2, s[4:5] sc0\n"
"s_waitcnt vmcnt(3)\n"
"v_readfirstlane_b32 s45, v109\n"
"s_nop 0\n"
"s_and_b32 s45, s45, 255\n"
"s_cmp_ge_u32 s45, 8\n"
"s_cbranch_scc1 Ldone42_%=\n"
"global_atomic_add v109, v3, v2, s[4:5] sc0\n"
"s_waitcnt vmcnt(3)\n"
"v_readfirstlane_b32 s45, v110\n"
"s_nop 0\n"
"s_and_b32 s45, s45, 255\n"
"s_cmp_ge_u32 s45, 8\n"
"s_cbranch_scc1 Ldone42_%=\n"
"global_atomic_add v110, v3, v2, s[4:5] sc0\n"
"s_waitcnt vmcnt(3)\n"
"v_readfirstlane_b32 s45, v111\n"
"s_nop 0\n"
"s_and_b32 s45, s45, 255\n"
"s_cmp_ge_u32 s45, 8\n"
"s_cbranch_scc1 Ldone42_%=\n"
"global_atomic_add v111, v3, v2, s[4:5] sc0\n"
"s_add_u32 s25, s25, 4\n"
"s_cmp_lt_u32 s25, 1048576\n"
"s_cbranch_scc1 Lpoll41_%=\n"
"Ldone42_%=:\n"
"s_mov_b64 exec, -1\n"
"Lrb44_%=:\n"
"global_load_dwordx4 v[32:35], v1, s[64:65] offset:-4096 sc1\n"
"global_load_dwordx4 v[36:39], v1, s[64:65] offset:-3072 sc1\n"
"global_load_dwordx4 v[40:43], v1, s[64:65] offset:-2048 sc1\n"
"global_load_dwordx4 v[44:47], v1, s[64:65] offset:-1024 sc1\n"
"global_load_dwordx4 v[48:51], v1, s[64:65] offset:0 sc1\n"
"global_load_dwordx4 v[52:55], v1, s[64:65] offset:1024 sc1\n"
"global_load_dwordx4 v[56:59], v1, s[64:65] offset:2048 sc1\n"
"global_load_dwordx4 v[60:63], v1, s[64:65] offset:3072 sc1\n"
"s_add_u32 s64, s64, 8192\n"
"s_addc_u32 s65, s65, 0\n"
"global_load_dwordx4 v[64:67], v1, s[64:65] offset:-4096 sc1\n"
"global_load_dwordx4 v[68:71], v1, s[64:65] offset:-3072 sc1\n"
"s_waitcnt vmcnt(8) lgkmcnt(8)\n"
"v_mfma_f32_16x16x32_bf16 v[12:15], v[112:115], v[32:35], v[12:15]\n"
"ds_read_b128 v[112:115], v1 offset:4096\n"
"v_mfma_f32_16x16x32_bf16 v[12:15], v[116:119], v[36:39], v[12:15]\n"
"ds_read_b128 v[116:119], v1 offset:9216\n"
"global_load_dwordx4 v[72:75], v1, s[64:65] offset:-2048 sc1\n"
"v_mfma_f32_16x16x32_bf16 v[16:19], v[120:123], v[32:35], v[16:19]\n"
"ds_read_b128 v[120:123], v1 offset:10240\n"
"v_mfma_f32_16x16x32_bf16 v[16:19], v[124:127], v[36:39], v[16:19]\n"
"s_waitcnt lgkmcnt(10)\n"
"ds_read_b128 v[124:127], v1 offset:15360\n"
"v_mfma_f32_16x16x32_bf16 v[20:23], v[128:131], v[32:35], v[20:23]\n"
"ds_read_b128 v[128:131], v1 offset:11264\n"
"v_mfma_f32_16x16x32_bf16 v[20:23], v[132:135], v[36:39], v[20:23]\n"
"s_waitcnt lgkmcnt(10)\n"
"ds_read_b128 v[132:135], v1 offset:16384\n"
"global_load_dwordx4 v[76:79], v1, s[64:65] offset:-1024 sc1\n"
"v_mfma_f32_16x16x32_bf16 v[24:27], v[136:139], v[32:35], v[24:27]\n"
"ds_read_b128 v[136:139], v1 offset:12288\n"
"v_mfma_f32_16x16x32_bf16 v[24:27], v[140:143], v[36:39], v[24:27]\n"
"s_waitcnt lgkmcnt(5)\n"
"ds_read_b128 v[140:143], v1 offset:17408\n"
"v_mfma_f32_16x16x32_bf16 v[28:31], v[112:115], v[32:35], v[28:31]\n"
"ds_read_b128 v[112:115], v1 offset:13312\n"
"v_mfma_f32_16x16x32_bf16 v[28:31], v[116:119], v[36:39], v[28:31]\n"
"s_waitcnt vmcnt(8) lgkmcnt(5)\n"
"ds_read_b128 v[116:119], v1 offset:18432\n"
"v_mfma_f32_16x16x32_bf16 v[12:15], v[120:123], v[40:43], v[12:15]\n"
"ds_read_b128 v[120:123], v1 offset:14336\n"
"v_mfma_f32_16x16x32_bf16 v[12:15], v[124:127], v[44:47], v[12:15]\n"
"s_waitcnt lgkmcnt(5)\n"
"ds_read_b128 v[124:127], v1 offset:19456\n"
"global_load_dwordx4 v[80:83], v1, s[64:65] offset:0 sc1\n"
"v_mfma_f32_16x16x32_bf16 v[16:19], v[128:131], v[40:43], v[16:19]\n"
"ds_read_b128 v[128:131], v1 offset:20480\n"
"v_mfma_f32_16x16x32_bf16 v[16:19], v[132:135], v[44:47], v[16:19]\n"
"s_waitcnt lgkmcnt(5)\n"
"ds_read_b128 v[132:135], v1 offset:25600\n"
"v_mfma_f32_16x16x32_bf16 v[20:23], v[136:139], v[40:43], v[20:23]\n"
"ds_read_b128 v[136:139], v1 offset:21504\n"
"v_mfma_f32_16x16x32_bf16 v[20:23], v[140:143], v[44:47], v[20:23]\n"
"s_waitcnt lgkmcnt(5)\n"
"ds_read_b128 v[140:143], v1 offset:26624\n"
"global_load_dwordx4 v[84:87], v1, s[64:65] offset:1024 sc1\n"
"v_mfma_f32_16x16x32_bf16 v[24:27], v[112:115], v[40:43], v[24:27]\n"
"ds_read_b128 v[112:115], v1 offset:22528\n"
"v_mfma_f32_16x16x32_bf16 v[24:27], v[116:119], v[44:47], v[24:27]\n"
"s_waitcnt lgkmcnt(5)\n"
"ds_read_b128 v[116:119], v1 offset:27648\n"
"v_mfma_f32_16x16x32_bf16 v[28:31], v[120:123], v[40:43], v[28:31]\n"
"ds_read_b128 v[120:123], v1 offset:23552\n"
"v_mfma_f32_16x16x32_bf16 v[28:31], v[124:127], v[44:47], v[28:31]\n"
"s_waitcnt vmcnt(8) lgkmcnt(5)\n"
"ds_read_b128 v[124:127], v1 offset:28672\n"
"v_mfma_f32_16x16x32_bf16 v[12:15], v[128:131], v[48:51], v[12:15]\n"
"ds_read_b128 v[128:131], v1 offset:24576\n"
"v_mfma_f32_16x16x32_bf16 v[12:15], v[132:135], v[52:55], v[12:15]\n"
"s_waitcnt lgkmcnt(5)\n"
"ds_read_b128 v[132:135], v1 offset:29696\n"
"global_load_dwordx4 v[88:91], v1, s[64:65] offset:2048 sc1\n"
"v_mfma_f32_16x16x32_bf16 v[16:19], v[136:139], v[48:51], v[16:19]\n"
"ds_read_b128 v[136:139], v1 offset:30720\n"
"v_mfma_f32_16x16x32_bf16 v[16:19], v[140:143], v[52:55], v[16:19]\n"
"s_waitcnt lgkmcnt(5)\n"
"ds_read_b128 v[140:143], v1 offset:35840\n"
"v_mfma_f32_16x16x32_bf16 v[20:23], v[112:115], v[48:51], v[20:23]\n"
"ds_read_b128 v[112:115], v1 offset:31744\n"
"v_mfma_f32_16x16x32_bf16 v[20:23], v[116:119], v[52:55], v[20:23]\n"
"s_waitcnt lgkmcnt(5)\n"
"ds_read_b128 v[116:119], v1 offset:36864\n"
"global_load_dwordx4 v[92:95], v1, s[64:65] offset:3072 sc1\n"
"v_mfma_f32_16x16x32_bf16 v[24:27], v[120:123], v[48:51], v[24:27]\n"
"ds_read_b128 v[120:123], v1 offset:32768\n"
"v_mfma_f32_16x16x32_bf16 v[24:27], v[124:127], v[52:55], v[24:27]\n"
"s_waitcnt lgkmcnt(5)\n"
"ds_read_b128 v[124:127], v1 offset:37888\n"
"v_mfma_f32_16x16x32_bf16 v[28:31], v[128:131], v[48:51], v[28:31]\n"
"ds_read_b128 v[128:131], v1 offset:33792\n"
"v_mfma_f32_16x16x32_bf16 v[28:31], v[132:135], v[52:55], v[28:31]\n"
"s_waitcnt vmcnt(8) lgkmcnt(5)\n"
"ds_read_b128 v[132:135], v1 offset:38912\n"
"v_mfma_f32_16x16x32_bf16 v[12:15], v[136:139], v[56:59], v[12:15]\n"
"ds_read_b128 v[136:139], v1 offset:34816\n"
"v_mfma_f32_16x16x32_bf16 v[12:15], v[140:143], v[60:63], v[12:15]\n"
"s_waitcnt lgkmcnt(5)\n"
"s_add_u32 s64, s64, 8192\n"
"s_addc_u32 s65, s65, 0\n"
"ds_read_b128 v[140:143], v1 offset:39936\n"
"global_load_dwordx4 v[96:99], v1, s[64:65] offset:-4096 sc1\n"
"v_mfma_f32_16x16x32_bf16 v[16:19], v[112:115], v[56:59], v[16:19]\n"
"ds_read_b128 v[112:115], v1 offset:40960\n"
"v_mfma_f32_16x16x32_bf16 v[16:19], v[116:119], v[60:63], v[16:19]\n"
"s_waitcnt lgkmcnt(5)\n"
"ds_read_b128 v[116:119], v1 offset:46080\n"
"v_mfma_f32_16x16x32_bf16 v[20:23], v[120:123], v[56:59], v[20:23]\n"
"ds_read_b128 v[120:123], v1 offset:41984\n"
"v_mfma_f32_16x16x32_bf16 v[20:23], v[124:127], v[60:63], v[20:23]\n"
"s_waitcnt lgkmcnt(5)\n"
"ds_read_b128 v[124:127], v1 offset:47104\n"
"global_load_dwordx4 v[100:103], v1, s[64:65] offset:-3072 sc1\n"
"v_mfma_f32_16x16x32_bf16 v[24:27], v[128:131], v[56:59], v[24:27]\n"
"ds_read_b128 v[128:131], v1 offset:43008\n"
"v_mfma_f32_16x16x32_bf16 v[24:27], v[132:135], v[60:63], v[24:27]\n"
"s_waitcnt lgkmcnt(5)\n"
"ds_read_b128 v[132:135], v1 offset:48128\n"
"v_mfma_f32_16x16x32_bf16 v[28:31], v[136:139], v[56:59], v[28:31]\n"
"ds_read_b128 v[136:139], v1 offset:44032\n"
"v_mfma_f32_16x16x32_bf16 v[28:31], v[140:143], v[60:63], v[28:31]\n"
"s_waitcnt vmcnt(8) lgkmcnt(5)\n"
"ds_read_b128 v[140:143], v1 offset:49152\n"
"v_mfma_f32_16x16x32_bf16 v[12:15], v[112:115], v[64:67], v[12:15]\n"
"ds_read_b128 v[112:115], v1 offset:45056\n"
"v_mfma_f32_16x16x32_bf16 v[12:15], v[116:119], v[68:71], v[12:15]\n"
"s_waitcnt lgkmcnt(5)\n"
"ds_read_b128 v[116:119], v1 offset:50176\n"
"global_load_dwordx4 v[104:107], v1, s[64:65] offset:-2048 sc1\n"
"v_mfma_f32_16x16x32_bf16 v[16:19], v[120:123], v[64:67], v[16:19]\n"
"ds_read_b128 v[120:123], v1 offset:51200\n"
"v_mfma_f32_16x16x32_bf16 v[16:19], v[124:127], v[68:71], v[16:19]\n"
"s_waitcnt lgkmcnt(5)\n"
"ds_read_b128 v[124:127], v1 offset:56320\n"
"v_mfma_f32_16x16x32_bf16 v[20:23], v[128:131], v[64:67], v[20:23]\n"
"ds_read_b128 v[128:131], v1 offset:52224\n"
"v_mfma_f32_16x16x32_bf16 v[20:23], v[132:135], v[68:71], v[20:23]\n"
"s_waitcnt lgkmcnt(5)\n"
"ds_read_b128 v[132:135], v1 offset:57344\n"
"global_load_dwordx4 v[108:111], v1, s[64:65] offset:-1024 sc1\n"
"v_mfma_f32_16x16x32_bf16 v[24:27], v[136:139], v[64:67], v[24:27]\n"
"ds_read_b128 v[136:139], v1 offset:53248\n"
"v_mfma_f32_16x16x32_bf16 v[24:27], v[140:143], v[68:71], v[24:27]\n"
"s_waitcnt lgkmcnt(5)\n"
"ds_read_b128 v[140:143], v1 offset:58368\n"
"v_mfma_f32_16x16x32_bf16 v[28:31], v[112:115], v[64:67], v[28:31]\n"
"ds_read_b128 v[112:115], v1 offset:54272\n"
"v_mfma_f32_16x16x32_bf16 v[28:31], v[116:119], v[68:71], v[28:31]\n"
"s_waitcnt vmcnt(8) lgkmcnt(5)\n"
"s_add_u32 s60, s60, 4096\n"
"s_addc_u32 s61, s61, 0\n"
"s_add_u32 m0, s91, 118784\n"
"ds_read_b128 v[116:119], v1 offset:59392\n"
"global_load_lds_dwordx4 v1, s[60:61] offset:0 sc1\n"
"v_mfma_f32_16x16x32_bf16 v[12:15], v[120:123], v[72:75], v[12:15]\n"
"ds_read_b128 v[120:123], v1 offset:55296\n"
"v_mfma_f32_16x16x32_bf16 v[12:15], v[124:127], v[76:79], v[12:15]\n"
"s_waitcnt lgkmcnt(5)\n"
"ds_read_b128 v[124:127], v1 offset:60416\n"
"v_mfma_f32_16x16x32_bf16 v[16:19], v[128:131], v[72:75], v[16:19]\n"
"ds_read_b128 v[128:131], v1 offset:61440\n"
"v_mfma_f32_16x16x32_bf16 v[16:19], v[132:135], v[76:79], v[16:19]\n"
"s_waitcnt lgkmcnt(5)\n"
"s_add_u32 m0, s91, 121856\n"
"ds_read_b128 v[132:135], v8 offset:1024\n"
"global_load_lds_dwordx4 v1, s[60:61] offset:1024 sc1\n"
"v_mfma_f32_16x16x32_bf16 v[20:23], v[136:139], v[72:75], v[20:23]\n"
"ds_read_b128 v[136:139], v1 offset:62464\n"
"v_mfma_f32_16x16x32_bf16 v[20:23], v[140:143], v[76:79], v[20:23]\n"
"s_waitcnt lgkmcnt(5)\n"
"ds_read_b128 v[140:143], v8 offset:2048\n"
"v_mfma_f32_16x16x32_bf16 v[24:27], v[112:115], v[72:75], v[24:27]\n"
"ds_read_b128 v[112:115], v1 offset:63488\n"
"v_mfma_f32_16x16x32_bf16 v[24:27], v[116:119], v[76:79], v[24:27]\n"
"s_waitcnt lgkmcnt(5)\n"
"s_add_u32 m0, s91, 124928\n"
"ds_read_b128 v[116:119], v8 offset:3072\n"
"global_load_lds_dwordx4 v1, s[60:61] offset:2048 sc1\n"
"v_mfma_f32_16x16x32_bf16 v[28:31], v[120:123], v[72:75], v[28:31]\n"
"ds_read_b128 v[120:123], v1 offset:64512\n"
"v_mfma_f32_16x16x32_bf16 v[28:31], v[124:127], v[76:79], v[28:31]\n"
"s_waitcnt vmcnt(9) lgkmcnt(5)\n"
"ds_read_b128 v[124:127], v8 offset:4096\n"
"v_mfma_f32_16x16x32_bf16 v[12:15], v[128:131], v[80:83], v[12:15]\n"
"ds_read_b128 v[128:131], v8 offset:0\n"
"v_mfma_f32_16x16x32_bf16 v[12:15], v[132:135], v[84:87], v[12:15]\n"
"s_waitcnt lgkmcnt(5)\n"
"s_mov_b64 exec, 0xff\n"
"global_atomic_add v174, v0, s[4:5]\n"
"s_mov_b64 exec, -1\n"
"ds_read_b128 v[132:135], v8 offset:5120\n"
"v_mfma_f32_16x16x32_bf16 v[16:19], v[136:139], v[80:83], v[16:19]\n"
"ds_read_b128 v[136:139], v8 offset:6144\n"
"v_mfma_f32_16x16x32_bf16 v[16:19], v[140:143], v[84:87], v[16:19]\n"
"s_waitcnt lgkmcnt(5)\n"
"ds_read_b128 v[140:143], v8 offset:11264\n"
"v_mfma_f32_16x16x32_bf16 v[20:23], v[112:115], v[80:83], v[20:23]\n"
"ds_read_b128 v[112:115], v8 offset:7168\n"
"v_mfma_f32_16x16x32_bf16 v[20:23], v[116:119], v[84:87], v[20:23]\n"
"s_waitcnt lgkmcnt(5)\n"
"s_add_u32 m0, s91, 128000\n"
"ds_read_b128 v[116:119], v8 offset:12288\n"
"global_load_lds_dwordx4 v1, s[60:61] offset:3072 sc1\n"
"v_mfma_f32_16x16x32_bf16 v[24:27], v[120:123], v[80:83], v[24:27]\n"
"ds_read_b128 v[120:123], v8 offset:8192\n"
"v_mfma_f32_16x16x32_bf16 v[24:27], v[124:127], v[84:87], v[24:27]\n"
"s_waitcnt lgkmcnt(5)\n"
"ds_read_b128 v[124:127], v8 offset:13312\n"
"v_mfma_f32_16x16x32_bf16 v[28:31], v[128:131], v[80:83], v[28:31]\n"
"ds_read_b128 v[128:131], v8 offset:9216\n"
"v_mfma_f32_16x16x32_bf16 v[28:31], v[132:135], v[84:87], v[28:31]\n"
"s_waitcnt vmcnt(9) lgkmcnt(5)\n"
"s_add_u32 s60, s60, 4096\n"
"s_addc_u32 s61, s61, 0\n"
"s_add_u32 m0, s91, 135168\n"
"ds_read_b128 v[132:135], v8 offset:14336\n"
"global_load_lds_dwordx4 v1, s[60:61] offset:0 sc1\n"
"v_mfma_f32_16x16x32_bf16 v[12:15], v[136:139], v[88:91], v[12:15]\n"
"ds_read_b128 v[136:139], v8 offset:10240\n"
"v_mfma_f32_16x16x32_bf16 v[12:15], v[140:143], v[92:95], v[12:15]\n"
"s_waitcnt lgkmcnt(5)\n"
"ds_read_b128 v[140:143], v8 offset:15360\n"
"v_mfma_f32_16x16x32_bf16 v[16:19], v[112:115], v[88:91], v[16:19]\n"
"ds_read_b128 v[112:115], v8 offset:16384\n"
"v_mfma_f32_16x16x32_bf16 v[16:19], v[116:119], v[92:95], v[16:19]\n"
"s_waitcnt lgkmcnt(5)\n"
"s_add_u32 m0, s91, 138240\n"
"ds_read_b128 v[116:119], v8 offset:21504\n"
"global_load_lds_dwordx4 v1, s[60:61] offset:1024 sc1\n"
"v_mfma_f32_16x16x32_bf16 v[20:23], v[120:123], v[88:91], v[20:23]\n"
"ds_read_b128 v[120:123], v8 offset:17408\n"
"v_mfma_f32_16x16x32_bf16 v[20:23], v[124:127], v[92:95], v[20:23]\n"
"s_waitcnt lgkmcnt(5)\n"
"ds_read_b128 v[124:127], v8 offset:22528\n"
"v_mfma_f32_16x16x32_bf16 v[24:27], v[128:131], v[88:91], v[24:27]\n"
"ds_read_b128 v[128:131], v8 offset:18432\n"
"v_mfma_f32_16x16x32_bf16 v[24:27], v[132:135], v[92:95], v[24:27]\n"
"s_waitcnt lgkmcnt(5)\n"
"ds_read_b128 v[132:135], v8 offset:23552\n"
"v_mfma_f32_16x16x32_bf16 v[28:31], v[136:139], v[88:91], v[28:31]\n"
"ds_read_b128 v[136:139], v8 offset:19456\n"
"v_mfma_f32_16x16x32_bf16 v[28:31], v[140:143], v[92:95], v[28:31]\n"
"s_waitcnt vmcnt(9) lgkmcnt(5)\n"
"ds_read_b128 v[140:143], v8 offset:24576\n"
"v_mfma_f32_16x16x32_bf16 v[12:15], v[112:115], v[96:99], v[12:15]\n"
"ds_read_b128 v[112:115], v8 offset:20480\n"
"v_mfma_f32_16x16x32_bf16 v[12:15], v[116:119], v[100:103], v[12:15]\n"
"s_waitcnt lgkmcnt(5)\n"
"ds_read_b128 v[116:119], v8 offset:25600\n"
"v_mfma_f32_16x16x32_bf16 v[16:19], v[120:123], v[96:99], v[16:19]\n"
"ds_read_b128 v[120:123], v8 offset:26624\n"
"v_mfma_f32_16x16x32_bf16 v[16:19], v[124:127], v[100:103], v[16:19]\n"
"s_waitcnt lgkmcnt(5)\n"
"ds_read_b128 v[124:127], v8 offset:31744\n"
"v_mfma_f32_16x16x32_bf16 v[20:23], v[128:131], v[96:99], v[20:23]\n"
"ds_read_b128 v[128:131], v8 offset:27648\n"
"v_mfma_f32_16x16x32_bf16 v[20:23], v[132:135], v[100:103], v[20:23]\n"
"s_waitcnt lgkmcnt(5)\n"
"ds_read_b128 v[132:135], v8 offset:32768\n"
"v_mfma_f32_16x16x32_bf16 v[24:27], v[136:139], v[96:99], v[24:27]\n"
"ds_read_b128 v[136:139], v8 offset:28672\n"
"v_mfma_f32_16x16x32_bf16 v[24:27], v[140:143], v[100:103], v[24:27]\n"
"s_waitcnt lgkmcnt(5)\n"
"ds_read_b128 v[140:143], v8 offset:33792\n"
"v_mfma_f32_16x16x32_bf16 v[28:31], v[112:115], v[96:99], v[28:31]\n"
"ds_read_b128 v[112:115], v8 offset:29696\n"
"v_mfma_f32_16x16x32_bf16 v[28:31], v[116:119], v[100:103], v[28:31]\n"
"s_waitcnt vmcnt(7) lgkmcnt(5)\n"
"ds_read_b128 v[116:119], v8 offset:34816\n"
"v_mfma_f32_16x16x32_bf16 v[12:15], v[120:123], v[104:107], v[12:15]\n"
"ds_read_b128 v[120:123], v8 offset:30720\n"
"v_mfma_f32_16x16x32_bf16 v[12:15], v[124:127], v[108:111], v[12:15]\n"
"s_waitcnt lgkmcnt(5)\n"
"ds_read_b128 v[124:127], v8 offset:35840\n"
"v_mfma_f32_16x16x32_bf16 v[16:19], v[128:131], v[104:107], v[16:19]\n"
"v_mfma_f32_16x16x32_bf16 v[16:19], v[132:135], v[108:111], v[16:19]\n"
"s_waitcnt lgkmcnt(4)\n"
"v_mfma_f32_16x16x32_bf16 v[20:23], v[136:139], v[104:107], v[20:23]\n"
"v_mfma_f32_16x16x32_bf16 v[20:23], v[140:143], v[108:111], v[20:23]\n"
"s_waitcnt lgkmcnt(2)\n"
"v_mfma_f32_16x16x32_bf16 v[24:27], v[112:115], v[104:107], v[24:27]\n"
"v_mfma_f32_16x16x32_bf16 v[24:27], v[116:119], v[108:111], v[24:27]\n"
"s_waitcnt lgkmcnt(0)\n"
"v_cmp_class_f32 vcc, v12, v172\n"
"v_mfma_f32_16x16x32_bf16 v[28:31], v[120:123], v[104:107], v[28:31]\n"
"v_mfma_f32_16x16x32_bf16 v[28:31], v[124:127], v[108:111], v[28:31]\n"
"s_nop 2\n"
"s_cbranch_vccnz Lsb45_%=\n"
"v_add_u32 v9, 131072, v174\n"
"v_add_u32 v3, 131072, v3\n"
"v_mov_b32 v0, 1\n"
"v_cvt_pk_bf16_f32 v32, v12, v13\n"
"v_cvt_pk_bf16_f32 v33, v14, v15\n"
"v_cvt_pk_bf16_f32 v34, v16, v17\n"
"v_cvt_pk_bf16_f32 v35, v18, v19\n"
"v_cvt_pk_bf16_f32 v36, v20, v21\n"
"v_cvt_pk_bf16_f32 v37, v22, v23\n"
"v_cvt_pk_bf16_f32 v38, v24, v25\n"
"v_cvt_pk_bf16_f32 v39, v26, v27\n"
"buffer_store_dwordx4 v[32:35], v1, s[12:15], s54 offen sc1\n"
"buffer_store_dwordx4 v[36:39], v1, s[12:15], s54 offen offset:1024 sc1\n"
"v_pk_add_f32 v[44:45], v[12:13], v[14:15]\n"
"v_pk_mul_f32 v[46:47], v[12:13], v[12:13]\n"
"v_pk_fma_f32 v[46:47], v[14:15], v[14:15], v[46:47]\n"
"v_pk_add_f32 v[44:45], v[44:45], v[16:17]\n"
"v_pk_fma_f32 v[46:47], v[16:17], v[16:17], v[46:47]\n"
"v_pk_add_f32 v[44:45], v[44:45], v[18:19]\n"
"v_pk_fma_f32 v[46:47], v[18:19], v[18:19], v[46:47]\n"
"v_pk_add_f32 v[44:45], v[44:45], v[20:21]\n"
"v_pk_fma_f32 v[46:47], v[20:21], v[20:21], v[46:47]\n"
"v_pk_add_f32 v[44:45], v[44:45], v[22:23]\n"
"v_pk_fma_f32 v[46:47], v[22:23], v[22:23], v[46:47]\n"
"v_pk_add_f32 v[44:45], v[44:45], v[24:25]\n"
"v_pk_fma_f32 v[46:47], v[24:25], v[24:25], v[46:47]\n"
"v_pk_add_f32 v[44:45], v[44:45], v[26:27]\n"
"v_pk_fma_f32 v[46:47], v[26:27], v[26:27], v[46:47]\n"
"v_pk_add_f32 v[44:45], v[44:45], v[28:29]\n"
"v_pk_fma_f32 v[46:47], v[28:29], v[28:29], v[46:47]\n"
"v_pk_add_f32 v[44:45], v[44:45], v[30:31]\n"
"v_pk_fma_f32 v[46:47], v[30:31], v[30:31], v[46:47]\n"
"v_add_f32 v42, v44, v45\n"
"v_add_f32 v43, v46, v47\n"
"v_cvt_pk_bf16_f32 v40, v28, v29\n"
"v_cvt_pk_bf16_f32 v41, v30, v31\n"
"v_permlane16_swap_b32 v42, v43\n"
"v_add_f32 v42, v42, v43\n"
"v_mov_b32 v43, v42\n"
"s_nop 1\n"
"v_permlane32_swap_b32 v42, v43\n"
"v_add_f32 v42, v42, v43\n"
"v_mov_b32 v43, v42\n"
"s_nop 1\n"
"v_permlane16_swap_b32 v42, v43\n"
"s_nop 1\n"
"v_permlane32_swap_b32 v40, v42\n"
"v_permlane32_swap_b32 v41, v43\n"
"buffer_store_dwordx4 v[40:43], v5, s[12:15], s30 offen sc1\n"
"s_mov_b64 exec, 0xff\n"
"global_atomic_add v9, v0, s[4:5]\n"
"s_mov_b64 exec, 1\n"
"global_atomic_add v104, v3, v2, s[4:5] sc0\n"
"s_mov_b64 exec, -1\n"
"s_mul_i32 s46, s21, 160\n"
"v_lshrrev_b32 v64, 4, v4\n"
"v_lshlrev_b32 v65, 4, v64\n"
"v_sub_u32 v64, v10, v6\n"
"v_add_u32 v64, v64, v65\n"
"buffer_load_dwordx4 v[48:51], v65, s[92:95], s46 offen\n"
"buffer_load_dwordx4 v[52:55], v65, s[92:95], s46 offen offset:64\n"
"buffer_load_dwordx2 v[56:57], v64, s[92:95], s46 offen\n"
"s_barrier\n"
"s_add_u32 s46, s91, 90112\n"
"v_add_u32 v127, s46, v1\n"
"v_add_u32 v126, 0x10000, v127\n"
"s_add_u32 m0, s91, 141312\n"
"s_nop 0\n"
"global_load_lds_dwordx4 v1, s[60:61] offset:2048 sc1\n"
"s_add_u32 m0, s91, 144384\n"
"s_nop 0\n"
"global_load_lds_dwordx4 v1, s[60:61] offset:3072 sc1\n"
"s_mov_b64 exec, 1\n"
"global_atomic_add v105, v3, v2, s[4:5] sc0\n"
"s_mov_b64 exec, -1\n"
"s_add_u32 s60, s60, 4096\n"
"s_addc_u32 s61, s61, 0\n"
"s_add_u32 m0, s91, 151552\n"
"s_nop 0\n"
"global_load_lds_dwordx4 v1, s[60:61] offset:0 sc1\n"
"s_add_u32 m0, s91, 154624\n"
"s_nop 0\n"
"global_load_lds_dwordx4 v1, s[60:61] offset:1024 sc1\n"
"s_add_u32 m0, s91, 157696\n"
"s_nop 0\n"
"global_load_lds_dwordx4 v1, s[60:61] offset:2048 sc1\n"
"s_mov_b64 exec, 1\n"
"global_atomic_add v106, v3, v2, s[4:5] sc0\n"
"s_mov_b64 exec, -1\n"
"s_add_u32 m0, s91, 87040\n"
"s_nop 0\n"
"global_load_lds_dwordx4 v1, s[60:61] offset:3072 sc1\n"
"s_add_u32 s60, s60, 4096\n"
"s_addc_u32 s61, s61, 0\n"
"s_add_u32 m0, s91, 94208\n"
"s_nop 0\n"
"global_load_lds_dwordx4 v1, s[60:61] offset:0 sc1\n"
"s_add_u32 m0, s91, 97280\n"
"s_nop 0\n"
"global_load_lds_dwordx4 v1, s[60:61] offset:1024 sc1\n"
"s_mov_b64 exec, 0\n"
"s_cmp_eq_u32 s22, 0\n"
"s_cbranch_scc1 Lpad13_%=\n"
"s_cmp_eq_u32 s22, 1\n"
"s_cbranch_scc1 Lpad14_%=\n"
"s_cmp_eq_u32 s22, 2\n"
"s_cbranch_scc1 Lpadback3_%=\n"
"s_branch Lpadback3_%=\n"
"Lpadback3_%=:\n"
"s_mov_b64 exec, -1\n"
"s_mov_b64 exec, 1\n"
"global_atomic_add v107, v3, v2, s[4:5] sc0\n"
"s_mov_b64 exec, -1\n"
"s_add_u32 s64, s4, s30\n"
"s_addc_u32 s65, s5, 0\n"
"s_mul_i32 s51, s22, 21504\n"
"s_and_b32 s52, s22, 1\n"
"s_mul_i32 s53, s52, 43008\n"
"s_add_u32 s54, s53, 0\n"
"v_add_u32 v120, s54, v1\n"
"s_sub_u32 s54, 43008, s53\n"
"v_add_u32 v121, s54, v1\n"
"s_mul_i32 s54, s34, 14336\n"
"s_mul_i32 s55, s20, 221184\n"
"Lpad8_%=:\n"
"s_cbranch_execz Lpadback1_%=\n"
"s_add_u32 s54, s54, s55\n"
"s_add_u32 s54, s54, s31\n"
"s_mul_i32 s53, s52, 110592\n"
"s_add_u32 s55, s54, s53\n"
"s_sub_u32 s56, s54, s53\n"
"s_add_u32 s56, s56, 110592\n"
"s_lshl_b32 s53, s35, 2\n"
"s_add_u32 s53, s53, s22\n"
"s_lshl_b32 s53, s53, 10\n"
"s_add_u32 s84, s55, s53\n"
"s_add_u32 s85, s84, 55296\n"
"s_add_u32 s86, s56, s53\n"
"s_add_u32 s87, s86, 55296\n"
"s_cmp_lt_u32 s21, 6\n"
"s_cselect_b32 s54, 3, 2\n"
"s_mul_i32 s54, s54, s35\n"
"s_add_u32 s54, s54, 8\n"
"s_lshl_b32 s54, s54, 10\n"
"s_lshl_b32 s53, s22, 9\n"
"s_add_u32 s53, s53, s54\n"
"s_add_u32 s88, s55, s53\n"
"s_add_u32 s90, s56, s53\n"
"s_lshr_b32 s53, s22, 1\n"
"s_lshl_b32 s53, s53, 9\n"
"s_add_u32 s53, s53, s54\n"
"s_add_u32 s53, s53, 2048\n"
"s_add_u32 s89, s55, s53\n"
"s_cmp_lt_u32 s21, 6\n"
"s_cselect_b32 s89, s89, 2147418112\n"
"s_lshl_b32 s44, s20, 2\n"
"s_add_u32 s44, s44, s34\n"
"s_lshl_b32 s44, s44, 3\n"
"s_lshl_b32 s44, s44, 7\n"
"s_add_u32 s44, s44, s27\n"
"s_add_u32 s44, s44, 262144\n"
"v_lshl_add_u32 v123, v4, 7, s44\n"
"s_lshl_b32 s44, s20, 2\n"
"s_add_u32 s44, s44, s22\n"
"s_lshl_b32 s44, s44, 3\n"
"s_add_u32 s44, s44, s21\n"
"s_lshl_b32 s44, s44, 7\n"
"s_add_u32 s44, s44, s27\n"
"s_add_u32 s44, s44, 262144\n"
"v_mov_b32 v124, s44\n"
"v_mov_b32 v0, 1\n"
"v_add_u32 v122, 54784, v1\n"
"v_cndmask_b32 v122, v1, v122, s[36:37]\n"
"s_add_u32 s46, s51, 20480\n"
"v_add_u32 v136, s46, v1\n"
"v_and_b32 v139, 0xf0, v1\n"
"s_lshl_b32 s46, s22, 8\n"
"s_add_u32 s46, s46, 86016\n"
"v_add_u32 v137, s46, v139\n"
"s_lshl_b32 s46, s52, 9\n"
"s_add_u32 s53, s46, 86016\n"
"v_add_u32 v138, s53, v139\n"
"s_sub_u32 s53, 86528, s46\n"
"v_add_u32 v139, s53, v139\n"
"s_mov_b64 exec, 1\n"
"s_waitcnt vmcnt(14)\n"
"v_readfirstlane_b32 s45, v104\n"
"s_nop 0\n"
"s_and_b32 s45, s45, 255\n"
"s_cmp_ge_u32 s45, 8\n"
"s_cbranch_scc1 Ldone47_%=\n"
"global_atomic_add v104, v3, v2, s[4:5] sc0\n"
"s_waitcnt vmcnt(9)\n"
"v_readfirstlane_b32 s45, v105\n"
"s_nop 0\n"
"s_and_b32 s45, s45, 255\n"
"s_cmp_ge_u32 s45, 8\n"
"s_cbranch_scc1 Ldone47_%=\n"
"global_atomic_add v105, v3, v2, s[4:5] sc0\n"
"s_waitcnt vmcnt(6)\n"
"v_readfirstlane_b32 s45, v106\n"
"s_nop 0\n"
"s_and_b32 s45, s45, 255\n"
"s_cmp_ge_u32 s45, 8\n"
"s_cbranch_scc1 Ldone47_%=\n"
"global_atomic_add v106, v3, v2, s[4:5] sc0\n"
"s_waitcnt vmcnt(3)\n"
"v_readfirstlane_b32 s45, v107\n"
"s_nop 0\n"
"s_and_b32 s45, s45, 255\n"
"s_cmp_ge_u32 s45, 8\n"
"s_cbranch_scc1 Ldone47_%=\n"
"global_atomic_add v107, v3, v2, s[4:5] sc0\n"
"Lpoll46_%=:\n"
"s_waitcnt vmcnt(3)\n"
"v_readfirstlane_b32 s45, v104\n"
"s_nop 0\n"
"s_and_b32 s45, s45, 255\n"
"s_cmp_ge_u32 s45, 8\n"
"s_cbranch_scc1 Ldone47_%=\n"
"global_atomic_add v104, v3, v2, s[4:5] sc0\n"
"s_waitcnt vmcnt(3)\n"
"v_readfirstlane_b32 s45, v105\n"
"s_nop 0\n"
"s_and_b32 s45, s45, 255\n"
"s_cmp_ge_u32 s45, 8\n"
"s_cbranch_scc1 Ldone47_%=\n"
"global_atomic_add v105, v3, v2, s[4:5] sc0\n"
"s_waitcnt vmcnt(3)\n"
"v_readfirstlane_b32 s45, v106\n"
"s_nop 0\n"
"s_and_b32 s45, s45, 255\n"
"s_cmp_ge_u32 s45, 8\n"
"s_cbranch_scc1 Ldone47_%=\n"
"global_atomic_add v106, v3, v2, s[4:5] sc0\n"
"s_waitcnt vmcnt(3)\n"
"v_readfirstlane_b32 s45, v107\n"
"s_nop 0\n"
"s_and_b32 s45, s45, 255\n"
"s_cmp_ge_u32 s45, 8\n"
"s_cbranch_scc1 Ldone47_%=\n"
"global_atomic_add v107, v3, v2, s[4:5] sc0\n"
"s_add_u32 s25, s25, 4\n"
"s_cmp_lt_u32 s25, 1048576\n"
"s_cbranch_scc1 Lpoll46_%=\n"
"Ldone47_%=:\n"
"s_mov_b64 exec, -1\n"
"s_add_u32 s46, s30, 20480\n"
"buffer_load_dwordx2 v[112:113], v1, s[12:15], s46 offen sc1\n"
"buffer_load_dwordx2 v[114:115], v1, s[12:15], s46 offen offset:1024 sc1\n"
"s_add_u32 m0, s51, 0\n"
"s_nop 0\n"
"global_load_lds_dwordx4 v1, s[64:65] offset:0 sc1\n"
"global_load_lds_dwordx4 v1, s[64:65] offset:1024 sc1\n"
"global_load_lds_dwordx4 v1, s[64:65] offset:2048 sc1\n"
"global_load_lds_dwordx4 v1, s[64:65] offset:3072 sc1\n"
"s_add_u32 m0, s51, 4096\n"
"s_add_u32 s64, s64, 4096\n"
"s_addc_u32 s65, s65, 0\n"
"global_load_lds_dwordx4 v1, s[64:65] offset:0 sc1\n"
"global_load_lds_dwordx4 v1, s[64:65] offset:1024 sc1\n"
"global_load_lds_dwordx4 v1, s[64:65] offset:2048 sc1\n"
"global_load_lds_dwordx4 v1, s[64:65] offset:3072 sc1\n"
"s_waitcnt vmcnt(23)\n"
"v_lshlrev_b32 v64, 16, v48\n"
"v_and_b32 v65, 0xffff0000, v48\n"
"v_pk_add_f32 v[12:13], v[12:13], v[64:65]\n"
"v_lshlrev_b32 v64, 16, v49\n"
"v_and_b32 v65, 0xffff0000, v49\n"
"v_pk_add_f32 v[14:15], v[14:15], v[64:65]\n"
"v_lshlrev_b32 v64, 16, v50\n"
"v_and_b32 v65, 0xffff0000, v50\n"
"v_pk_add_f32 v[16:17], v[16:17], v[64:65]\n"
"v_lshlrev_b32 v64, 16, v51\n"
"v_and_b32 v65, 0xffff0000, v51\n"
"v_pk_add_f32 v[18:19], v[18:19], v[64:65]\n"
"s_waitcnt vmcnt(22)\n"
"v_lshlrev_b32 v64, 16, v52\n"
"v_and_b32 v65, 0xffff0000, v52\n"
"v_pk_add_f32 v[20:21], v[20:21], v[64:65]\n"
"v_lshlrev_b32 v64, 16, v53\n"
"v_and_b32 v65, 0xffff0000, v53\n"
"v_pk_add_f32 v[22:23], v[22:23], v[64:65]\n"
"v_lshlrev_b32 v64, 16, v54\n"
"v_and_b32 v65, 0xffff0000, v54\n"
"v_pk_add_f32 v[24:25], v[24:25], v[64:65]\n"
"v_lshlrev_b32 v64, 16, v55\n"
"v_and_b32 v65, 0xffff0000, v55\n"
"v_pk_add_f32 v[26:27], v[26:27], v[64:65]\n"
"s_waitcnt vmcnt(21)\n"
"v_lshlrev_b32 v64, 16, v56\n"
"v_and_b32 v65, 0xffff0000, v56\n"
"v_pk_add_f32 v[28:29], v[28:29], v[64:65]\n"
"v_lshlrev_b32 v64, 16, v57\n"
"v_and_b32 v65, 0xffff0000, v57\n"
"v_pk_add_f32 v[30:31], v[30:31], v[64:65]\n"
"s_mul_i32 s46, s20, 221184\n"
"s_add_u32 s46, s46, s31\n"
"s_mul_i32 s47, s22, 14336\n"
"s_add_u32 s46, s46, s47\n"
"s_add_u32 s47, s22, 0\n"
"s_and_b32 s47, s47, 3\n"
"s_mul_i32 s48, s47, 55296\n"
"s_add_u32 s48, s48, s46\n"
"s_add_u32 s70, s48, 4096\n"
"s_add_u32 s70, s4, s70\n"
"s_addc_u32 s71, s5, 0\n"
"s_add_u32 s48, s48, 12288\n"
"s_cmp_lt_u32 s22, 3\n"
"s_cselect_b32 s8, s48, 2147418112\n"
"s_mul_i32 s47, s47, 15360\n"
"s_add_u32 s47, s47, 102400\n"
"v_add_u32 v172, s47, v1\n"
"s_add_u32 s47, s22, 1\n"
"s_and_b32 s47, s47, 3\n"
"s_mul_i32 s48, s47, 55296\n"
"s_add_u32 s48, s48, s46\n"
"s_add_u32 s72, s48, 4096\n"
"s_add_u32 s72, s4, s72\n"
"s_addc_u32 s73, s5, 0\n"
"s_add_u32 s48, s48, 12288\n"
"s_cmp_lt_u32 s22, 3\n"
"s_cselect_b32 s9, s48, 2147418112\n"
"s_mul_i32 s47, s47, 15360\n"
"s_add_u32 s47, s47, 102400\n"
"v_add_u32 v173, s47, v1\n"
"s_add_u32 s47, s22, 2\n"
"s_and_b32 s47, s47, 3\n"
"s_mul_i32 s48, s47, 55296\n"
"s_add_u32 s48, s48, s46\n"
"s_add_u32 s74, s48, 4096\n"
"s_add_u32 s74, s4, s74\n"
"s_addc_u32 s75, s5, 0\n"
"s_add_u32 s48, s48, 12288\n"
"s_cmp_lt_u32 s22, 3\n"
"s_cselect_b32 s10, s48, 2147418112\n"
"s_mul_i32 s47, s47, 15360\n"
"s_add_u32 s47, s47, 107520\n"
"v_add_u32 v174, s47, v1\n"
"s_add_u32 s47, s22, 3\n"
"s_and_b32 s47, s47, 3\n"
"s_mul_i32 s48, s47, 55296\n"
"s_add_u32 s48, s48, s46\n"
"s_add_u32 s76, s48, 4096\n"
"s_add_u32 s76, s4, s76\n"
"s_addc_u32 s77, s5, 0\n"
"s_add_u32 s48, s48, 12288\n"
"s_cmp_lt_u32 s22, 3\n"
"s_cselect_b32 s11, s48, 2147418112\n"
"s_mul_i32 s47, s47, 15360\n"
"s_add_u32 s47, s47, 112640\n"
"v_add_u32 v125, s47, v1\n"
"s_mul_i32 s79, s22, 25600\n"
"s_mul_i32 s60, s21, 129024\n"
"s_add_u32 s60, s60, s79\n"
"s_add_u32 s60, s60, 6894592\n"
"v_mov_b32 v0, 0x100\n"
"s_mov_b32 s50, 3\n"
"ds_write_b32 v8, v2 offset:21504\n"
"s_waitcnt lgkmcnt(0)\n"
"Lrc48_%=:\n"
"s_waitcnt vmcnt(6)\n"
"s_barrier\n"
"ds_read_b128 v[32:35], v120 offset:0\n"
"ds_read_b128 v[36:39], v120 offset:1024\n"
"ds_read_b128 v[40:43], v120 offset:21504\n"
"ds_read_b128 v[44:47], v120 offset:22528\n"
"ds_read_b128 v[48:51], v121 offset:0\n"
"ds_read_b128 v[52:55], v121 offset:1024\n"
"s_waitcnt lgkmcnt(4)\n"
"v_mfma_f32_16x16x32_bf16 v[64:67], a[168:171], v[32:35], 0\n"
"ds_read_b128 v[56:59], v121 offset:21504\n"
"v_add_f32 v112, v112, v114\n"
"v_mfma_f32_16x16x32_bf16 v[64:67], a[172:175], v[36:39], v[64:67]\n"
"s_add_u32 m0, s51, 8192\n"
"s_add_u32 s64, s64, 4096\n"
"s_addc_u32 s65, s65, 0\n"
"global_load_lds_dwordx4 v1, s[64:65] offset:0 sc1\n"
"v_add_f32 v113, v113, v115\n"
"v_mfma_f32_16x16x32_bf16 v[68:71], a[252:255], v[32:35], 0\n"
"ds_read_b128 v[60:63], v121 offset:22528\n"
"v_permlane16_swap_b32 v112, v113\n"
"v_mfma_f32_16x16x32_bf16 v[68:71], v[176:179], v[36:39], v[68:71]\n"
"s_waitcnt vmcnt(3)\n"
"s_barrier\n"
"s_waitcnt lgkmcnt(4)\n"
"v_add_f32 v112, v112, v113\n"
"v_mov_b32 v113, v112\n"
"v_mfma_f32_16x16x32_bf16 v[72:75], a[168:171], v[40:43], 0\n"
"ds_read_b128 v[32:35], v120 offset:2048\n"
"v_permlane32_swap_b32 v112, v113\n"
"v_mfma_f32_16x16x32_bf16 v[72:75], a[172:175], v[44:47], v[72:75]\n"
"global_load_lds_dwordx4 v1, s[64:65] offset:1024 sc1\n"
"v_add_f32 v112, v112, v113\n"
"v_mfma_f32_16x16x32_bf16 v[76:79], a[252:255], v[40:43], 0\n"
"ds_read_b128 v[36:39], v120 offset:3072\n"
"v_mov_b32 v113, v112\n"
"v_mfma_f32_16x16x32_bf16 v[76:79], v[176:179], v[44:47], v[76:79]\n"
"s_waitcnt lgkmcnt(4)\n"
"v_permlane16_swap_b32 v112, v113\n"
"v_mul_f32 v112, 0xbacccccd, v112\n"
"v_mfma_f32_16x16x32_bf16 v[80:83], a[168:171], v[48:51], 0\n"
"ds_read_b128 v[40:43], v120 offset:23552\n"
"v_mul_f32 v113, 0x3acccccd, v113\n"
"v_mfma_f32_16x16x32_bf16 v[80:83], a[172:175], v[52:55], v[80:83]\n"
"global_load_lds_dwordx4 v1, s[64:65] offset:2048 sc1\n"
"v_fma_f32 v113, -v112, v112, v113\n"
"v_mfma_f32_16x16x32_bf16 v[84:87], a[252:255], v[48:51], 0\n"
"ds_read_b128 v[44:47], v120 offset:24576\n"
"v_max_f32 v114, 0, v113\n"
"v_mfma_f32_16x16x32_bf16 v[84:87], v[176:179], v[52:55], v[84:87]\n"
"s_waitcnt lgkmcnt(4)\n"
"v_fma_f32 v113, v113, 0, v114\n"
"v_add_f32 v114, 0x3727c5ac, v113\n"
"v_mfma_f32_16x16x32_bf16 v[88:91], a[168:171], v[56:59], 0\n"
"ds_read_b128 v[48:51], v121 offset:2048\n"
"v_mfma_f32_16x16x32_bf16 v[88:91], a[172:175], v[60:63], v[88:91]\n"
"global_load_lds_dwordx4 v1, s[64:65] offset:3072 sc1\n"
"v_rsq_f32 v115, v114\n"
"v_mfma_f32_16x16x32_bf16 v[92:95], a[252:255], v[56:59], 0\n"
"ds_read_b128 v[52:55], v121 offset:3072\n"
"v_and_b32 v119, 0xffff0000, v112\n"
"v_mfma_f32_16x16x32_bf16 v[92:95], v[176:179], v[60:63], v[92:95]\n"
"s_waitcnt lgkmcnt(4)\n"
"v_sub_f32 v118, v112, v119\n"
"v_cvt_pk_bf16_f32 v116, v119, v118\n"
"v_mfma_f32_16x16x32_bf16 v[64:67], a[176:179], v[32:35], v[64:67]\n"
"ds_read_b128 v[56:59], v121 offset:23552\n"
"v_mul_f32 v113, v113, v115\n"
"v_mfma_f32_16x16x32_bf16 v[64:67], a[180:183], v[36:39], v[64:67]\n"
"s_add_u32 m0, s51, 12288\n"
"s_add_u32 s64, s64, 4096\n"
"s_addc_u32 s65, s65, 0\n"
"global_load_lds_dwordx4 v1, s[64:65] offset:0 sc1\n"
"v_mul_f32 v113, v113, v115\n"
"v_mfma_f32_16x16x32_bf16 v[68:71], v[180:183], v[32:35], v[68:71]\n"
"ds_read_b128 v[60:63], v121 offset:24576\n"
"v_add_f32 v113, 0x3727c5ac, v113\n"
"v_mfma_f32_16x16x32_bf16 v[68:71], v[184:187], v[36:39], v[68:71]\n"
"s_waitcnt lgkmcnt(4)\n"
"v_rsq_f32 v113, v113\n"
"v_mfma_f32_16x16x32_bf16 v[72:75], a[176:179], v[40:43], v[72:75]\n"
"ds_read_b128 v[32:35], v120 offset:4096\n"
"v_mul_f32 v112, v113, v115\n"
"v_mfma_f32_16x16x32_bf16 v[72:75], a[180:183], v[44:47], v[72:75]\n"
"global_load_lds_dwordx4 v1, s[64:65] offset:1024 sc1\n"
"v_mfma_f32_16x16x32_bf16 v[76:79], v[180:183], v[40:43], v[76:79]\n"
"ds_read_b128 v[36:39], v120 offset:5120\n"
"v_mfma_f32_16x16x32_bf16 v[76:79], v[184:187], v[44:47], v[76:79]\n"
"s_waitcnt lgkmcnt(4)\n"
"v_rcp_f32 v113, v112\n"
"s_nop 0\n"
"v_and_b32 v114, 0xffff0000, v113\n"
"v_sub_f32 v115, v113, v114\n"
"v_mfma_f32_16x16x32_bf16 v[80:83], a[176:179], v[48:51], v[80:83]\n"
"ds_read_b128 v[40:43], v120 offset:25600\n"
"v_cvt_pk_bf16_f32 v117, v119, v114\n"
"v_mfma_f32_16x16x32_bf16 v[80:83], a[180:183], v[52:55], v[80:83]\n"
"global_load_lds_dwordx4 v1, s[64:65] offset:2048 sc1\n"
"v_cvt_pk_bf16_f32 v118, v115, v2\n"
"v_mfma_f32_16x16x32_bf16 v[84:87], v[180:183], v[48:51], v[84:87]\n"
"ds_read_b128 v[44:47], v120 offset:26624\n"
"v_mov_b32 v119, 0\n"
"v_mfma_f32_16x16x32_bf16 v[84:87], v[184:187], v[52:55], v[84:87]\n"
"s_waitcnt lgkmcnt(4)\n"
"v_mul_f32 v140, 0xc0135761, v112\n"
"v_mul_f32 v141, v112, v112\n"
"v_mfma_f32_16x16x32_bf16 v[88:91], a[176:179], v[56:59], v[88:91]\n"
"ds_read_b128 v[48:51], v121 offset:4096\n"
"v_mul_f32 v141, v141, v140\n"
"v_mfma_f32_16x16x32_bf16 v[88:91], a[180:183], v[60:63], v[88:91]\n"
"global_load_lds_dwordx4 v1, s[64:65] offset:3072 sc1\n"
"v_mul_f32 v141, 0x3d372713, v141\n"
"v_mfma_f32_16x16x32_bf16 v[92:95], v[180:183], v[56:59], v[92:95]\n"
"ds_read_b128 v[52:55], v121 offset:5120\n"
"v_mov_b32 v142, v113\n"
"v_mfma_f32_16x16x32_bf16 v[92:95], v[184:187], v[60:63], v[92:95]\n"
"s_waitcnt lgkmcnt(4)\n"
"ds_write_b128 v136, v[116:119]\n"
"v_mfma_f32_16x16x32_bf16 v[64:67], a[184:187], v[32:35], v[64:67]\n"
"ds_read_b128 v[56:59], v121 offset:25600\n"
"v_mfma_f32_16x16x32_bf16 v[64:67], a[188:191], v[36:39], v[64:67]\n"
"s_add_u32 m0, s51, 16384\n"
"s_add_u32 s64, s64, 4096\n"
"s_addc_u32 s65, s65, 0\n"
"ds_write_b128 v137, v[140:143]\n"
"global_load_lds_dwordx4 v1, s[64:65] offset:0 sc1\n"
"v_mfma_f32_16x16x32_bf16 v[68:71], v[188:191], v[32:35], v[68:71]\n"
"ds_read_b128 v[60:63], v121 offset:26624\n"
"v_mfma_f32_16x16x32_bf16 v[68:71], v[192:195], v[36:39], v[68:71]\n"
"s_waitcnt vmcnt(5)\n"
"s_barrier\n"
"s_waitcnt lgkmcnt(6)\n"
"v_mfma_f32_16x16x32_bf16 v[72:75], a[184:187], v[40:43], v[72:75]\n"
"ds_read_b128 v[32:35], v120 offset:6144\n"
"v_mfma_f32_16x16x32_bf16 v[72:75], a[188:191], v[44:47], v[72:75]\n"
"global_load_lds_dwordx4 v1, s[64:65] offset:1024 sc1\n"
"v_mfma_f32_16x16x32_bf16 v[76:79], v[188:191], v[40:43], v[76:79]\n"
"ds_read_b128 v[36:39], v120 offset:7168\n"
"v_mfma_f32_16x16x32_bf16 v[76:79], v[192:195], v[44:47], v[76:79]\n"
"s_waitcnt lgkmcnt(6)\n"
"v_mfma_f32_16x16x32_bf16 v[80:83], a[184:187], v[48:51], v[80:83]\n"
"ds_read_b128 v[40:43], v120 offset:27648\n"
"v_mfma_f32_16x16x32_bf16 v[80:83], a[188:191], v[52:55], v[80:83]\n"
"global_load_lds_dwordx4 v1, s[64:65] offset:2048 sc1\n"
"v_mfma_f32_16x16x32_bf16 v[84:87], v[188:191], v[48:51], v[84:87]\n"
"ds_read_b128 v[44:47], v120 offset:28672\n"
"v_mfma_f32_16x16x32_bf16 v[84:87], v[192:195], v[52:55], v[84:87]\n"
"s_waitcnt lgkmcnt(4)\n"
"v_mfma_f32_16x16x32_bf16 v[88:91], a[184:187], v[56:59], v[88:91]\n"
"ds_read_b128 v[48:51], v121 offset:6144\n"
"v_mfma_f32_16x16x32_bf16 v[88:91], a[188:191], v[60:63], v[88:91]\n"
"global_load_lds_dwordx4 v1, s[64:65] offset:3072 sc1\n"
"v_mfma_f32_16x16x32_bf16 v[92:95], v[188:191], v[56:59], v[92:95]\n"
"ds_read_b128 v[52:55], v121 offset:7168\n"
"v_mfma_f32_16x16x32_bf16 v[92:95], v[192:195], v[60:63], v[92:95]\n"
"s_waitcnt lgkmcnt(4)\n"
"v_mfma_f32_16x16x32_bf16 v[64:67], a[192:195], v[32:35], v[64:67]\n"
"ds_read_b128 v[56:59], v121 offset:27648\n"
"v_mfma_f32_16x16x32_bf16 v[64:67], a[196:199], v[36:39], v[64:67]\n"
"ds_read_b128 a[168:171], v127 offset:12288\n"
"v_mfma_f32_16x16x32_bf16 v[68:71], v[196:199], v[32:35], v[68:71]\n"
"ds_read_b128 v[60:63], v121 offset:28672\n"
"v_mfma_f32_16x16x32_bf16 v[68:71], v[200:203], v[36:39], v[68:71]\n"
"s_waitcnt lgkmcnt(5)\n"
"v_mfma_f32_16x16x32_bf16 v[72:75], a[192:195], v[40:43], v[72:75]\n"
"ds_read_b128 v[32:35], v120 offset:8192\n"
"v_mfma_f32_16x16x32_bf16 v[72:75], a[196:199], v[44:47], v[72:75]\n"
"ds_read_b128 a[172:175], v127 offset:16384\n"
"v_mfma_f32_16x16x32_bf16 v[76:79], v[196:199], v[40:43], v[76:79]\n"
"ds_read_b128 v[36:39], v120 offset:9216\n"
"v_mfma_f32_16x16x32_bf16 v[76:79], v[200:203], v[44:47], v[76:79]\n"
"s_waitcnt lgkmcnt(6)\n"
"v_mfma_f32_16x16x32_bf16 v[80:83], a[192:195], v[48:51], v[80:83]\n"
"ds_read_b128 v[40:43], v120 offset:29696\n"
"v_mfma_f32_16x16x32_bf16 v[80:83], a[196:199], v[52:55], v[80:83]\n"
"ds_read_b128 a[252:255], v127 offset:20480\n"
"v_mfma_f32_16x16x32_bf16 v[84:87], v[196:199], v[48:51], v[84:87]\n"
"ds_read_b128 v[44:47], v120 offset:30720\n"
"v_mfma_f32_16x16x32_bf16 v[84:87], v[200:203], v[52:55], v[84:87]\n"
"s_waitcnt lgkmcnt(6)\n"
"s_add_u32 s66, s66, 24576\n"
"s_addc_u32 s67, s67, 0\n"
"global_load_dwordx4 a[176:179], v1, s[66:67] offset:-3072 sc1\n"
"v_mfma_f32_16x16x32_bf16 v[88:91], a[192:195], v[56:59], v[88:91]\n"
"ds_read_b128 v[48:51], v121 offset:8192\n"
"v_mfma_f32_16x16x32_bf16 v[88:91], a[196:199], v[60:63], v[88:91]\n"
"ds_read_b128 a[180:183], v127 offset:24576\n"
"v_mfma_f32_16x16x32_bf16 v[92:95], v[196:199], v[56:59], v[92:95]\n"
"ds_read_b128 v[52:55], v121 offset:9216\n"
"v_mfma_f32_16x16x32_bf16 v[92:95], v[200:203], v[60:63], v[92:95]\n"
"s_waitcnt lgkmcnt(6)\n"
"global_load_dwordx4 a[184:187], v1, s[66:67] offset:-2048 sc1\n"
"v_mfma_f32_16x16x32_bf16 v[64:67], a[200:203], v[32:35], v[64:67]\n"
"ds_read_b128 v[56:59], v121 offset:29696\n"
"v_mfma_f32_16x16x32_bf16 v[64:67], a[204:207], v[36:39], v[64:67]\n"
"ds_read_b128 a[188:191], v127 offset:28672\n"
"v_mfma_f32_16x16x32_bf16 v[68:71], v[204:207], v[32:35], v[68:71]\n"
"ds_read_b128 v[60:63], v121 offset:30720\n"
"v_mfma_f32_16x16x32_bf16 v[68:71], v[208:211], v[36:39], v[68:71]\n"
"s_waitcnt lgkmcnt(6)\n"
"v_mfma_f32_16x16x32_bf16 v[72:75], a[200:203], v[40:43], v[72:75]\n"
"ds_read_b128 v[32:35], v120 offset:10240\n"
"v_mfma_f32_16x16x32_bf16 v[72:75], a[204:207], v[44:47], v[72:75]\n"
"ds_read_b128 v[192:195], v127 offset:32768\n"
"v_mfma_f32_16x16x32_bf16 v[76:79], v[204:207], v[40:43], v[76:79]\n"
"ds_read_b128 v[36:39], v120 offset:11264\n"
"v_mfma_f32_16x16x32_bf16 v[76:79], v[208:211], v[44:47], v[76:79]\n"
"s_waitcnt lgkmcnt(6)\n"
"global_load_dwordx4 a[192:195], v1, s[66:67] offset:-1024 sc1\n"
"v_mfma_f32_16x16x32_bf16 v[80:83], a[200:203], v[48:51], v[80:83]\n"
"ds_read_b128 v[40:43], v120 offset:31744\n"
"v_mfma_f32_16x16x32_bf16 v[80:83], a[204:207], v[52:55], v[80:83]\n"
"ds_read_b128 a[196:199], v127 offset:36864\n"
"v_mfma_f32_16x16x32_bf16 v[84:87], v[204:207], v[48:51], v[84:87]\n"
"ds_read_b128 v[44:47], v120 offset:32768\n"
"v_mfma_f32_16x16x32_bf16 v[84:87], v[208:211], v[52:55], v[84:87]\n"
"s_waitcnt lgkmcnt(6)\n"
"v_mfma_f32_16x16x32_bf16 v[88:91], a[200:203], v[56:59], v[88:91]\n"
"ds_read_b128 v[48:51], v121 offset:10240\n"
"v_mfma_f32_16x16x32_bf16 v[88:91], a[204:207], v[60:63], v[88:91]\n"
"ds_read_b128 v[196:199], v127 offset:40960\n"
"v_mfma_f32_16x16x32_bf16 v[92:95], v[204:207], v[56:59], v[92:95]\n"
"ds_read_b128 v[52:55], v121 offset:11264\n"
"v_mfma_f32_16x16x32_bf16 v[92:95], v[208:211], v[60:63], v[92:95]\n"
"s_waitcnt lgkmcnt(6)\n"
"global_load_dwordx4 v[200:203], v1, s[66:67] offset:0 sc1\n"
"v_mfma_f32_16x16x32_bf16 v[64:67], a[208:211], v[32:35], v[64:67]\n"
"ds_read_b128 v[56:59], v121 offset:31744\n"
"v_mfma_f32_16x16x32_bf16 v[64:67], a[212:215], v[36:39], v[64:67]\n"
"ds_read_b128 a[200:203], v127 offset:45056\n"
"v_mfma_f32_16x16x32_bf16 v[68:71], v[212:215], v[32:35], v[68:71]\n"
"ds_read_b128 v[60:63], v121 offset:32768\n"
"v_mfma_f32_16x16x32_bf16 v[68:71], v[216:219], v[36:39], v[68:71]\n"
"s_waitcnt vmcnt(4)\n"
"s_barrier\n"
"s_waitcnt lgkmcnt(6)\n"
"v_mfma_f32_16x16x32_bf16 v[72:75], a[208:211], v[40:43], v[72:75]\n"
"ds_read_b128 v[32:35], v120 offset:12288\n"
"v_mfma_f32_16x16x32_bf16 v[72:75], a[212:215], v[44:47], v[72:75]\n"
"ds_read_b128 a[204:207], v127 offset:49152\n"
"v_mfma_f32_16x16x32_bf16 v[76:79], v[212:215], v[40:43], v[76:79]\n"
"ds_read_b128 v[36:39], v120 offset:13312\n"
"v_mfma_f32_16x16x32_bf16 v[76:79], v[216:219], v[44:47], v[76:79]\n"
"s_waitcnt lgkmcnt(6)\n"
"global_load_dwordx4 v[204:207], v1, s[66:67] offset:1024 sc1\n"
"v_mfma_f32_16x16x32_bf16 v[80:83], a[208:211], v[48:51], v[80:83]\n"
"ds_read_b128 v[40:43], v120 offset:33792\n"
"v_mfma_f32_16x16x32_bf16 v[80:83], a[212:215], v[52:55], v[80:83]\n"
"v_mfma_f32_16x16x32_bf16 v[84:87], v[212:215], v[48:51], v[84:87]\n"
"ds_read_b128 v[44:47], v120 offset:34816\n"
"v_mfma_f32_16x16x32_bf16 v[84:87], v[216:219], v[52:55], v[84:87]\n"
"s_waitcnt lgkmcnt(5)\n"
"global_load_dwordx4 v[208:211], v1, s[66:67] offset:2048 sc1\n"
"v_mfma_f32_16x16x32_bf16 v[88:91], a[208:211], v[56:59], v[88:91]\n"
"ds_read_b128 v[48:51], v121 offset:12288\n"
"v_mfma_f32_16x16x32_bf16 v[88:91], a[212:215], v[60:63], v[88:91]\n"
"v_mfma_f32_16x16x32_bf16 v[92:95], v[212:215], v[56:59], v[92:95]\n"
"ds_read_b128 v[52:55], v121 offset:13312\n"
"v_mfma_f32_16x16x32_bf16 v[92:95], v[216:219], v[60:63], v[92:95]\n"
"s_waitcnt lgkmcnt(4)\n"
"s_mov_b64 exec, 0xff\n"
"global_atomic_add v9, v0, s[4:5]\n"
"s_mov_b64 exec, -1\n"
"v_mov_b32 v0, 1\n"
"v_mfma_f32_16x16x32_bf16 v[64:67], a[216:219], v[32:35], v[64:67]\n"
"ds_read_b128 v[56:59], v121 offset:33792\n"
"v_mfma_f32_16x16x32_bf16 v[64:67], a[220:223], v[36:39], v[64:67]\n"
"ds_read_b128 a[208:211], v127 offset:53248\n"
"v_mfma_f32_16x16x32_bf16 v[68:71], v[220:223], v[32:35], v[68:71]\n"
"ds_read_b128 v[60:63], v121 offset:34816\n"
"v_mfma_f32_16x16x32_bf16 v[68:71], v[224:227], v[36:39], v[68:71]\n"
"s_waitcnt lgkmcnt(5)\n"
"global_load_dwordx4 a[212:215], v1, s[66:67] offset:3072 sc1\n"
"v_mfma_f32_16x16x32_bf16 v[72:75], a[216:219], v[40:43], v[72:75]\n"
"ds_read_b128 v[32:35], v120 offset:14336\n"
"v_mfma_f32_16x16x32_bf16 v[72:75], a[220:223], v[44:47], v[72:75]\n"
"ds_read_b128 v[212:215], v127 offset:57344\n"
"v_mfma_f32_16x16x32_bf16 v[76:79], v[220:223], v[40:43], v[76:79]\n"
"ds_read_b128 v[36:39], v120 offset:15360\n"
"v_mfma_f32_16x16x32_bf16 v[76:79], v[224:227], v[44:47], v[76:79]\n"
"s_waitcnt lgkmcnt(6)\n"
"v_mfma_f32_16x16x32_bf16 v[80:83], a[216:219], v[48:51], v[80:83]\n"
"ds_read_b128 v[40:43], v120 offset:35840\n"
"v_mfma_f32_16x16x32_bf16 v[80:83], a[220:223], v[52:55], v[80:83]\n"
"v_mfma_f32_16x16x32_bf16 v[84:87], v[220:223], v[48:51], v[84:87]\n"
"ds_read_b128 v[44:47], v120 offset:36864\n"
"v_mfma_f32_16x16x32_bf16 v[84:87], v[224:227], v[52:55], v[84:87]\n"
"s_waitcnt lgkmcnt(5)\n"
"s_add_u32 s66, s66, 8192\n"
"s_addc_u32 s67, s67, 0\n"
"global_load_dwordx4 v[216:219], v1, s[66:67] offset:-4096 sc1\n"
"v_mfma_f32_16x16x32_bf16 v[88:91], a[216:219], v[56:59], v[88:91]\n"
"ds_read_b128 v[48:51], v121 offset:14336\n"
"v_mfma_f32_16x16x32_bf16 v[88:91], a[220:223], v[60:63], v[88:91]\n"
"v_mfma_f32_16x16x32_bf16 v[92:95], v[220:223], v[56:59], v[92:95]\n"
"ds_read_b128 v[52:55], v121 offset:15360\n"
"v_mfma_f32_16x16x32_bf16 v[92:95], v[224:227], v[60:63], v[92:95]\n"
"s_waitcnt lgkmcnt(4)\n"
"v_mfma_f32_16x16x32_bf16 v[64:67], a[224:227], v[32:35], v[64:67]\n"
"ds_read_b128 v[56:59], v121 offset:35840\n"
"v_mfma_f32_16x16x32_bf16 v[64:67], a[228:231], v[36:39], v[64:67]\n"
"ds_read_b128 a[216:219], v127 offset:61440\n"
"v_mfma_f32_16x16x32_bf16 v[68:71], v[228:231], v[32:35], v[68:71]\n"
"ds_read_b128 v[60:63], v121 offset:36864\n"
"v_mfma_f32_16x16x32_bf16 v[68:71], v[232:235], v[36:39], v[68:71]\n"
"s_waitcnt lgkmcnt(5)\n"
"global_load_dwordx4 a[220:223], v1, s[66:67] offset:-3072 sc1\n"
"v_mfma_f32_16x16x32_bf16 v[72:75], a[224:227], v[40:43], v[72:75]\n"
"ds_read_b128 v[32:35], v120 offset:16384\n"
"v_mfma_f32_16x16x32_bf16 v[72:75], a[228:231], v[44:47], v[72:75]\n"
"ds_read_b128 v[220:223], v126 offset:0\n"
"v_mfma_f32_16x16x32_bf16 v[76:79], v[228:231], v[40:43], v[76:79]\n"
"ds_read_b128 v[36:39], v120 offset:17408\n"
"v_mfma_f32_16x16x32_bf16 v[76:79], v[232:235], v[44:47], v[76:79]\n"
"s_waitcnt lgkmcnt(6)\n"
"global_load_dwordx4 v[224:227], v1, s[66:67] offset:-2048 sc1\n"
"v_mfma_f32_16x16x32_bf16 v[80:83], a[224:227], v[48:51], v[80:83]\n"
"ds_read_b128 v[40:43], v120 offset:37888\n"
"v_mfma_f32_16x16x32_bf16 v[80:83], a[228:231], v[52:55], v[80:83]\n"
"v_mfma_f32_16x16x32_bf16 v[84:87], v[228:231], v[48:51], v[84:87]\n"
"ds_read_b128 v[44:47], v120 offset:38912\n"
"v_mfma_f32_16x16x32_bf16 v[84:87], v[232:235], v[52:55], v[84:87]\n"
"s_waitcnt lgkmcnt(5)\n"
"v_mfma_f32_16x16x32_bf16 v[88:91], a[224:227], v[56:59], v[88:91]\n"
"ds_read_b128 v[48:51], v121 offset:16384\n"
"v_mfma_f32_16x16x32_bf16 v[88:91], a[228:231], v[60:63], v[88:91]\n"
"v_mfma_f32_16x16x32_bf16 v[92:95], v[228:231], v[56:59], v[92:95]\n"
"ds_read_b128 v[52:55], v121 offset:17408\n"
"v_mfma_f32_16x16x32_bf16 v[92:95], v[232:235], v[60:63], v[92:95]\n"
"s_waitcnt lgkmcnt(4)\n"
"global_load_dwordx4 a[224:227], v1, s[66:67] offset:-1024 sc1\n"
"v_mfma_f32_16x16x32_bf16 v[64:67], a[232:235], v[32:35], v[64:67]\n"
"ds_read_b128 v[56:59], v121 offset:37888\n"
"v_mfma_f32_16x16x32_bf16 v[64:67], a[236:239], v[36:39], v[64:67]\n"
"ds_read_b128 a[228:231], v126 offset:4096\n"
"v_mfma_f32_16x16x32_bf16 v[68:71], v[236:239], v[32:35], v[68:71]\n"
"ds_read_b128 v[60:63], v121 offset:38912\n"
"v_mfma_f32_16x16x32_bf16 v[68:71], v[240:243], v[36:39], v[68:71]\n"
"s_waitcnt lgkmcnt(5)\n"
"v_mfma_f32_16x16x32_bf16 v[72:75], a[232:235], v[40:43], v[72:75]\n"
"ds_read_b128 v[32:35], v120 offset:18432\n"
"v_mfma_f32_16x16x32_bf16 v[72:75], a[236:239], v[44:47], v[72:75]\n"
"ds_read_b128 v[228:231], v127 offset:0\n"
"v_mfma_f32_16x16x32_bf16 v[76:79], v[236:239], v[40:43], v[76:79]\n"
"ds_read_b128 v[36:39], v120 offset:19456\n"
"v_mfma_f32_16x16x32_bf16 v[76:79], v[240:243], v[44:47], v[76:79]\n"
"s_waitcnt lgkmcnt(6)\n"
"global_load_dwordx4 v[232:235], v1, s[66:67] offset:0 sc1\n"
"v_mfma_f32_16x16x32_bf16 v[80:83], a[232:235], v[48:51], v[80:83]\n"
"ds_read_b128 v[40:43], v120 offset:39936\n"
"v_mfma_f32_16x16x32_bf16 v[80:83], a[236:239], v[52:55], v[80:83]\n"
"v_mfma_f32_16x16x32_bf16 v[84:87], v[236:239], v[48:51], v[84:87]\n"
"ds_read_b128 v[44:47], v120 offset:40960\n"
"v_mfma_f32_16x16x32_bf16 v[84:87], v[240:243], v[52:55], v[84:87]\n"
"s_waitcnt lgkmcnt(5)\n"
"v_mfma_f32_16x16x32_bf16 v[88:91], a[232:235], v[56:59], v[88:91]\n"
"ds_read_b128 v[48:51], v121 offset:18432\n"
"v_mfma_f32_16x16x32_bf16 v[88:91], a[236:239], v[60:63], v[88:91]\n"
"v_mfma_f32_16x16x32_bf16 v[92:95], v[236:239], v[56:59], v[92:95]\n"
"ds_read_b128 v[52:55], v121 offset:19456\n"
"v_mfma_f32_16x16x32_bf16 v[92:95], v[240:243], v[60:63], v[92:95]\n"
"s_waitcnt lgkmcnt(4)\n"
"global_load_dwordx4 a[232:235], v1, s[66:67] offset:1024 sc1\n"
"v_mfma_f32_16x16x32_bf16 v[64:67], a[240:243], v[32:35], v[64:67]\n"
"ds_read_b128 v[56:59], v121 offset:39936\n"
"v_mfma_f32_16x16x32_bf16 v[64:67], a[244:247], v[36:39], v[64:67]\n"
"ds_read_b128 v[96:99], v138 offset:0\n"
"v_mfma_f32_16x16x32_bf16 v[68:71], v[244:247], v[32:35], v[68:71]\n"
"ds_read_b128 v[60:63], v121 offset:40960\n"
"v_mfma_f32_16x16x32_bf16 v[68:71], v[248:251], v[36:39], v[68:71]\n"
"s_waitcnt lgkmcnt(5)\n"
"v_mfma_f32_16x16x32_bf16 v[72:75], a[240:243], v[40:43], v[72:75]\n"
"ds_read_b128 v[32:35], v120 offset:20480\n"
"v_mfma_f32_16x16x32_bf16 v[72:75], a[244:247], v[44:47], v[72:75]\n"
"ds_read_b128 v[100:103], v138 offset:256\n"
"v_mfma_f32_16x16x32_bf16 v[76:79], v[244:247], v[40:43], v[76:79]\n"
"ds_read_b128 v[36:39], v120 offset:41984\n"
"v_mfma_f32_16x16x32_bf16 v[76:79], v[248:251], v[44:47], v[76:79]\n"
"s_waitcnt lgkmcnt(6)\n"
"global_load_dwordx4 a[236:239], v1, s[66:67] offset:2048 sc1\n"
"v_mfma_f32_16x16x32_bf16 v[80:83], a[240:243], v[48:51], v[80:83]\n"
"Lpad9_%=:\n"
"s_cbranch_execz Lpadback2_%=\n"
"ds_read_b128 v[40:43], v121 offset:20480\n"
"v_mfma_f32_16x16x32_bf16 v[80:83], a[244:247], v[52:55], v[80:83]\n"
"ds_read_b128 v[104:107], v139 offset:0\n"
"v_mfma_f32_16x16x32_bf16 v[84:87], v[244:247], v[48:51], v[84:87]\n"
"ds_read_b128 v[44:47], v121 offset:41984\n"
"v_mfma_f32_16x16x32_bf16 v[84:87], v[248:251], v[52:55], v[84:87]\n"
"s_waitcnt lgkmcnt(6)\n"
"global_load_dwordx4 v[236:239], v1, s[66:67] offset:3072 sc1\n"
"v_mfma_f32_16x16x32_bf16 v[88:91], a[240:243], v[56:59], v[88:91]\n"
"ds_read_b128 v[48:51], v120 offset:0\n"
"v_mfma_f32_16x16x32_bf16 v[88:91], a[244:247], v[60:63], v[88:91]\n"
"ds_read_b128 v[108:111], v139 offset:256\n"
"v_mfma_f32_16x16x32_bf16 v[92:95], v[244:247], v[56:59], v[92:95]\n"
"ds_read_b128 v[52:55], v120 offset:1024\n"
"v_mfma_f32_16x16x32_bf16 v[92:95], v[248:251], v[60:63], v[92:95]\n"
"s_waitcnt lgkmcnt(6)\n"
"v_mfma_f32_16x16x32_bf16 v[64:67], a[248:251], v[32:35], v[64:67]\n"
"ds_read_b128 v[56:59], v120 offset:21504\n"
"v_mfma_f32_16x16x32_bf16 v[68:71], v[252:255], v[32:35], v[68:71]\n"
"ds_read_b128 v[240:243], v127 offset:4096\n"
"v_mfma_f32_16x16x32_bf16 v[72:75], a[248:251], v[36:39], v[72:75]\n"
"ds_read_b128 v[60:63], v120 offset:22528\n"
"v_mfma_f32_16x16x32_bf16 v[76:79], v[252:255], v[36:39], v[76:79]\n"
"s_waitcnt lgkmcnt(6)\n"
"s_add_u32 s66, s66, 8192\n"
"s_addc_u32 s67, s67, 0\n"
"global_load_dwordx4 a[240:243], v1, s[66:67] offset:-4096 sc1\n"
"v_mfma_f32_16x16x32_bf16 v[80:83], a[248:251], v[40:43], v[80:83]\n"
"ds_read_b128 v[32:35], v120 offset:2048\n"
"v_mfma_f32_16x16x32_bf16 v[84:87], v[252:255], v[40:43], v[84:87]\n"
"ds_read_b128 a[244:247], v127 offset:8192\n"
"v_mfma_f32_16x16x32_bf16 v[88:91], a[248:251], v[44:47], v[88:91]\n"
"ds_read_b128 v[36:39], v120 offset:3072\n"
"v_mfma_f32_16x16x32_bf16 v[92:95], v[252:255], v[44:47], v[92:95]\n"
"s_waitcnt lgkmcnt(6)\n"
"v_mfma_f32_16x16x32_bf16 v[176:179], a[0:3], v[48:51], 0\n"
"v_mfma_f32_16x16x32_bf16 v[176:179], a[4:7], v[52:55], v[176:179]\n"
"ds_read_b128 v[40:43], v120 offset:23552\n"
"v_mfma_f32_16x16x32_bf16 v[180:183], a[84:87], v[48:51], 0\n"
"v_mfma_f32_16x16x32_bf16 v[180:183], a[88:91], v[52:55], v[180:183]\n"
"s_waitcnt lgkmcnt(4)\n"
"ds_read_b128 v[44:47], v120 offset:24576\n"
"v_add_f32 v140, v64, v72\n"
"v_add_f32 v141, v80, v88\n"
"v_add_f32 v140, v140, v141\n"
"v_cmp_class_f32 vcc, v140, s50\n"
"global_load_dwordx4 v[244:247], v1, s[66:67] offset:-3072 sc1\n"
"v_mfma_f32_16x16x32_bf16 v[184:187], a[0:3], v[56:59], 0\n"
"v_mul_f32 v112, v64, v64\n"
"v_mul_f32 v113, v65, v65\n"
"v_mfma_f32_16x16x32_bf16 v[184:187], a[4:7], v[60:63], v[184:187]\n"
"ds_read_b128 v[48:51], v120 offset:4096\n"
"v_mul_f32 v114, v66, v66\n"
"v_mul_f32 v115, v67, v67\n"
"v_fma_f32 v112, v112, v97, v96\n"
"v_fma_f32 v113, v113, v97, v96\n"
"v_fma_f32 v114, v114, v97, v96\n"
"v_mfma_f32_16x16x32_bf16 v[188:191], a[84:87], v[56:59], 0\n"
"v_fma_f32 v115, v115, v97, v96\n"
"v_mul_f32 v112, v112, v64\n"
"v_mfma_f32_16x16x32_bf16 v[188:191], a[88:91], v[60:63], v[188:191]\n"
"s_cbranch_vccnz Lmc50_%=\n"
"Lmb51_%=:\n"
"s_barrier\n"
"s_waitcnt lgkmcnt(3)\n"
"ds_read_b128 v[52:55], v120 offset:5120\n"
"ds_read_b32 v140, v8 offset:21504\n"
"global_load_dwordx4 v[248:251], v1, s[66:67] offset:-2048 sc1\n"
"v_mul_f32 v113, v113, v65\n"
"v_mul_f32 v114, v114, v66\n"
"v_mul_f32 v115, v115, v67\n"
"v_mfma_f32_16x16x32_bf16 v[176:179], a[8:11], v[32:35], v[176:179]\n"
"v_exp_f32 v112, v112\n"
"v_mfma_f32_16x16x32_bf16 v[176:179], a[12:15], v[36:39], v[176:179]\n"
"ds_read_b128 v[56:59], v120 offset:25600\n"
"v_exp_f32 v113, v113\n"
"v_exp_f32 v114, v114\n"
"v_exp_f32 v115, v115\n"
"v_fma_f32 v112, v112, v98, v98\n"
"v_fma_f32 v113, v113, v98, v98\n"
"v_mfma_f32_16x16x32_bf16 v[180:183], a[92:95], v[32:35], v[180:183]\n"
"v_fma_f32 v114, v114, v98, v98\n"
"v_fma_f32 v115, v115, v98, v98\n"
"v_mfma_f32_16x16x32_bf16 v[180:183], a[96:99], v[36:39], v[180:183]\n"
"s_waitcnt lgkmcnt(4)\n"
"ds_read_b128 v[60:63], v120 offset:26624\n"
"global_load_dwordx4 a[248:251], v1, s[66:67] offset:-1024 sc1\n"
"v_rcp_f32 v112, v112\n"
"v_rcp_f32 v113, v113\n"
"v_mfma_f32_16x16x32_bf16 v[184:187], a[8:11], v[40:43], v[184:187]\n"
"v_rcp_f32 v114, v114\n"
"v_mfma_f32_16x16x32_bf16 v[184:187], a[12:15], v[44:47], v[184:187]\n"
"ds_read_b128 v[32:35], v120 offset:6144\n"
"v_rcp_f32 v115, v115\n"
"v_mul_f32 v112, v112, v64\n"
"v_mul_f32 v113, v113, v65\n"
"v_mul_f32 v114, v114, v66\n"
"v_mul_f32 v115, v115, v67\n"
"v_cvt_pk_bf16_f32 v128, v112, v113\n"
"v_mfma_f32_16x16x32_bf16 v[188:191], a[92:95], v[40:43], v[188:191]\n"
"v_cvt_pk_bf16_f32 v129, v114, v115\n"
"v_mul_f32 v116, v68, v68\n"
"v_mfma_f32_16x16x32_bf16 v[188:191], a[96:99], v[44:47], v[188:191]\n"
"s_waitcnt lgkmcnt(3)\n"
"v_readfirstlane_b32 s45, v140\n"
"s_nop 0\n"
"s_cmp_lg_u32 s45, 0\n"
"s_cbranch_scc1 Lsc49_%=\n"
"ds_read_b128 v[36:39], v120 offset:7168\n"
"global_load_dwordx4 v[252:255], v1, s[66:67] offset:0 sc1\n"
"v_mul_f32 v117, v69, v69\n"
"v_mul_f32 v118, v70, v70\n"
"v_mul_f32 v119, v71, v71\n"
"v_fma_f32 v116, v116, v97, v96\n"
"v_fma_f32 v117, v117, v97, v96\n"
"v_fma_f32 v118, v118, v97, v96\n"
"v_mfma_f32_16x16x32_bf16 v[176:179], a[16:19], v[48:51], v[176:179]\n"
"v_fma_f32 v119, v119, v97, v96\n"
"v_mul_f32 v116, v116, v68\n"
"v_mfma_f32_16x16x32_bf16 v[176:179], a[20:23], v[52:55], v[176:179]\n"
"ds_read_b128 v[40:43], v120 offset:27648\n"
"v_mul_f32 v117, v117, v69\n"
"v_mul_f32 v118, v118, v70\n"
"v_mul_f32 v119, v119, v71\n"
"v_exp_f32 v116, v116\n"
"v_mfma_f32_16x16x32_bf16 v[180:183], a[100:103], v[48:51], v[180:183]\n"
"v_exp_f32 v117, v117\n"
"v_mfma_f32_16x16x32_bf16 v[180:183], a[104:107], v[52:55], v[180:183]\n"
"s_waitcnt lgkmcnt(3)\n"
"ds_read_b128 v[44:47], v120 offset:28672\n"
"global_load_dwordx4 a[84:87], v1, s[66:67] offset:1024 sc1\n"
"v_exp_f32 v118, v118\n"
"v_exp_f32 v119, v119\n"
"v_fma_f32 v116, v116, v98, v98\n"
"v_fma_f32 v117, v117, v98, v98\n"
"v_fma_f32 v118, v118, v98, v98\n"
"v_mfma_f32_16x16x32_bf16 v[184:187], a[16:19], v[56:59], v[184:187]\n"
"v_fma_f32 v119, v119, v98, v98\n"
"v_mfma_f32_16x16x32_bf16 v[184:187], a[20:23], v[60:63], v[184:187]\n"
"ds_read_b128 v[48:51], v120 offset:8192\n"
"v_rcp_f32 v116, v116\n"
"v_rcp_f32 v117, v117\n"
"v_rcp_f32 v118, v118\n"
"v_mfma_f32_16x16x32_bf16 v[188:191], a[100:103], v[56:59], v[188:191]\n"
"v_rcp_f32 v119, v119\n"
"v_mfma_f32_16x16x32_bf16 v[188:191], a[104:107], v[60:63], v[188:191]\n"
"s_waitcnt lgkmcnt(3)\n"
"ds_read_b128 v[52:55], v120 offset:9216\n"
"global_load_dwordx4 a[88:91], v1, s[66:67] offset:2048 sc1\n"
"v_mul_f32 v116, v116, v68\n"
"v_mul_f32 v117, v117, v69\n"
"v_mul_f32 v118, v118, v70\n"
"v_mul_f32 v119, v119, v71\n"
"v_cvt_pk_bf16_f32 v130, v116, v117\n"
"v_cvt_pk_bf16_f32 v131, v118, v119\n"
"v_mfma_f32_16x16x32_bf16 v[176:179], a[24:27], v[32:35], v[176:179]\n"
"v_mfma_f32_16x16x32_bf16 v[176:179], a[28:31], v[36:39], v[176:179]\n"
"buffer_store_dwordx4 v[128:131], v1, s[12:15], s84 offen sc1\n"
"ds_read_b128 v[56:59], v120 offset:29696\n"
"v_mul_f32 v112, v72, v72\n"
"v_mul_f32 v113, v73, v73\n"
"v_mul_f32 v114, v74, v74\n"
"v_mul_f32 v115, v75, v75\n"
"v_fma_f32 v112, v112, v101, v100\n"
"v_fma_f32 v113, v113, v101, v100\n"
"v_fma_f32 v114, v114, v101, v100\n"
"v_mfma_f32_16x16x32_bf16 v[180:183], a[108:111], v[32:35], v[180:183]\n"
"v_fma_f32 v115, v115, v101, v100\n"
"v_mul_f32 v112, v112, v72\n"
"v_mfma_f32_16x16x32_bf16 v[180:183], a[112:115], v[36:39], v[180:183]\n"
"s_waitcnt lgkmcnt(3)\n"
"ds_read_b128 v[60:63], v120 offset:30720\n"
"global_load_dwordx4 a[92:95], v1, s[66:67] offset:3072 sc1\n"
"v_mul_f32 v113, v113, v73\n"
"v_mul_f32 v114, v114, v74\n"
"v_mul_f32 v115, v115, v75\n"
"v_exp_f32 v112, v112\n"
"v_mfma_f32_16x16x32_bf16 v[184:187], a[24:27], v[40:43], v[184:187]\n"
"v_exp_f32 v113, v113\n"
"v_mfma_f32_16x16x32_bf16 v[184:187], a[28:31], v[44:47], v[184:187]\n"
"ds_read_b128 v[32:35], v120 offset:10240\n"
"v_exp_f32 v114, v114\n"
"v_exp_f32 v115, v115\n"
"v_fma_f32 v112, v112, v102, v102\n"
"v_fma_f32 v113, v113, v102, v102\n"
"v_mfma_f32_16x16x32_bf16 v[188:191], a[108:111], v[40:43], v[188:191]\n"
"v_fma_f32 v114, v114, v102, v102\n"
"v_fma_f32 v115, v115, v102, v102\n"
"v_mfma_f32_16x16x32_bf16 v[188:191], a[112:115], v[44:47], v[188:191]\n"
"s_waitcnt lgkmcnt(3)\n"
"s_add_u32 s66, s66, 8192\n"
"s_addc_u32 s67, s67, 0\n"
"ds_read_b128 v[36:39], v120 offset:11264\n"
"global_load_dwordx4 a[96:99], v1, s[66:67] offset:-4096 sc1\n"
"v_rcp_f32 v112, v112\n"
"v_rcp_f32 v113, v113\n"
"v_rcp_f32 v114, v114\n"
"v_mfma_f32_16x16x32_bf16 v[176:179], a[32:35], v[48:51], v[176:179]\n"
"v_rcp_f32 v115, v115\n"
"v_mfma_f32_16x16x32_bf16 v[176:179], a[36:39], v[52:55], v[176:179]\n"
"ds_read_b128 v[40:43], v120 offset:31744\n"
"v_mul_f32 v112, v112, v72\n"
"v_mul_f32 v113, v113, v73\n"
"v_mul_f32 v114, v114, v74\n"
"v_mul_f32 v115, v115, v75\n"
"v_cvt_pk_bf16_f32 v132, v112, v113\n"
"v_cvt_pk_bf16_f32 v133, v114, v115\n"
"v_mfma_f32_16x16x32_bf16 v[180:183], a[116:119], v[48:51], v[180:183]\n"
"v_mul_f32 v116, v76, v76\n"
"v_mul_f32 v117, v77, v77\n"
"v_mfma_f32_16x16x32_bf16 v[180:183], a[120:123], v[52:55], v[180:183]\n"
"s_waitcnt lgkmcnt(3)\n"
"ds_read_b128 v[44:47], v120 offset:32768\n"
"global_load_dwordx4 a[100:103], v1, s[66:67] offset:-3072 sc1\n"
"v_mul_f32 v118, v78, v78\n"
"v_mul_f32 v119, v79, v79\n"
"v_fma_f32 v116, v116, v101, v100\n"
"v_fma_f32 v117, v117, v101, v100\n"
"v_fma_f32 v118, v118, v101, v100\n"
"v_mfma_f32_16x16x32_bf16 v[184:187], a[32:35], v[56:59], v[184:187]\n"
"v_fma_f32 v119, v119, v101, v100\n"
"v_mul_f32 v116, v116, v76\n"
"v_mfma_f32_16x16x32_bf16 v[184:187], a[36:39], v[60:63], v[184:187]\n"
"ds_read_b128 v[48:51], v120 offset:12288\n"
"v_mul_f32 v117, v117, v77\n"
"v_mul_f32 v118, v118, v78\n"
"v_mul_f32 v119, v119, v79\n"
"v_exp_f32 v116, v116\n"
"v_exp_f32 v117, v117\n"
"v_mfma_f32_16x16x32_bf16 v[188:191], a[116:119], v[56:59], v[188:191]\n"
"v_exp_f32 v118, v118\n"
"v_mfma_f32_16x16x32_bf16 v[188:191], a[120:123], v[60:63], v[188:191]\n"
"s_waitcnt lgkmcnt(3)\n"
"ds_read_b128 v[52:55], v120 offset:13312\n"
"global_load_dwordx4 a[104:107], v1, s[66:67] offset:-2048 sc1\n"
"v_exp_f32 v119, v119\n"
"v_fma_f32 v116, v116, v102, v102\n"
"v_fma_f32 v117, v117, v102, v102\n"
"v_fma_f32 v118, v118, v102, v102\n"
"v_mfma_f32_16x16x32_bf16 v[176:179], a[40:43], v[32:35], v[176:179]\n"
"v_fma_f32 v119, v119, v102, v102\n"
"v_mfma_f32_16x16x32_bf16 v[176:179], a[44:47], v[36:39], v[176:179]\n"
"ds_read_b128 v[56:59], v120 offset:33792\n"
"v_rcp_f32 v116, v116\n"
"v_rcp_f32 v117, v117\n"
"v_rcp_f32 v118, v118\n"
"v_mfma_f32_16x16x32_bf16 v[180:183], a[124:127], v[32:35], v[180:183]\n"
"v_rcp_f32 v119, v119\n"
"v_mfma_f32_16x16x32_bf16 v[180:183], a[128:131], v[36:39], v[180:183]\n"
"s_waitcnt lgkmcnt(3)\n"
"ds_read_b128 v[60:63], v120 offset:34816\n"
"v_mul_f32 v116, v116, v76\n"
"v_mul_f32 v117, v117, v77\n"
"v_mul_f32 v118, v118, v78\n"
"v_mul_f32 v119, v119, v79\n"
"v_cvt_pk_bf16_f32 v134, v116, v117\n"
"v_cvt_pk_bf16_f32 v135, v118, v119\n"
"v_mfma_f32_16x16x32_bf16 v[184:187], a[40:43], v[40:43], v[184:187]\n"
"v_mfma_f32_16x16x32_bf16 v[184:187], a[44:47], v[44:47], v[184:187]\n"
"buffer_store_dwordx4 v[132:135], v1, s[12:15], s85 offen sc1\n"
"ds_read_b128 v[32:35], v120 offset:14336\n"
"v_mul_f32 v112, v80, v80\n"
"v_mul_f32 v113, v81, v81\n"
"v_mul_f32 v114, v82, v82\n"
"v_mul_f32 v115, v83, v83\n"
"v_fma_f32 v112, v112, v105, v104\n"
"v_fma_f32 v113, v113, v105, v104\n"
"v_fma_f32 v114, v114, v105, v104\n"
"v_fma_f32 v115, v115, v105, v104\n"
"v_mfma_f32_16x16x32_bf16 v[188:191], a[124:127], v[40:43], v[188:191]\n"
"v_mul_f32 v112, v112, v80\n"
"v_mul_f32 v113, v113, v81\n"
"v_mfma_f32_16x16x32_bf16 v[188:191], a[128:131], v[44:47], v[188:191]\n"
"s_waitcnt lgkmcnt(3)\n"
"ds_read_b128 v[36:39], v120 offset:15360\n"
"global_load_dwordx4 a[108:111], v1, s[66:67] offset:-1024 sc1\n"
"v_mul_f32 v114, v114, v82\n"
"v_mul_f32 v115, v115, v83\n"
"v_exp_f32 v112, v112\n"
"v_exp_f32 v113, v113\n"
"v_mfma_f32_16x16x32_bf16 v[176:179], a[48:51], v[48:51], v[176:179]\n"
"v_exp_f32 v114, v114\n"
"v_mfma_f32_16x16x32_bf16 v[176:179], a[52:55], v[52:55], v[176:179]\n"
"ds_read_b128 v[40:43], v120 offset:35840\n"
"v_exp_f32 v115, v115\n"
"v_fma_f32 v112, v112, v106, v106\n"
"v_fma_f32 v113, v113, v106, v106\n"
"v_fma_f32 v114, v114, v106, v106\n"
"v_fma_f32 v115, v115, v106, v106\n"
"v_mfma_f32_16x16x32_bf16 v[180:183], a[132:135], v[48:51], v[180:183]\n"
"v_rcp_f32 v112, v112\n"
"v_mfma_f32_16x16x32_bf16 v[180:183], a[136:139], v[52:55], v[180:183]\n"
"s_waitcnt lgkmcnt(3)\n"
"ds_read_b128 v[44:47], v120 offset:36864\n"
"global_load_dwordx4 a[112:115], v1, s[66:67] offset:0 sc1\n"
"v_rcp_f32 v113, v113\n"
"v_rcp_f32 v114, v114\n"
"v_mfma_f32_16x16x32_bf16 v[184:187], a[48:51], v[56:59], v[184:187]\n"
"v_rcp_f32 v115, v115\n"
"v_mfma_f32_16x16x32_bf16 v[184:187], a[52:55], v[60:63], v[184:187]\n"
"ds_read_b128 v[48:51], v120 offset:16384\n"
"v_mul_f32 v112, v112, v80\n"
"v_mul_f32 v113, v113, v81\n"
"v_mul_f32 v114, v114, v82\n"
"v_mul_f32 v115, v115, v83\n"
"v_cvt_pk_bf16_f32 v128, v112, v113\n"
"v_cvt_pk_bf16_f32 v129, v114, v115\n"
"v_mul_f32 v116, v84, v84\n"
"v_mul_f32 v117, v85, v85\n"
"v_mfma_f32_16x16x32_bf16 v[188:191], a[132:135], v[56:59], v[188:191]\n"
"v_mul_f32 v118, v86, v86\n"
"v_mul_f32 v119, v87, v87\n"
"v_mfma_f32_16x16x32_bf16 v[188:191], a[136:139], v[60:63], v[188:191]\n"
"s_waitcnt lgkmcnt(3)\n"
"ds_read_b128 v[52:55], v120 offset:17408\n"
"global_load_dwordx4 a[116:119], v1, s[66:67] offset:1024 sc1\n"
"v_fma_f32 v116, v116, v105, v104\n"
"v_fma_f32 v117, v117, v105, v104\n"
"v_fma_f32 v118, v118, v105, v104\n"
"v_fma_f32 v119, v119, v105, v104\n"
"v_mul_f32 v116, v116, v84\n"
"v_mfma_f32_16x16x32_bf16 v[176:179], a[56:59], v[32:35], v[176:179]\n"
"v_mul_f32 v117, v117, v85\n"
"v_mul_f32 v118, v118, v86\n"
"v_mfma_f32_16x16x32_bf16 v[176:179], a[60:63], v[36:39], v[176:179]\n"
"ds_read_b128 v[56:59], v120 offset:37888\n"
"v_mul_f32 v119, v119, v87\n"
"v_exp_f32 v116, v116\n"
"v_exp_f32 v117, v117\n"
"v_mfma_f32_16x16x32_bf16 v[180:183], a[140:143], v[32:35], v[180:183]\n"
"v_exp_f32 v118, v118\n"
"v_mfma_f32_16x16x32_bf16 v[180:183], a[144:147], v[36:39], v[180:183]\n"
"s_waitcnt lgkmcnt(3)\n"
"ds_read_b128 v[60:63], v120 offset:38912\n"
"global_load_dwordx4 a[120:123], v1, s[66:67] offset:2048 sc1\n"
"v_exp_f32 v119, v119\n"
"v_fma_f32 v116, v116, v106, v106\n"
"v_fma_f32 v117, v117, v106, v106\n"
"v_fma_f32 v118, v118, v106, v106\n"
"v_fma_f32 v119, v119, v106, v106\n"
"v_mfma_f32_16x16x32_bf16 v[184:187], a[56:59], v[40:43], v[184:187]\n"
"v_rcp_f32 v116, v116\n"
"v_mfma_f32_16x16x32_bf16 v[184:187], a[60:63], v[44:47], v[184:187]\n"
"ds_read_b128 v[32:35], v120 offset:18432\n"
"v_rcp_f32 v117, v117\n"
"v_rcp_f32 v118, v118\n"
"v_rcp_f32 v119, v119\n"
"v_mul_f32 v116, v116, v84\n"
"v_mfma_f32_16x16x32_bf16 v[188:191], a[140:143], v[40:43], v[188:191]\n"
"v_mul_f32 v117, v117, v85\n"
"v_mul_f32 v118, v118, v86\n"
"v_mfma_f32_16x16x32_bf16 v[188:191], a[144:147], v[44:47], v[188:191]\n"
"s_waitcnt lgkmcnt(3)\n"
"ds_read_b128 v[36:39], v120 offset:19456\n"
"global_load_dwordx4 a[124:127], v1, s[66:67] offset:3072 sc1\n"
"v_mul_f32 v119, v119, v87\n"
"v_cvt_pk_bf16_f32 v130, v116, v117\n"
"v_cvt_pk_bf16_f32 v131, v118, v119\n"
"v_mfma_f32_16x16x32_bf16 v[176:179], a[64:67], v[48:51], v[176:179]\n"
"v_mfma_f32_16x16x32_bf16 v[176:179], a[68:71], v[52:55], v[176:179]\n"
"buffer_store_dwordx4 v[128:131], v1, s[12:15], s86 offen sc1\n"
"ds_read_b128 v[40:43], v120 offset:39936\n"
"v_mul_f32 v112, v88, v88\n"
"v_mul_f32 v113, v89, v89\n"
"v_mul_f32 v114, v90, v90\n"
"v_mul_f32 v115, v91, v91\n"
"v_fma_f32 v112, v112, v109, v108\n"
"v_fma_f32 v113, v113, v109, v108\n"
"v_fma_f32 v114, v114, v109, v108\n"
"v_fma_f32 v115, v115, v109, v108\n"
"v_mul_f32 v112, v112, v88\n"
"v_mfma_f32_16x16x32_bf16 v[180:183], a[148:151], v[48:51], v[180:183]\n"
"v_mul_f32 v113, v113, v89\n"
"v_mul_f32 v114, v114, v90\n"
"v_mfma_f32_16x16x32_bf16 v[180:183], a[152:155], v[52:55], v[180:183]\n"
"s_waitcnt lgkmcnt(3)\n"
"s_add_u32 s66, s66, 8192\n"
"s_addc_u32 s67, s67, 0\n"
"ds_read_b128 v[44:47], v120 offset:40960\n"
"global_load_dwordx4 a[128:131], v1, s[66:67] offset:-4096 sc1\n"
"v_mul_f32 v115, v115, v91\n"
"v_exp_f32 v112, v112\n"
"v_exp_f32 v113, v113\n"
"v_mfma_f32_16x16x32_bf16 v[184:187], a[64:67], v[56:59], v[184:187]\n"
"v_exp_f32 v114, v114\n"
"v_mfma_f32_16x16x32_bf16 v[184:187], a[68:71], v[60:63], v[184:187]\n"
"ds_read_b128 v[48:51], v120 offset:20480\n"
"v_exp_f32 v115, v115\n"
"v_fma_f32 v112, v112, v110, v110\n"
"v_fma_f32 v113, v113, v110, v110\n"
"v_fma_f32 v114, v114, v110, v110\n"
"v_fma_f32 v115, v115, v110, v110\n"
"v_mfma_f32_16x16x32_bf16 v[188:191], a[148:151], v[56:59], v[188:191]\n"
"v_rcp_f32 v112, v112\n"
"v_mfma_f32_16x16x32_bf16 v[188:191], a[152:155], v[60:63], v[188:191]\n"
"s_waitcnt lgkmcnt(3)\n"
"ds_read_b128 v[52:55], v120 offset:41984\n"
"global_load_dwordx4 a[132:135], v1, s[66:67] offset:-3072 sc1\n"
"v_rcp_f32 v113, v113\n"
"v_rcp_f32 v114, v114\n"
"v_rcp_f32 v115, v115\n"
"v_mfma_f32_16x16x32_bf16 v[176:179], a[72:75], v[32:35], v[176:179]\n"
"v_mul_f32 v112, v112, v88\n"
"v_mul_f32 v113, v113, v89\n"
"v_mfma_f32_16x16x32_bf16 v[176:179], a[76:79], v[36:39], v[176:179]\n"
"ds_read_b128 v[56:59], v121 offset:0\n"
"v_mul_f32 v114, v114, v90\n"
"v_mul_f32 v115, v115, v91\n"
"v_cvt_pk_bf16_f32 v132, v112, v113\n"
"v_cvt_pk_bf16_f32 v133, v114, v115\n"
"v_mul_f32 v116, v92, v92\n"
"v_mul_f32 v117, v93, v93\n"
"v_mfma_f32_16x16x32_bf16 v[180:183], a[156:159], v[32:35], v[180:183]\n"
"v_mul_f32 v118, v94, v94\n"
"v_mul_f32 v119, v95, v95\n"
"v_mfma_f32_16x16x32_bf16 v[180:183], a[160:163], v[36:39], v[180:183]\n"
"s_waitcnt lgkmcnt(3)\n"
"ds_read_b128 v[60:63], v121 offset:1024\n"
"global_load_dwordx4 a[136:139], v1, s[66:67] offset:-2048 sc1\n"
"v_fma_f32 v116, v116, v109, v108\n"
"v_fma_f32 v117, v117, v109, v108\n"
"v_fma_f32 v118, v118, v109, v108\n"
"v_fma_f32 v119, v119, v109, v108\n"
"v_mul_f32 v116, v116, v92\n"
"v_mul_f32 v117, v117, v93\n"
"v_mfma_f32_16x16x32_bf16 v[184:187], a[72:75], v[40:43], v[184:187]\n"
"v_mul_f32 v118, v118, v94\n"
"v_mul_f32 v119, v119, v95\n"
"v_mfma_f32_16x16x32_bf16 v[184:187], a[76:79], v[44:47], v[184:187]\n"
"ds_read_b128 v[32:35], v121 offset:21504\n"
"v_exp_f32 v116, v116\n"
"v_exp_f32 v117, v117\n"
"v_exp_f32 v118, v118\n"
"v_mfma_f32_16x16x32_bf16 v[188:191], a[156:159], v[40:43], v[188:191]\n"
"v_exp_f32 v119, v119\n"
"v_mfma_f32_16x16x32_bf16 v[188:191], a[160:163], v[44:47], v[188:191]\n"
"s_waitcnt lgkmcnt(3)\n"
"ds_read_b128 v[36:39], v121 offset:22528\n"
"global_load_dwordx4 a[140:143], v1, s[66:67] offset:-1024 sc1\n"
"v_fma_f32 v116, v116, v110, v110\n"
"v_fma_f32 v117, v117, v110, v110\n"
"v_fma_f32 v118, v118, v110, v110\n"
"v_fma_f32 v119, v119, v110, v110\n"
"v_mfma_f32_16x16x32_bf16 v[176:179], a[80:83], v[48:51], v[176:179]\n"
"v_rcp_f32 v116, v116\n"
"v_rcp_f32 v117, v117\n"
"v_rcp_f32 v118, v118\n"
"v_mfma_f32_16x16x32_bf16 v[180:183], a[164:167], v[48:51], v[180:183]\n"
"ds_read_b128 v[40:43], v121 offset:2048\n"
"v_rcp_f32 v119, v119\n"
"v_mul_f32 v116, v116, v92\n"
"v_mul_f32 v117, v117, v93\n"
"v_mfma_f32_16x16x32_bf16 v[184:187], a[80:83], v[52:55], v[184:187]\n"
"v_mul_f32 v118, v118, v94\n"
"v_mul_f32 v119, v119, v95\n"
"v_cvt_pk_bf16_f32 v134, v116, v117\n"
"v_cvt_pk_bf16_f32 v135, v118, v119\n"
"v_mfma_f32_16x16x32_bf16 v[188:191], a[164:167], v[52:55], v[188:191]\n"
"s_waitcnt lgkmcnt(3)\n"
"buffer_store_dwordx4 v[132:135], v1, s[12:15], s87 offen sc1\n"
"ds_read_b128 v[44:47], v121 offset:3072\n"
"buffer_load_dwordx4 a[144:147], v1, s[12:15], s68 offen offset:0 sc1\n"
"v_mfma_f32_16x16x32_bf16 v[64:67], a[0:3], v[56:59], 0\n"
"v_mfma_f32_16x16x32_bf16 v[64:67], a[4:7], v[60:63], v[64:67]\n"
"s_waitcnt lgkmcnt(2)\n"
"ds_read_b128 v[48:51], v121 offset:23552\n"
"ds_read_b128 v[52:55], v121 offset:24576\n"
"v_mfma_f32_16x16x32_bf16 v[72:75], a[0:3], v[32:35], 0\n"
"v_mfma_f32_16x16x32_bf16 v[72:75], a[4:7], v[36:39], v[72:75]\n"
"s_waitcnt lgkmcnt(2)\n"
"ds_read_b128 v[56:59], v121 offset:4096\n"
"ds_read_b128 v[60:63], v121 offset:5120\n"
"buffer_load_dwordx4 a[148:151], v1, s[12:15], s68 offen offset:1024 sc1\n"
"v_mul_f32 v112, v176, v176\n"
"v_mfma_f32_16x16x32_bf16 v[64:67], a[8:11], v[40:43], v[64:67]\n"
"v_mul_f32 v113, v177, v177\n"
"v_mul_f32 v114, v178, v178\n"
"v_mfma_f32_16x16x32_bf16 v[64:67], a[12:15], v[44:47], v[64:67]\n"
"s_waitcnt lgkmcnt(2)\n"
"ds_read_b128 v[32:35], v121 offset:25600\n"
"ds_read_b128 v[36:39], v121 offset:26624\n"
"v_mul_f32 v115, v179, v179\n"
"v_fma_f32 v112, v112, v97, v96\n"
"v_fma_f32 v113, v113, v97, v96\n"
"v_fma_f32 v114, v114, v97, v96\n"
"v_fma_f32 v115, v115, v97, v96\n"
"v_mul_f32 v112, v112, v176\n"
"v_mul_f32 v113, v113, v177\n"
"v_mfma_f32_16x16x32_bf16 v[72:75], a[8:11], v[48:51], v[72:75]\n"
"v_mul_f32 v114, v114, v178\n"
"v_mul_f32 v115, v115, v179\n"
"v_mfma_f32_16x16x32_bf16 v[72:75], a[12:15], v[52:55], v[72:75]\n"
"s_waitcnt lgkmcnt(2)\n"
"ds_read_b128 v[40:43], v121 offset:6144\n"
"ds_read_b128 v[44:47], v121 offset:7168\n"
"buffer_load_dwordx4 a[152:155], v1, s[12:15], s68 offen offset:2048 sc1\n"
"v_exp_f32 v112, v112\n"
"v_exp_f32 v113, v113\n"
"v_mfma_f32_16x16x32_bf16 v[64:67], a[16:19], v[56:59], v[64:67]\n"
"v_exp_f32 v114, v114\n"
"v_mfma_f32_16x16x32_bf16 v[64:67], a[20:23], v[60:63], v[64:67]\n"
"s_waitcnt lgkmcnt(2)\n"
"ds_read_b128 v[48:51], v121 offset:27648\n"
"ds_read_b128 v[52:55], v121 offset:28672\n"
"v_exp_f32 v115, v115\n"
"v_fma_f32 v112, v112, v98, v98\n"
"v_fma_f32 v113, v113, v98, v98\n"
"v_fma_f32 v114, v114, v98, v98\n"
"v_fma_f32 v115, v115, v98, v98\n"
"v_rcp_f32 v112, v112\n"
"v_mfma_f32_16x16x32_bf16 v[72:75], a[16:19], v[32:35], v[72:75]\n"
"v_rcp_f32 v113, v113\n"
"v_mfma_f32_16x16x32_bf16 v[72:75], a[20:23], v[36:39], v[72:75]\n"
"s_waitcnt lgkmcnt(2)\n"
"ds_read_b128 v[56:59], v121 offset:8192\n"
"ds_read_b128 v[60:63], v121 offset:9216\n"
"buffer_load_dwordx4 a[156:159], v1, s[12:15], s68 offen offset:3072 sc1\n"
"v_rcp_f32 v114, v114\n"
"v_rcp_f32 v115, v115\n"
"v_mul_f32 v112, v112, v176\n"
"v_mfma_f32_16x16x32_bf16 v[64:67], a[24:27], v[40:43], v[64:67]\n"
"v_mul_f32 v113, v113, v177\n"
"v_mul_f32 v114, v114, v178\n"
"v_mfma_f32_16x16x32_bf16 v[64:67], a[28:31], v[44:47], v[64:67]\n"
"s_waitcnt lgkmcnt(2)\n"
"ds_read_b128 v[32:35], v121 offset:29696\n"
"ds_read_b128 v[36:39], v121 offset:30720\n"
"v_mul_f32 v115, v115, v179\n"
"v_cvt_pk_bf16_f32 v136, v112, v113\n"
"v_cvt_pk_bf16_f32 v137, v114, v115\n"
"v_mul_f32 v116, v184, v184\n"
"v_mul_f32 v117, v185, v185\n"
"v_mul_f32 v118, v186, v186\n"
"v_mul_f32 v119, v187, v187\n"
"v_mfma_f32_16x16x32_bf16 v[72:75], a[24:27], v[48:51], v[72:75]\n"
"v_fma_f32 v116, v116, v101, v100\n"
"v_fma_f32 v117, v117, v101, v100\n"
"v_mfma_f32_16x16x32_bf16 v[72:75], a[28:31], v[52:55], v[72:75]\n"
"s_waitcnt lgkmcnt(2)\n"
"s_add_u32 s68, s68, 4096\n"
"ds_read_b128 v[40:43], v121 offset:10240\n"
"ds_read_b128 v[44:47], v121 offset:11264\n"
"buffer_load_dwordx4 a[160:163], v1, s[12:15], s68 offen offset:0 sc1\n"
"v_fma_f32 v118, v118, v101, v100\n"
"v_fma_f32 v119, v119, v101, v100\n"
"v_mul_f32 v116, v116, v184\n"
"v_mul_f32 v117, v117, v185\n"
"v_mul_f32 v118, v118, v186\n"
"v_mfma_f32_16x16x32_bf16 v[64:67], a[32:35], v[56:59], v[64:67]\n"
"v_mul_f32 v119, v119, v187\n"
"v_mfma_f32_16x16x32_bf16 v[64:67], a[36:39], v[60:63], v[64:67]\n"
"s_waitcnt lgkmcnt(2)\n"
"ds_read_b128 v[48:51], v121 offset:31744\n"
"ds_read_b128 v[52:55], v121 offset:32768\n"
"buffer_load_dwordx4 a[164:167], v1, s[12:15], s68 offen offset:1024 sc1\n"
"v_exp_f32 v116, v116\n"
"v_exp_f32 v117, v117\n"
"v_exp_f32 v118, v118\n"
"v_mfma_f32_16x16x32_bf16 v[72:75], a[32:35], v[32:35], v[72:75]\n"
"v_exp_f32 v119, v119\n"
"v_mfma_f32_16x16x32_bf16 v[72:75], a[36:39], v[36:39], v[72:75]\n"
"s_waitcnt lgkmcnt(2)\n"
"ds_read_b128 v[56:59], v121 offset:12288\n"
"ds_read_b128 v[60:63], v121 offset:13312\n"
"v_fma_f32 v116, v116, v102, v102\n"
"v_fma_f32 v117, v117, v102, v102\n"
"v_fma_f32 v118, v118, v102, v102\n"
"v_fma_f32 v119, v119, v102, v102\n"
"v_rcp_f32 v116, v116\n"
"v_mfma_f32_16x16x32_bf16 v[64:67], a[40:43], v[40:43], v[64:67]\n"
"v_rcp_f32 v117, v117\n"
"v_mfma_f32_16x16x32_bf16 v[64:67], a[44:47], v[44:47], v[64:67]\n"
"s_waitcnt lgkmcnt(2)\n"
"ds_read_b128 v[32:35], v121 offset:33792\n"
"ds_read_b128 v[36:39], v121 offset:34816\n"
"buffer_load_dwordx4 a[0:3], v1, s[12:15], s68 offen offset:2048 sc1\n"
"v_rcp_f32 v118, v118\n"
"v_rcp_f32 v119, v119\n"
"v_mul_f32 v116, v116, v184\n"
"v_mfma_f32_16x16x32_bf16 v[72:75], a[40:43], v[48:51], v[72:75]\n"
"v_mul_f32 v117, v117, v185\n"
"v_mul_f32 v118, v118, v186\n"
"v_mfma_f32_16x16x32_bf16 v[72:75], a[44:47], v[52:55], v[72:75]\n"
"s_waitcnt lgkmcnt(2)\n"
"s_waitcnt vmcnt(7)\n"
"s_mov_b64 exec, 0xff\n"
"global_atomic_add v123, v0, s[4:5]\n"
"s_mov_b64 exec, -1\n"
"ds_read_b128 v[40:43], v121 offset:14336\n"
"ds_read_b128 v[44:47], v121 offset:15360\n"
"v_mul_f32 v119, v119, v187\n"
"v_cvt_pk_bf16_f32 v138, v116, v117\n"
"v_cvt_pk_bf16_f32 v139, v118, v119\n"
"s_nop 0\n"
"v_permlane32_swap_b32 v136, v138\n"
"v_permlane32_swap_b32 v137, v139\n"
"v_mfma_f32_16x16x32_bf16 v[64:67], a[48:51], v[56:59], v[64:67]\n"
"v_mfma_f32_16x16x32_bf16 v[64:67], a[52:55], v[60:63], v[64:67]\n"
"s_waitcnt lgkmcnt(2)\n"
"buffer_store_dwordx4 v[136:139], v122, s[12:15], s88 offen sc1\n"
"ds_read_b128 v[48:51], v121 offset:35840\n"
"ds_read_b128 v[52:55], v121 offset:36864\n"
"buffer_load_dwordx4 a[4:7], v1, s[12:15], s68 offen offset:3072 sc1\n"
"v_mul_f32 v112, v180, v180\n"
"v_mul_f32 v113, v181, v181\n"
"v_mul_f32 v114, v182, v182\n"
"v_mul_f32 v115, v183, v183\n"
"v_fma_f32 v112, v112, v97, v96\n"
"v_fma_f32 v113, v113, v97, v96\n"
"v_fma_f32 v114, v114, v97, v96\n"
"v_mfma_f32_16x16x32_bf16 v[72:75], a[48:51], v[32:35], v[72:75]\n"
"v_fma_f32 v115, v115, v97, v96\n"
"v_mul_f32 v112, v112, v180\n"
"v_mfma_f32_16x16x32_bf16 v[72:75], a[52:55], v[36:39], v[72:75]\n"
"s_waitcnt lgkmcnt(2)\n"
"ds_read_b128 v[56:59], v121 offset:16384\n"
"ds_read_b128 v[60:63], v121 offset:17408\n"
"v_mul_f32 v113, v113, v181\n"
"v_mul_f32 v114, v114, v182\n"
"v_mul_f32 v115, v115, v183\n"
"v_exp_f32 v112, v112\n"
"v_exp_f32 v113, v113\n"
"v_mfma_f32_16x16x32_bf16 v[64:67], a[56:59], v[40:43], v[64:67]\n"
"v_exp_f32 v114, v114\n"
"v_mfma_f32_16x16x32_bf16 v[64:67], a[60:63], v[44:47], v[64:67]\n"
"s_waitcnt lgkmcnt(2)\n"
"s_add_u32 s68, s68, 4096\n"
"ds_read_b128 v[32:35], v121 offset:37888\n"
"ds_read_b128 v[36:39], v121 offset:38912\n"
"buffer_load_dwordx4 a[8:11], v1, s[12:15], s68 offen offset:0 sc1\n"
"v_exp_f32 v115, v115\n"
"v_fma_f32 v112, v112, v98, v98\n"
"v_fma_f32 v113, v113, v98, v98\n"
"v_fma_f32 v114, v114, v98, v98\n"
"v_mfma_f32_16x16x32_bf16 v[72:75], a[56:59], v[48:51], v[72:75]\n"
"v_fma_f32 v115, v115, v98, v98\n"
"v_mfma_f32_16x16x32_bf16 v[72:75], a[60:63], v[52:55], v[72:75]\n"
"s_waitcnt lgkmcnt(2)\n"
"ds_read_b128 v[40:43], v121 offset:18432\n"
"ds_read_b128 v[44:47], v121 offset:19456\n"
"v_rcp_f32 v112, v112\n"
"v_rcp_f32 v113, v113\n"
"v_rcp_f32 v114, v114\n"
"v_rcp_f32 v115, v115\n"
"v_mfma_f32_16x16x32_bf16 v[64:67], a[64:67], v[56:59], v[64:67]\n"
"v_mul_f32 v112, v112, v180\n"
"v_mul_f32 v113, v113, v181\n"
"v_mfma_f32_16x16x32_bf16 v[64:67], a[68:71], v[60:63], v[64:67]\n"
"s_waitcnt lgkmcnt(2)\n"
"ds_read_b128 v[48:51], v121 offset:39936\n"
"ds_read_b128 v[52:55], v121 offset:40960\n"
"buffer_load_dwordx4 a[12:15], v1, s[12:15], s68 offen offset:1024 sc1\n"
"v_mul_f32 v114, v114, v182\n"
"v_mul_f32 v115, v115, v183\n"
"v_cvt_pk_bf16_f32 v140, v112, v113\n"
"v_cvt_pk_bf16_f32 v141, v114, v115\n"
"v_mul_f32 v116, v188, v188\n"
"Lpad10_%=:\n"
"s_cbranch_execz Lpadback2_%=\n"
"v_mfma_f32_16x16x32_bf16 v[72:75], a[64:67], v[32:35], v[72:75]\n"
"v_mul_f32 v117, v189, v189\n"
"v_mul_f32 v118, v190, v190\n"
"v_mfma_f32_16x16x32_bf16 v[72:75], a[68:71], v[36:39], v[72:75]\n"
"s_waitcnt lgkmcnt(2)\n"
"ds_read_b128 v[56:59], v121 offset:20480\n"
"ds_read_b128 v[60:63], v121 offset:41984\n"
"v_mul_f32 v119, v191, v191\n"
"v_fma_f32 v116, v116, v101, v100\n"
"v_fma_f32 v117, v117, v101, v100\n"
"v_fma_f32 v118, v118, v101, v100\n"
"v_fma_f32 v119, v119, v101, v100\n"
"v_mul_f32 v116, v116, v188\n"
"v_mfma_f32_16x16x32_bf16 v[64:67], a[72:75], v[40:43], v[64:67]\n"
"v_mul_f32 v117, v117, v189\n"
"v_mul_f32 v118, v118, v190\n"
"v_mfma_f32_16x16x32_bf16 v[64:67], a[76:79], v[44:47], v[64:67]\n"
"s_waitcnt lgkmcnt(2)\n"
"v_mul_f32 v119, v119, v191\n"
"v_exp_f32 v116, v116\n"
"v_exp_f32 v117, v117\n"
"v_exp_f32 v118, v118\n"
"v_exp_f32 v119, v119\n"
"v_mfma_f32_16x16x32_bf16 v[72:75], a[72:75], v[48:51], v[72:75]\n"
"v_fma_f32 v116, v116, v102, v102\n"
"v_fma_f32 v117, v117, v102, v102\n"
"v_mfma_f32_16x16x32_bf16 v[72:75], a[76:79], v[52:55], v[72:75]\n"
"s_waitcnt lgkmcnt(0)\n"
"v_fma_f32 v118, v118, v102, v102\n"
"v_fma_f32 v119, v119, v102, v102\n"
"v_rcp_f32 v116, v116\n"
"v_rcp_f32 v117, v117\n"
"v_rcp_f32 v118, v118\n"
"v_mfma_f32_16x16x32_bf16 v[64:67], a[80:83], v[56:59], v[64:67]\n"
"v_rcp_f32 v119, v119\n"
"v_mul_f32 v116, v116, v188\n"
"v_mul_f32 v117, v117, v189\n"
"v_mul_f32 v118, v118, v190\n"
"v_mul_f32 v119, v119, v191\n"
"v_cvt_pk_bf16_f32 v142, v116, v117\n"
"v_mfma_f32_16x16x32_bf16 v[72:75], a[80:83], v[60:63], v[72:75]\n"
"v_cvt_pk_bf16_f32 v143, v118, v119\n"
"v_permlane32_swap_b32 v140, v142\n"
"s_nop 0\n"
"v_permlane32_swap_b32 v141, v143\n"
"buffer_store_dwordx4 v[140:143], v122, s[12:15], s89 offen sc1\n"
"s_mov_b64 exec, 1\n"
"global_atomic_add v132, v124, v2, s[4:5] sc0\n"
"s_mov_b64 exec, -1\n"
"v_mov_b32 v0, 0x100\n"
"v_mul_f32 v112, v64, v64\n"
"v_mul_f32 v113, v65, v65\n"
"v_mul_f32 v114, v66, v66\n"
"v_mul_f32 v115, v67, v67\n"
"v_fma_f32 v112, v112, v105, v104\n"
"v_fma_f32 v113, v113, v105, v104\n"
"v_fma_f32 v114, v114, v105, v104\n"
"v_fma_f32 v115, v115, v105, v104\n"
"v_mul_f32 v112, v112, v64\n"
"v_mul_f32 v113, v113, v65\n"
"v_mul_f32 v114, v114, v66\n"
"v_mul_f32 v115, v115, v67\n"
"v_exp_f32 v112, v112\n"
"v_exp_f32 v113, v113\n"
"v_exp_f32 v114, v114\n"
"v_exp_f32 v115, v115\n"
"v_fma_f32 v112, v112, v106, v106\n"
"v_fma_f32 v113, v113, v106, v106\n"
"v_fma_f32 v114, v114, v106, v106\n"
"v_fma_f32 v115, v115, v106, v106\n"
"v_rcp_f32 v112, v112\n"
"v_rcp_f32 v113, v113\n"
"v_rcp_f32 v114, v114\n"
"v_rcp_f32 v115, v115\n"
"v_mul_f32 v112, v112, v64\n"
"v_mul_f32 v113, v113, v65\n"
"v_mul_f32 v114, v114, v66\n"
"v_mul_f32 v115, v115, v67\n"
"v_cvt_pk_bf16_f32 v136, v112, v113\n"
"v_cvt_pk_bf16_f32 v137, v114, v115\n"
"v_mul_f32 v116, v72, v72\n"
"v_mul_f32 v117, v73, v73\n"
"v_mul_f32 v118, v74, v74\n"
"v_mul_f32 v119, v75, v75\n"
"v_fma_f32 v116, v116, v109, v108\n"
"v_fma_f32 v117, v117, v109, v108\n"
"v_fma_f32 v118, v118, v109, v108\n"
"v_fma_f32 v119, v119, v109, v108\n"
"v_mul_f32 v116, v116, v72\n"
"v_mul_f32 v117, v117, v73\n"
"v_mul_f32 v118, v118, v74\n"
"v_mul_f32 v119, v119, v75\n"
"v_exp_f32 v116, v116\n"
"v_exp_f32 v117, v117\n"
"v_exp_f32 v118, v118\n"
"v_exp_f32 v119, v119\n"
"v_fma_f32 v116, v116, v110, v110\n"
"v_fma_f32 v117, v117, v110, v110\n"
"v_fma_f32 v118, v118, v110, v110\n"
"v_fma_f32 v119, v119, v110, v110\n"
"v_rcp_f32 v116, v116\n"
"v_rcp_f32 v117, v117\n"
"v_rcp_f32 v118, v118\n"
"v_rcp_f32 v119, v119\n"
"v_mul_f32 v116, v116, v72\n"
"v_mul_f32 v117, v117, v73\n"
"v_mul_f32 v118, v118, v74\n"
"v_mul_f32 v119, v119, v75\n"
"v_cvt_pk_bf16_f32 v138, v116, v117\n"
"v_cvt_pk_bf16_f32 v139, v118, v119\n"
"s_nop 0\n"
"v_permlane32_swap_b32 v136, v138\n"
"v_permlane32_swap_b32 v137, v139\n"
"buffer_store_dwordx4 v[136:139], v122, s[12:15], s90 offen sc1\n"
"s_waitcnt vmcnt(1)\n"
"v_readfirstlane_b32 s45, v132\n"
"s_nop 0\n"
"s_and_b32 s45, s45, 0xff\n"
"s_cmp_lt_u32 s45, 8\n"
"s_cbranch_scc1 Les52_%=\n"
"Leb53_%=:\n"
"global_load_dwordx4 v[92:95], v1, s[72:73] offset:-4096 sc1\n"
"global_load_dwordx4 v[96:99], v1, s[72:73] offset:-3072 sc1\n"
"global_load_dwordx4 v[100:103], v1, s[72:73] offset:-2048 sc1\n"
"global_load_dwordx4 v[104:107], v1, s[72:73] offset:-1024 sc1\n"
"global_load_dwordx4 v[108:111], v1, s[72:73] offset:0 sc1\n"
"global_load_dwordx4 v[112:115], v1, s[72:73] offset:1024 sc1\n"
"global_load_dwordx4 v[116:119], v1, s[72:73] offset:2048 sc1\n"
"global_load_dwordx4 a[80:83], v1, s[72:73] offset:3072 sc1\n"
"s_waitcnt vmcnt(8)\n"
"s_mov_b64 exec, 0xff\n"
"global_atomic_add v123, v0, s[4:5]\n"
"s_mov_b64 exec, -1\n"
"global_load_dwordx4 a[76:79], v1, s[74:75] offset:-4096 sc1\n"
"global_load_dwordx4 a[72:75], v1, s[74:75] offset:-3072 sc1\n"
"global_load_dwordx4 a[68:71], v1, s[74:75] offset:-2048 sc1\n"
"s_mul_i32 s51, s24, 22528\n"
"s_add_u32 s51, s51, 18936832\n"
"s_mul_i32 s52, s21, 2560\n"
"s_add_u32 s52, s52, s51\n"
"s_waitcnt vmcnt(10)\n"
"v_mfma_f32_16x16x32_bf16 v[32:35], v[144:147], v[92:95], 0\n"
"v_mfma_f32_16x16x32_bf16 v[32:35], v[164:167], v[96:99], v[32:35]\n"
"global_load_dwordx4 a[64:67], v1, s[74:75] offset:-1024 sc1\n"
"v_mfma_f32_16x16x32_bf16 v[36:39], v[148:151], v[92:95], 0\n"
"v_mfma_f32_16x16x32_bf16 v[36:39], v[168:171], v[96:99], v[36:39]\n"
"v_mfma_f32_16x16x32_bf16 v[40:43], v[152:155], v[92:95], 0\n"
"v_mfma_f32_16x16x32_bf16 v[40:43], a[168:171], v[96:99], v[40:43]\n"
"global_load_dwordx4 a[60:63], v1, s[74:75] offset:0 sc1\n"
"v_mfma_f32_16x16x32_bf16 v[44:47], v[156:159], v[92:95], 0\n"
"v_mfma_f32_16x16x32_bf16 v[44:47], a[172:175], v[96:99], v[44:47]\n"
"v_mfma_f32_16x16x32_bf16 v[48:51], v[160:163], v[92:95], 0\n"
"v_mfma_f32_16x16x32_bf16 v[48:51], a[252:255], v[96:99], v[48:51]\n"
"s_waitcnt vmcnt(10)\n"
"v_mfma_f32_16x16x32_bf16 v[32:35], a[180:183], v[100:103], v[32:35]\n"
"v_mfma_f32_16x16x32_bf16 v[32:35], a[200:203], v[104:107], v[32:35]\n"
"global_load_dwordx4 a[56:59], v1, s[74:75] offset:1024 sc1\n"
"v_mfma_f32_16x16x32_bf16 v[36:39], a[188:191], v[100:103], v[36:39]\n"
"v_mfma_f32_16x16x32_bf16 v[36:39], a[204:207], v[104:107], v[36:39]\n"
"v_mfma_f32_16x16x32_bf16 v[40:43], v[192:195], v[100:103], v[40:43]\n"
"v_mfma_f32_16x16x32_bf16 v[40:43], a[208:211], v[104:107], v[40:43]\n"
"global_load_dwordx4 v[92:95], v1, s[74:75] offset:2048 sc1\n"
"v_mfma_f32_16x16x32_bf16 v[44:47], a[196:199], v[100:103], v[44:47]\n"
"v_mfma_f32_16x16x32_bf16 v[44:47], v[212:215], v[104:107], v[44:47]\n"
"v_mfma_f32_16x16x32_bf16 v[48:51], v[196:199], v[100:103], v[48:51]\n"
"v_mfma_f32_16x16x32_bf16 v[48:51], a[216:219], v[104:107], v[48:51]\n"
"s_waitcnt vmcnt(10)\n"
"v_mfma_f32_16x16x32_bf16 v[32:35], v[220:223], v[108:111], v[32:35]\n"
"v_mfma_f32_16x16x32_bf16 v[32:35], a[176:179], v[112:115], v[32:35]\n"
"global_load_dwordx4 v[96:99], v1, s[74:75] offset:3072 sc1\n"
"v_mfma_f32_16x16x32_bf16 v[36:39], a[228:231], v[108:111], v[36:39]\n"
"v_mfma_f32_16x16x32_bf16 v[36:39], a[184:187], v[112:115], v[36:39]\n"
"s_mov_b64 exec, 1\n"
"global_atomic_add v132, v124, v2, s[4:5] sc0\n"
"s_mov_b64 exec, -1\n"
"v_mfma_f32_16x16x32_bf16 v[40:43], v[228:231], v[108:111], v[40:43]\n"
"v_mfma_f32_16x16x32_bf16 v[40:43], a[192:195], v[112:115], v[40:43]\n"
"global_load_dwordx4 v[100:103], v1, s[76:77] offset:-4096 sc1\n"
"v_mfma_f32_16x16x32_bf16 v[44:47], v[240:243], v[108:111], v[44:47]\n"
"v_mfma_f32_16x16x32_bf16 v[44:47], v[200:203], v[112:115], v[44:47]\n"
"v_mfma_f32_16x16x32_bf16 v[48:51], a[244:247], v[108:111], v[48:51]\n"
"v_mfma_f32_16x16x32_bf16 v[48:51], v[204:207], v[112:115], v[48:51]\n"
"s_waitcnt vmcnt(11)\n"
"v_mfma_f32_16x16x32_bf16 v[32:35], v[208:211], v[116:119], v[32:35]\n"
"v_mfma_f32_16x16x32_bf16 v[32:35], a[224:227], a[80:83], v[32:35]\n"
"global_load_dwordx4 v[104:107], v1, s[76:77] offset:-3072 sc1\n"
"v_mfma_f32_16x16x32_bf16 v[36:39], a[212:215], v[116:119], v[36:39]\n"
"v_mfma_f32_16x16x32_bf16 v[36:39], v[232:235], a[80:83], v[36:39]\n"
"v_mfma_f32_16x16x32_bf16 v[40:43], v[216:219], v[116:119], v[40:43]\n"
"v_mfma_f32_16x16x32_bf16 v[40:43], a[232:235], a[80:83], v[40:43]\n"
"global_load_dwordx4 v[108:111], v1, s[76:77] offset:-2048 sc1\n"
"v_mfma_f32_16x16x32_bf16 v[44:47], a[220:223], v[116:119], v[44:47]\n"
"v_mfma_f32_16x16x32_bf16 v[44:47], a[236:239], a[80:83], v[44:47]\n"
"v_mfma_f32_16x16x32_bf16 v[48:51], v[224:227], v[116:119], v[48:51]\n"
"v_mfma_f32_16x16x32_bf16 v[48:51], v[236:239], a[80:83], v[48:51]\n"
"s_waitcnt vmcnt(10)\n"
"v_mfma_f32_16x16x32_bf16 v[52:55], v[144:147], a[76:79], 0\n"
"v_mfma_f32_16x16x32_bf16 v[52:55], v[164:167], a[72:75], v[52:55]\n"
"global_load_dwordx4 v[112:115], v1, s[76:77] offset:-1024 sc1\n"
"v_mfma_f32_16x16x32_bf16 v[56:59], v[148:151], a[76:79], 0\n"
"v_mfma_f32_16x16x32_bf16 v[56:59], v[168:171], a[72:75], v[56:59]\n"
"v_mfma_f32_16x16x32_bf16 v[60:63], v[152:155], a[76:79], 0\n"
"v_mfma_f32_16x16x32_bf16 v[60:63], a[168:171], a[72:75], v[60:63]\n"
"global_load_dwordx4 v[116:119], v1, s[76:77] offset:0 sc1\n"
"v_mfma_f32_16x16x32_bf16 v[64:67], v[156:159], a[76:79], 0\n"
"v_mfma_f32_16x16x32_bf16 v[64:67], a[172:175], a[72:75], v[64:67]\n"
"v_mfma_f32_16x16x32_bf16 v[68:71], v[160:163], a[76:79], 0\n"
"v_mfma_f32_16x16x32_bf16 v[68:71], a[252:255], a[72:75], v[68:71]\n"
"s_waitcnt vmcnt(10)\n"
"v_mfma_f32_16x16x32_bf16 v[52:55], a[180:183], a[68:71], v[52:55]\n"
"v_mfma_f32_16x16x32_bf16 v[52:55], a[200:203], a[64:67], v[52:55]\n"
"global_load_dwordx4 a[80:83], v1, s[76:77] offset:1024 sc1\n"
"v_mfma_f32_16x16x32_bf16 v[56:59], a[188:191], a[68:71], v[56:59]\n"
"v_mfma_f32_16x16x32_bf16 v[56:59], a[204:207], a[64:67], v[56:59]\n"
"v_mfma_f32_16x16x32_bf16 v[60:63], v[192:195], a[68:71], v[60:63]\n"
"v_mfma_f32_16x16x32_bf16 v[60:63], a[208:211], a[64:67], v[60:63]\n"
"global_load_dwordx4 a[76:79], v1, s[76:77] offset:2048 sc1\n"
"v_mfma_f32_16x16x32_bf16 v[64:67], a[196:199], a[68:71], v[64:67]\n"
"v_mfma_f32_16x16x32_bf16 v[64:67], v[212:215], a[64:67], v[64:67]\n"
"v_mfma_f32_16x16x32_bf16 v[68:71], v[196:199], a[68:71], v[68:71]\n"
"v_mfma_f32_16x16x32_bf16 v[68:71], a[216:219], a[64:67], v[68:71]\n"
"s_waitcnt vmcnt(10)\n"
"v_mfma_f32_16x16x32_bf16 v[52:55], v[220:223], a[60:63], v[52:55]\n"
"v_mfma_f32_16x16x32_bf16 v[52:55], a[176:179], a[56:59], v[52:55]\n"
"global_load_dwordx4 a[72:75], v1, s[76:77] offset:3072 sc1\n"
"v_mfma_f32_16x16x32_bf16 v[56:59], a[228:231], a[60:63], v[56:59]\n"
"v_mfma_f32_16x16x32_bf16 v[56:59], a[184:187], a[56:59], v[56:59]\n"
"v_mfma_f32_16x16x32_bf16 v[60:63], v[228:231], a[60:63], v[60:63]\n"
"v_mfma_f32_16x16x32_bf16 v[60:63], a[192:195], a[56:59], v[60:63]\n"
"s_waitcnt vmcnt(8)\n"
"v_readfirstlane_b32 s45, v132\n"
"s_nop 0\n"
"s_cmp_lt_u32 s45, 0x800\n"
"s_cbranch_scc1 Lls54_%=\n"
"Llb55_%=:\n"
"s_add_u32 s72, s72, 8192\n"
"s_addc_u32 s73, s73, 0\n"
"global_load_dwordx4 a[68:71], v1, s[72:73] offset:-4096 sc1\n"
"v_mfma_f32_16x16x32_bf16 v[64:67], v[240:243], a[60:63], v[64:67]\n"
"v_mfma_f32_16x16x32_bf16 v[64:67], v[200:203], a[56:59], v[64:67]\n"
"v_mfma_f32_16x16x32_bf16 v[68:71], a[244:247], a[60:63], v[68:71]\n"
"v_mfma_f32_16x16x32_bf16 v[68:71], v[204:207], a[56:59], v[68:71]\n"
"v_mfma_f32_16x16x32_bf16 v[52:55], v[208:211], v[92:95], v[52:55]\n"
"v_mfma_f32_16x16x32_bf16 v[52:55], a[224:227], v[96:99], v[52:55]\n"
"global_load_dwordx4 a[64:67], v1, s[72:73] offset:-3072 sc1\n"
"v_mfma_f32_16x16x32_bf16 v[56:59], a[212:215], v[92:95], v[56:59]\n"
"v_mfma_f32_16x16x32_bf16 v[56:59], v[232:235], v[96:99], v[56:59]\n"
"v_mfma_f32_16x16x32_bf16 v[60:63], v[216:219], v[92:95], v[60:63]\n"
"v_mfma_f32_16x16x32_bf16 v[60:63], a[232:235], v[96:99], v[60:63]\n"
"global_load_dwordx4 a[60:63], v1, s[72:73] offset:-2048 sc1\n"
"v_mfma_f32_16x16x32_bf16 v[64:67], a[220:223], v[92:95], v[64:67]\n"
"v_mfma_f32_16x16x32_bf16 v[64:67], a[236:239], v[96:99], v[64:67]\n"
"v_mfma_f32_16x16x32_bf16 v[68:71], v[224:227], v[92:95], v[68:71]\n"
"v_mfma_f32_16x16x32_bf16 v[68:71], v[236:239], v[96:99], v[68:71]\n"
"s_waitcnt vmcnt(9)\n"
"v_mfma_f32_16x16x32_bf16 v[72:75], v[144:147], v[100:103], 0\n"
"v_mfma_f32_16x16x32_bf16 v[72:75], v[164:167], v[104:107], v[72:75]\n"
"global_load_dwordx4 a[56:59], v1, s[72:73] offset:-1024 sc1\n"
"v_mfma_f32_16x16x32_bf16 v[76:79], v[148:151], v[100:103], 0\n"
"v_mfma_f32_16x16x32_bf16 v[76:79], v[168:171], v[104:107], v[76:79]\n"
"v_mfma_f32_16x16x32_bf16 v[80:83], v[152:155], v[100:103], 0\n"
"v_mfma_f32_16x16x32_bf16 v[80:83], a[168:171], v[104:107], v[80:83]\n"
"buffer_load_dwordx4 v[92:95], v1, s[12:15], s9 offen offset:0 sc1\n"
"v_mfma_f32_16x16x32_bf16 v[84:87], v[156:159], v[100:103], 0\n"
"v_mfma_f32_16x16x32_bf16 v[84:87], a[172:175], v[104:107], v[84:87]\n"
"v_mfma_f32_16x16x32_bf16 v[88:91], v[160:163], v[100:103], 0\n"
"v_mfma_f32_16x16x32_bf16 v[88:91], a[252:255], v[104:107], v[88:91]\n"
"s_waitcnt vmcnt(9)\n"
"v_mfma_f32_16x16x32_bf16 v[72:75], a[180:183], v[108:111], v[72:75]\n"
"v_mfma_f32_16x16x32_bf16 v[72:75], a[200:203], v[112:115], v[72:75]\n"
"buffer_load_dwordx4 v[96:99], v1, s[12:15], s9 offen offset:1024 sc1\n"
"v_mfma_f32_16x16x32_bf16 v[76:79], a[188:191], v[108:111], v[76:79]\n"
"v_mfma_f32_16x16x32_bf16 v[76:79], a[204:207], v[112:115], v[76:79]\n"
"v_mfma_f32_16x16x32_bf16 v[80:83], v[192:195], v[108:111], v[80:83]\n"
"v_mfma_f32_16x16x32_bf16 v[80:83], a[208:211], v[112:115], v[80:83]\n"
"s_add_u32 s74, s74, 8192\n"
"s_addc_u32 s75, s75, 0\n"
"global_load_dwordx4 v[100:103], v1, s[74:75] offset:-4096 sc1\n"
"v_mfma_f32_16x16x32_bf16 v[84:87], a[196:199], v[108:111], v[84:87]\n"
"v_mfma_f32_16x16x32_bf16 v[84:87], v[212:215], v[112:115], v[84:87]\n"
"v_mfma_f32_16x16x32_bf16 v[88:91], v[196:199], v[108:111], v[88:91]\n"
"v_mfma_f32_16x16x32_bf16 v[88:91], a[216:219], v[112:115], v[88:91]\n"
"s_waitcnt vmcnt(9)\n"
"v_mfma_f32_16x16x32_bf16 v[72:75], v[220:223], v[116:119], v[72:75]\n"
"v_mfma_f32_16x16x32_bf16 v[72:75], a[176:179], a[80:83], v[72:75]\n"
"s_barrier\n"
"global_load_dwordx4 v[104:107], v1, s[74:75] offset:-3072 sc1\n"
"v_mfma_f32_16x16x32_bf16 v[76:79], a[228:231], v[116:119], v[76:79]\n"
"v_mfma_f32_16x16x32_bf16 v[76:79], a[184:187], a[80:83], v[76:79]\n"
"v_mfma_f32_16x16x32_bf16 v[80:83], v[228:231], v[116:119], v[80:83]\n"
"v_mfma_f32_16x16x32_bf16 v[80:83], a[192:195], a[80:83], v[80:83]\n"
"global_load_dwordx4 v[108:111], v1, s[74:75] offset:-2048 sc1\n"
"v_mfma_f32_16x16x32_bf16 v[84:87], v[240:243], v[116:119], v[84:87]\n"
"v_mfma_f32_16x16x32_bf16 v[84:87], v[200:203], a[80:83], v[84:87]\n"
"v_mfma_f32_16x16x32_bf16 v[88:91], a[244:247], v[116:119], v[88:91]\n"
"v_mfma_f32_16x16x32_bf16 v[88:91], v[204:207], a[80:83], v[88:91]\n"
"s_waitcnt vmcnt(9)\n"
"v_mfma_f32_16x16x32_bf16 v[72:75], v[208:211], a[76:79], v[72:75]\n"
"v_mfma_f32_16x16x32_bf16 v[72:75], a[224:227], a[72:75], v[72:75]\n"
"global_load_dwordx4 v[112:115], v1, s[74:75] offset:-1024 sc1\n"
"v_mfma_f32_16x16x32_bf16 v[76:79], a[212:215], a[76:79], v[76:79]\n"
"v_mfma_f32_16x16x32_bf16 v[76:79], v[232:235], a[72:75], v[76:79]\n"
"v_mfma_f32_16x16x32_bf16 v[80:83], v[216:219], a[76:79], v[80:83]\n"
"v_mfma_f32_16x16x32_bf16 v[80:83], a[232:235], a[72:75], v[80:83]\n"
"buffer_load_dwordx4 v[116:119], v1, s[12:15], s10 offen offset:0 sc1\n"
"v_mfma_f32_16x16x32_bf16 v[84:87], a[220:223], a[76:79], v[84:87]\n"
"v_mfma_f32_16x16x32_bf16 v[84:87], a[236:239], a[72:75], v[84:87]\n"
"v_mfma_f32_16x16x32_bf16 v[88:91], v[224:227], a[76:79], v[88:91]\n"
"v_mfma_f32_16x16x32_bf16 v[88:91], v[236:239], a[72:75], v[88:91]\n"
"s_waitcnt vmcnt(9)\n"
"v_mfma_f32_16x16x32_bf16 v[32:35], a[240:243], a[68:71], v[32:35]\n"
"v_mfma_f32_16x16x32_bf16 v[32:35], a[84:87], a[64:67], v[32:35]\n"
"s_mul_i32 s46, s24, 20480\n"
"s_mul_i32 s47, s21, 160\n"
"s_add_u32 s46, s46, s47\n"
"s_mul_i32 s47, s22, 15360\n"
"s_add_u32 s47, s47, 102400\n"
"buffer_load_dwordx4 a[80:83], v1, s[12:15], s10 offen offset:1024 sc1\n"
"v_add_u32 v139, 0, v4\n"
"v_mul_u32_u24 v133, 6554, v139\n"
"v_mfma_f32_16x16x32_bf16 v[36:39], v[244:247], a[68:71], v[36:39]\n"
"v_lshrrev_b32 v133, 16, v133\n"
"v_mul_u32_u24 v133, 1120, v133\n"
"v_mfma_f32_16x16x32_bf16 v[36:39], a[88:91], a[64:67], v[36:39]\n"
"v_lshl_add_u32 v133, v139, 4, v133\n"
"v_add_u32 v133, s46, v133\n"
"v_mfma_f32_16x16x32_bf16 v[40:43], v[248:251], a[68:71], v[40:43]\n"
"v_add_u32 v139, 64, v4\n"
"v_mul_u32_u24 v134, 6554, v139\n"
"v_mfma_f32_16x16x32_bf16 v[40:43], a[92:95], a[64:67], v[40:43]\n"
"s_add_u32 s76, s76, 8192\n"
"s_addc_u32 s77, s77, 0\n"
"global_load_dwordx4 a[76:79], v1, s[76:77] offset:-4096 sc1\n"
"v_lshrrev_b32 v134, 16, v134\n"
"v_mfma_f32_16x16x32_bf16 v[44:47], a[248:251], a[68:71], v[44:47]\n"
"v_mul_u32_u24 v134, 1120, v134\n"
"v_lshl_add_u32 v134, v139, 4, v134\n"
"v_mfma_f32_16x16x32_bf16 v[44:47], a[96:99], a[64:67], v[44:47]\n"
"v_add_u32 v134, s46, v134\n"
"v_add_u32 v139, 128, v4\n"
"v_mfma_f32_16x16x32_bf16 v[48:51], v[252:255], a[68:71], v[48:51]\n"
"v_mul_u32_u24 v135, 6554, v139\n"
"v_lshrrev_b32 v135, 16, v135\n"
"v_mfma_f32_16x16x32_bf16 v[48:51], a[100:103], a[64:67], v[48:51]\n"
"s_waitcnt vmcnt(9)\n"
"v_mul_u32_u24 v135, 1120, v135\n"
"v_lshl_add_u32 v135, v139, 4, v135\n"
"v_mfma_f32_16x16x32_bf16 v[32:35], a[104:107], a[60:63], v[32:35]\n"
"v_add_u32 v135, s46, v135\n"
"v_and_b32 v139, 15, v4\n"
"v_mfma_f32_16x16x32_bf16 v[32:35], a[124:127], a[56:59], v[32:35]\n"
"global_load_dwordx4 a[72:75], v1, s[76:77] offset:-3072 sc1\n"
"v_mul_u32_u24 v139, 1120, v139\n"
"v_mfma_f32_16x16x32_bf16 v[36:39], a[108:111], a[60:63], v[36:39]\n"
"v_add_u32 v139, s46, v139\n"
"v_subrev_u32 v139, s47, v139\n"
"v_mfma_f32_16x16x32_bf16 v[36:39], a[128:131], a[56:59], v[36:39]\n"
"s_add_u32 m0, s79, 0\n"
"s_nop 0\n"
"buffer_load_dwordx4 v1, s[80:83], s60 offen offset:0 sc1 lds\n"
"v_mfma_f32_16x16x32_bf16 v[40:43], a[112:115], a[60:63], v[40:43]\n"
"v_sub_u32 v136, v6, v139\n"
"v_sub_u32 v137, v10, v139\n"
"v_mfma_f32_16x16x32_bf16 v[40:43], a[132:135], a[56:59], v[40:43]\n"
"global_load_dwordx4 a[68:71], v1, s[76:77] offset:-2048 sc1\n"
"v_add_u32 v138, s47, v1\n"
"v_mfma_f32_16x16x32_bf16 v[44:47], a[116:119], a[60:63], v[44:47]\n"
"v_mfma_f32_16x16x32_bf16 v[44:47], a[136:139], a[56:59], v[44:47]\n"
"s_lshl_b32 s44, s20, 2\n"
"s_add_u32 s44, s44, s22\n"
"s_lshl_b32 s44, s44, 3\n"
"s_lshl_b32 s44, s44, 7\n"
"s_add_u32 s44, s44, s27\n"
"s_add_u32 s44, s44, 393216\n"
"v_lshl_add_u32 v142, v4, 7, s44\n"
"v_mfma_f32_16x16x32_bf16 v[48:51], a[120:123], a[60:63], v[48:51]\n"
"v_mfma_f32_16x16x32_bf16 v[48:51], a[140:143], a[56:59], v[48:51]\n"
"s_waitcnt vmcnt(10)\n"
"s_lshl_b32 s44, s20, 2\n"
"s_add_u32 s44, s44, s22\n"
"s_lshl_b32 s44, s44, 3\n"
"s_add_u32 s44, s44, s21\n"
"s_lshl_b32 s44, s44, 7\n"
"s_add_u32 s44, s44, s27\n"
"s_add_u32 s44, s44, 393216\n"
"v_mov_b32 v3, s44\n"
"v_mov_b32 v0, 1\n"
"v_mfma_f32_16x16x32_bf16 v[32:35], a[144:147], v[92:95], v[32:35]\n"
"v_mfma_f32_16x16x32_bf16 v[32:35], a[164:167], v[96:99], v[32:35]\n"
"global_load_dwordx4 a[64:67], v1, s[76:77] offset:-1024 sc1\n"
"v_mfma_f32_16x16x32_bf16 v[36:39], a[148:151], v[92:95], v[36:39]\n"
"v_mfma_f32_16x16x32_bf16 v[36:39], a[0:3], v[96:99], v[36:39]\n"
"buffer_load_dwordx4 v1, s[80:83], s60 offen offset:1024 sc1 lds\n"
"v_mfma_f32_16x16x32_bf16 v[40:43], a[152:155], v[92:95], v[40:43]\n"
"v_mfma_f32_16x16x32_bf16 v[40:43], a[4:7], v[96:99], v[40:43]\n"
"buffer_load_dwordx4 a[60:63], v1, s[12:15], s11 offen offset:0 sc1\n"
"v_mfma_f32_16x16x32_bf16 v[44:47], a[156:159], v[92:95], v[44:47]\n"
"v_mfma_f32_16x16x32_bf16 v[44:47], a[8:11], v[96:99], v[44:47]\n"
"v_mfma_f32_16x16x32_bf16 v[48:51], a[160:163], v[92:95], v[48:51]\n"
"v_mfma_f32_16x16x32_bf16 v[48:51], a[12:15], v[96:99], v[48:51]\n"
"s_waitcnt vmcnt(11)\n"
"v_mfma_f32_16x16x32_bf16 v[52:55], a[240:243], v[100:103], v[52:55]\n"
"v_mfma_f32_16x16x32_bf16 v[52:55], a[84:87], v[104:107], v[52:55]\n"
"buffer_load_dwordx4 a[56:59], v1, s[12:15], s11 offen offset:1024 sc1\n"
"v_mfma_f32_16x16x32_bf16 v[56:59], v[244:247], v[100:103], v[56:59]\n"
"v_mfma_f32_16x16x32_bf16 v[56:59], a[88:91], v[104:107], v[56:59]\n"
"buffer_load_dwordx4 v1, s[80:83], s60 offen offset:2048 sc1 lds\n"
"ds_write_b128 v173, v[32:35] offset:0\n"
"v_mfma_f32_16x16x32_bf16 v[60:63], v[248:251], v[100:103], v[60:63]\n"
"v_mfma_f32_16x16x32_bf16 v[60:63], a[92:95], v[104:107], v[60:63]\n"
"global_load_dwordx4 v[92:95], v1, s[70:71] offset:-4096 sc1\n"
"v_mfma_f32_16x16x32_bf16 v[64:67], a[248:251], v[100:103], v[64:67]\n"
"v_mfma_f32_16x16x32_bf16 v[64:67], a[96:99], v[104:107], v[64:67]\n"
"v_mfma_f32_16x16x32_bf16 v[68:71], v[252:255], v[100:103], v[68:71]\n"
"v_mfma_f32_16x16x32_bf16 v[68:71], a[100:103], v[104:107], v[68:71]\n"
"s_waitcnt vmcnt(12)\n"
"ds_write_b128 v173, v[36:39] offset:1024\n"
"v_mfma_f32_16x16x32_bf16 v[52:55], a[104:107], v[108:111], v[52:55]\n"
"v_mfma_f32_16x16x32_bf16 v[52:55], a[124:127], v[112:115], v[52:55]\n"
"global_load_dwordx4 v[96:99], v1, s[70:71] offset:-3072 sc1\n"
"v_mfma_f32_16x16x32_bf16 v[56:59], a[108:111], v[108:111], v[56:59]\n"
"v_mfma_f32_16x16x32_bf16 v[56:59], a[128:131], v[112:115], v[56:59]\n"
"buffer_load_dwordx4 v1, s[80:83], s60 offen offset:3072 sc1 lds\n"
"v_mfma_f32_16x16x32_bf16 v[60:63], a[112:115], v[108:111], v[60:63]\n"
"v_mfma_f32_16x16x32_bf16 v[60:63], a[132:135], v[112:115], v[60:63]\n"
"global_load_dwordx4 v[100:103], v1, s[70:71] offset:-2048 sc1\n"
"ds_write_b128 v173, v[40:43] offset:2048\n"
"v_mfma_f32_16x16x32_bf16 v[64:67], a[116:119], v[108:111], v[64:67]\n"
"v_mfma_f32_16x16x32_bf16 v[64:67], a[136:139], v[112:115], v[64:67]\n"
"v_mfma_f32_16x16x32_bf16 v[68:71], a[120:123], v[108:111], v[68:71]\n"
"v_mfma_f32_16x16x32_bf16 v[68:71], a[140:143], v[112:115], v[68:71]\n"
"s_waitcnt vmcnt(13)\n"
"v_mfma_f32_16x16x32_bf16 v[52:55], a[144:147], v[116:119], v[52:55]\n"
"v_mfma_f32_16x16x32_bf16 v[52:55], a[164:167], a[80:83], v[52:55]\n"
"global_load_dwordx4 v[104:107], v1, s[70:71] offset:-1024 sc1\n"
"ds_write_b128 v173, v[44:47] offset:3072\n"
"v_mfma_f32_16x16x32_bf16 v[56:59], a[148:151], v[116:119], v[56:59]\n"
"v_mfma_f32_16x16x32_bf16 v[56:59], a[0:3], a[80:83], v[56:59]\n"
"s_add_u32 m0, s79, 4096\n"
"s_add_u32 s60, s60, 4096\n"
"buffer_load_dwordx4 v1, s[80:83], s60 offen offset:0 sc1 lds\n"
"v_mfma_f32_16x16x32_bf16 v[60:63], a[152:155], v[116:119], v[60:63]\n"
"v_mfma_f32_16x16x32_bf16 v[60:63], a[4:7], a[80:83], v[60:63]\n"
"global_load_dwordx4 v[108:111], v1, s[70:71] offset:0 sc1\n"
"v_mfma_f32_16x16x32_bf16 v[64:67], a[156:159], v[116:119], v[64:67]\n"
"v_mfma_f32_16x16x32_bf16 v[64:67], a[8:11], a[80:83], v[64:67]\n"
"ds_write_b128 v173, v[48:51] offset:4096\n"
"v_mfma_f32_16x16x32_bf16 v[68:71], a[160:163], v[116:119], v[68:71]\n"
"v_mfma_f32_16x16x32_bf16 v[68:71], a[12:15], a[80:83], v[68:71]\n"
"s_waitcnt vmcnt(14)\n"
"v_mfma_f32_16x16x32_bf16 v[72:75], a[240:243], a[76:79], v[72:75]\n"
"v_mfma_f32_16x16x32_bf16 v[72:75], a[84:87], a[72:75], v[72:75]\n"
"global_load_dwordx4 v[112:115], v1, s[70:71] offset:1024 sc1\n"
"v_mfma_f32_16x16x32_bf16 v[76:79], v[244:247], a[76:79], v[76:79]\n"
"v_mfma_f32_16x16x32_bf16 v[76:79], a[88:91], a[72:75], v[76:79]\n"
"buffer_load_dwordx4 v1, s[80:83], s60 offen offset:1024 sc1 lds\n"
"ds_write_b128 v174, v[52:55] offset:0\n"
"v_mfma_f32_16x16x32_bf16 v[80:83], v[248:251], a[76:79], v[80:83]\n"
"v_mfma_f32_16x16x32_bf16 v[80:83], a[92:95], a[72:75], v[80:83]\n"
"global_load_dwordx4 v[116:119], v1, s[70:71] offset:2048 sc1\n"
"v_mfma_f32_16x16x32_bf16 v[84:87], a[248:251], a[76:79], v[84:87]\n"
"v_mfma_f32_16x16x32_bf16 v[84:87], a[96:99], a[72:75], v[84:87]\n"
"v_mfma_f32_16x16x32_bf16 v[88:91], v[252:255], a[76:79], v[88:91]\n"
"v_mfma_f32_16x16x32_bf16 v[88:91], a[100:103], a[72:75], v[88:91]\n"
"s_waitcnt vmcnt(14)\n"
"ds_write_b128 v174, v[56:59] offset:1024\n"
"v_mfma_f32_16x16x32_bf16 v[72:75], a[104:107], a[68:71], v[72:75]\n"
"v_mfma_f32_16x16x32_bf16 v[72:75], a[124:127], a[64:67], v[72:75]\n"
"global_load_dwordx4 a[80:83], v1, s[70:71] offset:3072 sc1\n"
"v_mfma_f32_16x16x32_bf16 v[76:79], a[108:111], a[68:71], v[76:79]\n"
"v_mfma_f32_16x16x32_bf16 v[76:79], a[128:131], a[64:67], v[76:79]\n"
"buffer_load_dwordx4 v1, s[80:83], s60 offen offset:2048 sc1 lds\n"
"v_mfma_f32_16x16x32_bf16 v[80:83], a[112:115], a[68:71], v[80:83]\n"
"v_mfma_f32_16x16x32_bf16 v[80:83], a[132:135], a[64:67], v[80:83]\n"
"s_add_u32 s70, s70, 8192\n"
"s_addc_u32 s71, s71, 0\n"
"global_load_dwordx4 a[76:79], v1, s[70:71] offset:-4096 sc1\n"
"ds_write_b128 v174, v[60:63] offset:2048\n"
"v_mfma_f32_16x16x32_bf16 v[84:87], a[116:119], a[68:71], v[84:87]\n"
"v_mfma_f32_16x16x32_bf16 v[84:87], a[136:139], a[64:67], v[84:87]\n"
"v_mfma_f32_16x16x32_bf16 v[88:91], a[120:123], a[68:71], v[88:91]\n"
"v_mfma_f32_16x16x32_bf16 v[88:91], a[140:143], a[64:67], v[88:91]\n"
"s_waitcnt vmcnt(14)\n"
"v_mfma_f32_16x16x32_bf16 v[72:75], a[144:147], a[60:63], v[72:75]\n"
"v_mfma_f32_16x16x32_bf16 v[72:75], a[164:167], a[56:59], v[72:75]\n"
"global_load_dwordx4 a[72:75], v1, s[70:71] offset:-3072 sc1\n"
"ds_write_b128 v174, v[64:67] offset:3072\n"
"v_mfma_f32_16x16x32_bf16 v[76:79], a[148:151], a[60:63], v[76:79]\n"
"v_mfma_f32_16x16x32_bf16 v[76:79], a[0:3], a[56:59], v[76:79]\n"
"buffer_load_dwordx4 v1, s[80:83], s60 offen offset:3072 sc1 lds\n"
"v_mfma_f32_16x16x32_bf16 v[80:83], a[152:155], a[60:63], v[80:83]\n"
"v_mfma_f32_16x16x32_bf16 v[80:83], a[4:7], a[56:59], v[80:83]\n"
"global_load_dwordx4 a[68:71], v1, s[70:71] offset:-2048 sc1\n"
"v_mfma_f32_16x16x32_bf16 v[84:87], a[156:159], a[60:63], v[84:87]\n"
"v_mfma_f32_16x16x32_bf16 v[84:87], a[8:11], a[56:59], v[84:87]\n"
"ds_write_b128 v174, v[68:71] offset:4096\n"
"v_mfma_f32_16x16x32_bf16 v[88:91], a[160:163], a[60:63], v[88:91]\n"
"v_mfma_f32_16x16x32_bf16 v[88:91], a[12:15], a[56:59], v[88:91]\n"
"s_waitcnt vmcnt(14)\n"
"v_mfma_f32_16x16x32_bf16 v[12:15], v[144:147], v[92:95], v[12:15]\n"
"v_mfma_f32_16x16x32_bf16 v[12:15], v[164:167], v[96:99], v[12:15]\n"
"global_load_dwordx4 a[64:67], v1, s[70:71] offset:-1024 sc1\n"
"v_mfma_f32_16x16x32_bf16 v[16:19], v[148:151], v[92:95], v[16:19]\n"
"v_mfma_f32_16x16x32_bf16 v[16:19], v[168:171], v[96:99], v[16:19]\n"
"s_add_u32 m0, s79, 8192\n"
"s_add_u32 s60, s60, 4096\n"
"buffer_load_dwordx4 v1, s[80:83], s60 offen offset:0 sc1 lds\n"
"ds_write_b128 v125, v[72:75] offset:0\n"
"v_mfma_f32_16x16x32_bf16 v[20:23], v[152:155], v[92:95], v[20:23]\n"
"v_mfma_f32_16x16x32_bf16 v[20:23], a[168:171], v[96:99], v[20:23]\n"
"buffer_load_dwordx4 a[60:63], v1, s[12:15], s8 offen offset:0 sc1\n"
"ds_write_b128 v125, v[76:79] offset:1024\n"
"v_mfma_f32_16x16x32_bf16 v[24:27], v[156:159], v[92:95], v[24:27]\n"
"v_mfma_f32_16x16x32_bf16 v[24:27], a[172:175], v[96:99], v[24:27]\n"
"ds_write_b128 v125, v[80:83] offset:2048\n"
"v_mfma_f32_16x16x32_bf16 v[28:31], v[160:163], v[92:95], v[28:31]\n"
"v_mfma_f32_16x16x32_bf16 v[28:31], a[252:255], v[96:99], v[28:31]\n"
"s_waitcnt vmcnt(14)\n"
"ds_write_b128 v125, v[84:87] offset:3072\n"
"v_mfma_f32_16x16x32_bf16 v[12:15], a[180:183], v[100:103], v[12:15]\n"
"v_mfma_f32_16x16x32_bf16 v[12:15], a[200:203], v[104:107], v[12:15]\n"
"buffer_load_dwordx4 a[56:59], v1, s[12:15], s8 offen offset:1024 sc1\n"
"ds_write_b128 v125, v[88:91] offset:4096\n"
"v_mfma_f32_16x16x32_bf16 v[16:19], a[188:191], v[100:103], v[16:19]\n"
"v_mfma_f32_16x16x32_bf16 v[16:19], a[204:207], v[104:107], v[16:19]\n"
"buffer_load_dwordx4 v1, s[80:83], s60 offen offset:1024 sc1 lds\n"
"v_mfma_f32_16x16x32_bf16 v[20:23], v[192:195], v[100:103], v[20:23]\n"
"v_mfma_f32_16x16x32_bf16 v[20:23], a[208:211], v[104:107], v[20:23]\n"
"buffer_load_dwordx4 v1, s[80:83], s60 offen offset:2048 sc1 lds\n"
"v_mfma_f32_16x16x32_bf16 v[24:27], a[196:199], v[100:103], v[24:27]\n"
"v_mfma_f32_16x16x32_bf16 v[24:27], v[212:215], v[104:107], v[24:27]\n"
"s_waitcnt lgkmcnt(0)\n"
"s_barrier\n"
"buffer_load_dwordx4 v1, s[80:83], s60 offen offset:3072 sc1 lds\n"
"ds_read_b128 v[32:35], v172 offset:0\n"
"v_mfma_f32_16x16x32_bf16 v[28:31], v[196:199], v[100:103], v[28:31]\n"
"ds_read_b128 v[36:39], v172 offset:1024\n"
"v_mfma_f32_16x16x32_bf16 v[28:31], a[216:219], v[104:107], v[28:31]\n"
"s_waitcnt vmcnt(15)\n"
"ds_read_b128 v[40:43], v172 offset:2048\n"
"v_mfma_f32_16x16x32_bf16 v[12:15], v[220:223], v[108:111], v[12:15]\n"
"ds_read_b128 v[44:47], v172 offset:3072\n"
"v_mfma_f32_16x16x32_bf16 v[12:15], a[176:179], v[112:115], v[12:15]\n"
"s_add_u32 m0, s79, 12288\n"
"s_add_u32 s60, s60, 4096\n"
"buffer_load_dwordx4 v1, s[80:83], s60 offen offset:0 sc1 lds\n"
"ds_read_b128 v[48:51], v172 offset:4096\n"
"v_mfma_f32_16x16x32_bf16 v[16:19], a[228:231], v[108:111], v[16:19]\n"
"ds_read_b128 v[52:55], v172 offset:5120\n"
"v_mfma_f32_16x16x32_bf16 v[16:19], a[184:187], v[112:115], v[16:19]\n"
"ds_read_b128 v[56:59], v172 offset:6144\n"
"v_mfma_f32_16x16x32_bf16 v[20:23], v[228:231], v[108:111], v[20:23]\n"
"ds_read_b128 v[60:63], v172 offset:7168\n"
"v_mfma_f32_16x16x32_bf16 v[20:23], a[192:195], v[112:115], v[20:23]\n"
"buffer_load_dwordx4 v1, s[80:83], s60 offen offset:1024 sc1 lds\n"
"ds_read_b128 v[64:67], v172 offset:8192\n"
"v_mfma_f32_16x16x32_bf16 v[24:27], v[240:243], v[108:111], v[24:27]\n"
"ds_read_b128 v[68:71], v172 offset:9216\n"
"v_mfma_f32_16x16x32_bf16 v[24:27], v[200:203], v[112:115], v[24:27]\n"
"ds_read_b128 v[72:75], v172 offset:10240\n"
"v_mfma_f32_16x16x32_bf16 v[28:31], a[244:247], v[108:111], v[28:31]\n"
"ds_read_b128 v[76:79], v172 offset:11264\n"
"v_mfma_f32_16x16x32_bf16 v[28:31], v[204:207], v[112:115], v[28:31]\n"
"s_waitcnt vmcnt(14)\n"
"ds_read_b128 v[80:83], v172 offset:12288\n"
"v_mfma_f32_16x16x32_bf16 v[12:15], v[208:211], v[116:119], v[12:15]\n"
"ds_read_b128 v[84:87], v172 offset:13312\n"
"s_waitcnt lgkmcnt(8)\n"
"v_add_f32 v32, v32, v52\n"
"v_mfma_f32_16x16x32_bf16 v[12:15], a[224:227], a[80:83], v[12:15]\n"
"buffer_load_dwordx4 v1, s[80:83], s60 offen offset:2048 sc1 lds\n"
"ds_read_b128 v[88:91], v172 offset:14336\n"
"v_mfma_f32_16x16x32_bf16 v[16:19], a[212:215], v[116:119], v[16:19]\n"
"v_add_f32 v33, v33, v53\n"
"v_mfma_f32_16x16x32_bf16 v[16:19], v[232:235], a[80:83], v[16:19]\n"
"buffer_load_dwordx4 v1, s[80:83], s60 offen offset:3072 sc1 lds\n"
"v_mfma_f32_16x16x32_bf16 v[20:23], v[216:219], v[116:119], v[20:23]\n"
"v_add_f32 v34, v34, v54\n"
"v_add_f32 v35, v35, v55\n"
"v_mfma_f32_16x16x32_bf16 v[20:23], a[232:235], a[80:83], v[20:23]\n"
"s_add_u32 m0, s79, 16384\n"
"s_add_u32 s60, s60, 4096\n"
"buffer_load_dwordx4 v1, s[80:83], s60 offen offset:0 sc1 lds\n"
"v_mfma_f32_16x16x32_bf16 v[24:27], a[220:223], v[116:119], v[24:27]\n"
"s_waitcnt lgkmcnt(8)\n"
"v_add_f32 v36, v36, v56\n"
"v_add_f32 v37, v37, v57\n"
"v_mfma_f32_16x16x32_bf16 v[24:27], a[236:239], a[80:83], v[24:27]\n"
"Lpad11_%=:\n"
"s_cbranch_execz Lpadback2_%=\n"
"v_add_f32 v38, v38, v58\n"
"v_add_f32 v39, v39, v59\n"
"v_mfma_f32_16x16x32_bf16 v[28:31], v[224:227], v[116:119], v[28:31]\n"
"s_waitcnt lgkmcnt(7)\n"
"v_add_f32 v40, v40, v60\n"
"v_add_f32 v41, v41, v61\n"
"v_mfma_f32_16x16x32_bf16 v[28:31], v[236:239], a[80:83], v[28:31]\n"
"s_waitcnt vmcnt(14)\n"
"v_add_f32 v42, v42, v62\n"
"v_add_f32 v43, v43, v63\n"
"v_mfma_f32_16x16x32_bf16 v[12:15], a[240:243], a[76:79], v[12:15]\n"
"s_waitcnt lgkmcnt(6)\n"
"v_add_f32 v44, v44, v64\n"
"v_add_f32 v45, v45, v65\n"
"v_mfma_f32_16x16x32_bf16 v[12:15], a[84:87], a[72:75], v[12:15]\n"
"buffer_load_dwordx4 v1, s[80:83], s60 offen offset:1024 sc1 lds\n"
"v_mfma_f32_16x16x32_bf16 v[16:19], v[244:247], a[76:79], v[16:19]\n"
"v_add_f32 v46, v46, v66\n"
"v_add_f32 v47, v47, v67\n"
"v_mfma_f32_16x16x32_bf16 v[16:19], a[88:91], a[72:75], v[16:19]\n"
"s_waitcnt lgkmcnt(5)\n"
"v_add_f32 v48, v48, v68\n"
"v_add_f32 v49, v49, v69\n"
"v_mfma_f32_16x16x32_bf16 v[20:23], v[248:251], a[76:79], v[20:23]\n"
"v_add_f32 v50, v50, v70\n"
"v_add_f32 v51, v51, v71\n"
"v_mfma_f32_16x16x32_bf16 v[20:23], a[92:95], a[72:75], v[20:23]\n"
"buffer_load_dwordx4 v1, s[80:83], s60 offen offset:2048 sc1 lds\n"
"v_mfma_f32_16x16x32_bf16 v[24:27], a[248:251], a[76:79], v[24:27]\n"
"s_waitcnt lgkmcnt(4)\n"
"v_add_f32 v32, v32, v72\n"
"v_add_f32 v33, v33, v73\n"
"v_mfma_f32_16x16x32_bf16 v[24:27], a[96:99], a[72:75], v[24:27]\n"
"buffer_load_dwordx4 v1, s[80:83], s60 offen offset:3072 sc1 lds\n"
"v_mfma_f32_16x16x32_bf16 v[28:31], v[252:255], a[76:79], v[28:31]\n"
"v_add_f32 v34, v34, v74\n"
"v_add_f32 v35, v35, v75\n"
"v_mfma_f32_16x16x32_bf16 v[28:31], a[100:103], a[72:75], v[28:31]\n"
"s_waitcnt vmcnt(14)\n"
"s_waitcnt lgkmcnt(3)\n"
"v_add_f32 v36, v36, v76\n"
"v_add_f32 v37, v37, v77\n"
"v_mfma_f32_16x16x32_bf16 v[12:15], a[104:107], a[68:71], v[12:15]\n"
"v_add_f32 v38, v38, v78\n"
"v_add_f32 v39, v39, v79\n"
"v_mfma_f32_16x16x32_bf16 v[12:15], a[124:127], a[64:67], v[12:15]\n"
"s_add_u32 m0, s79, 20480\n"
"s_add_u32 s60, s60, 4096\n"
"buffer_load_dwordx4 v1, s[80:83], s60 offen offset:0 sc1 lds\n"
"v_mfma_f32_16x16x32_bf16 v[16:19], a[108:111], a[68:71], v[16:19]\n"
"s_waitcnt lgkmcnt(2)\n"
"v_add_f32 v40, v40, v80\n"
"v_add_f32 v41, v41, v81\n"
"v_mfma_f32_16x16x32_bf16 v[16:19], a[128:131], a[64:67], v[16:19]\n"
"v_add_f32 v42, v42, v82\n"
"v_add_f32 v43, v43, v83\n"
"v_mfma_f32_16x16x32_bf16 v[20:23], a[112:115], a[68:71], v[20:23]\n"
"s_waitcnt lgkmcnt(1)\n"
"v_add_f32 v44, v44, v84\n"
"v_add_f32 v45, v45, v85\n"
"v_mfma_f32_16x16x32_bf16 v[20:23], a[132:135], a[64:67], v[20:23]\n"
"buffer_load_dwordx4 v1, s[80:83], s60 offen offset:1024 sc1 lds\n"
"v_mfma_f32_16x16x32_bf16 v[24:27], a[116:119], a[68:71], v[24:27]\n"
"v_add_f32 v46, v46, v86\n"
"v_add_f32 v47, v47, v87\n"
"v_mfma_f32_16x16x32_bf16 v[24:27], a[136:139], a[64:67], v[24:27]\n"
"s_waitcnt lgkmcnt(0)\n"
"v_add_f32 v48, v48, v88\n"
"v_add_f32 v49, v49, v89\n"
"v_mfma_f32_16x16x32_bf16 v[28:31], a[120:123], a[68:71], v[28:31]\n"
"v_add_f32 v50, v50, v90\n"
"v_add_f32 v51, v51, v91\n"
"v_mfma_f32_16x16x32_bf16 v[28:31], a[140:143], a[64:67], v[28:31]\n"
"s_waitcnt vmcnt(13)\n"
"v_mfma_f32_16x16x32_bf16 v[12:15], a[144:147], a[60:63], v[12:15]\n"
"v_mfma_f32_16x16x32_bf16 v[12:15], a[164:167], a[56:59], v[12:15]\n"
"buffer_load_dwordx4 v1, s[80:83], s60 offen offset:2048 sc1 lds\n"
"v_mfma_f32_16x16x32_bf16 v[16:19], a[148:151], a[60:63], v[16:19]\n"
"v_mfma_f32_16x16x32_bf16 v[16:19], a[0:3], a[56:59], v[16:19]\n"
"buffer_load_dwordx4 v1, s[80:83], s60 offen offset:3072 sc1 lds\n"
"v_mfma_f32_16x16x32_bf16 v[20:23], a[152:155], a[60:63], v[20:23]\n"
"v_mfma_f32_16x16x32_bf16 v[20:23], a[4:7], a[56:59], v[20:23]\n"
"s_add_u32 m0, s79, 24576\n"
"s_add_u32 s60, s60, 4096\n"
"buffer_load_dwordx4 v1, s[80:83], s60 offen offset:0 sc1 lds\n"
"v_mfma_f32_16x16x32_bf16 v[24:27], a[156:159], a[60:63], v[24:27]\n"
"v_mfma_f32_16x16x32_bf16 v[24:27], a[8:11], a[56:59], v[24:27]\n"
"v_mfma_f32_16x16x32_bf16 v[28:31], a[160:163], a[60:63], v[28:31]\n"
"v_mfma_f32_16x16x32_bf16 v[28:31], a[12:15], a[56:59], v[28:31]\n"
"v_pk_add_f32 v[12:13], v[12:13], v[32:33]\n"
"v_pk_add_f32 v[14:15], v[14:15], v[34:35]\n"
"v_pk_add_f32 v[16:17], v[16:17], v[36:37]\n"
"v_pk_add_f32 v[18:19], v[18:19], v[38:39]\n"
"v_pk_add_f32 v[20:21], v[20:21], v[40:41]\n"
"v_pk_add_f32 v[22:23], v[22:23], v[42:43]\n"
"v_pk_add_f32 v[24:25], v[24:25], v[44:45]\n"
"v_pk_add_f32 v[26:27], v[26:27], v[46:47]\n"
"v_pk_add_f32 v[28:29], v[28:29], v[48:49]\n"
"v_pk_add_f32 v[30:31], v[30:31], v[50:51]\n"
"s_cmp_lg_u32 s23, 0\n"
"s_cbranch_scc1 Llast2_%=\n"
"v_cvt_pk_bf16_f32 v32, v12, v13\n"
"v_cvt_pk_bf16_f32 v33, v14, v15\n"
"v_cvt_pk_bf16_f32 v34, v16, v17\n"
"v_cvt_pk_bf16_f32 v35, v18, v19\n"
"v_cvt_pk_bf16_f32 v36, v20, v21\n"
"v_cvt_pk_bf16_f32 v37, v22, v23\n"
"v_cvt_pk_bf16_f32 v38, v24, v25\n"
"v_cvt_pk_bf16_f32 v39, v26, v27\n"
"buffer_store_dwordx4 v[32:35], v1, s[12:15], s52 offen sc1\n"
"buffer_store_dwordx4 v[36:39], v1, s[12:15], s52 offen offset:1024 sc1\n"
"v_pk_add_f32 v[44:45], v[12:13], v[14:15]\n"
"v_pk_mul_f32 v[46:47], v[12:13], v[12:13]\n"
"v_pk_fma_f32 v[46:47], v[14:15], v[14:15], v[46:47]\n"
"v_pk_add_f32 v[44:45], v[44:45], v[16:17]\n"
"v_pk_fma_f32 v[46:47], v[16:17], v[16:17], v[46:47]\n"
"v_pk_add_f32 v[44:45], v[44:45], v[18:19]\n"
"v_pk_fma_f32 v[46:47], v[18:19], v[18:19], v[46:47]\n"
"v_pk_add_f32 v[44:45], v[44:45], v[20:21]\n"
"v_pk_fma_f32 v[46:47], v[20:21], v[20:21], v[46:47]\n"
"v_pk_add_f32 v[44:45], v[44:45], v[22:23]\n"
"v_pk_fma_f32 v[46:47], v[22:23], v[22:23], v[46:47]\n"
"v_pk_add_f32 v[44:45], v[44:45], v[24:25]\n"
"v_pk_fma_f32 v[46:47], v[24:25], v[24:25], v[46:47]\n"
"v_pk_add_f32 v[44:45], v[44:45], v[26:27]\n"
"v_pk_fma_f32 v[46:47], v[26:27], v[26:27], v[46:47]\n"
"v_pk_add_f32 v[44:45], v[44:45], v[28:29]\n"
"v_pk_fma_f32 v[46:47], v[28:29], v[28:29], v[46:47]\n"
"v_pk_add_f32 v[44:45], v[44:45], v[30:31]\n"
"v_pk_fma_f32 v[46:47], v[30:31], v[30:31], v[46:47]\n"
"v_add_f32 v42, v44, v45\n"
"v_add_f32 v43, v46, v47\n"
"v_cvt_pk_bf16_f32 v40, v28, v29\n"
"v_cvt_pk_bf16_f32 v41, v30, v31\n"
"v_permlane16_swap_b32 v42, v43\n"
"v_add_f32 v42, v42, v43\n"
"v_mov_b32 v43, v42\n"
"s_nop 1\n"
"v_permlane32_swap_b32 v42, v43\n"
"v_add_f32 v42, v42, v43\n"
"v_mov_b32 v43, v42\n"
"s_nop 1\n"
"v_permlane16_swap_b32 v42, v43\n"
"s_nop 1\n"
"v_permlane32_swap_b32 v40, v42\n"
"v_permlane32_swap_b32 v41, v43\n"
"buffer_store_dwordx4 v[40:43], v5, s[12:15], s51 offen sc1\n"
"s_mov_b64 exec, 0xff\n"
"global_atomic_add v142, v0, s[4:5]\n"
"s_mov_b64 exec, 1\n"
"global_atomic_add v108, v3, v2, s[4:5] sc0\n"
"s_mov_b64 exec, -1\n"
"s_add_u32 s64, s51, 4096\n"
"s_add_u32 s64, s4, s64\n"
"s_addc_u32 s65, s5, 0\n"
"s_mul_i32 s49, s22, 7\n"
"s_min_u32 s49, s49, 19\n"
"s_lshl_b32 s49, s49, 10\n"
"s_mul_i32 s46, s21, 129024\n"
"s_add_u32 s46, s46, s49\n"
"s_add_u32 s66, s46, 6996992\n"
"s_add_u32 s66, s4, s66\n"
"s_addc_u32 s67, s5, 0\n"
"s_waitcnt vmcnt(5)\n"
"s_barrier\n"
"s_mov_b64 exec, 1\n"
"global_atomic_add v109, v3, v2, s[4:5] sc0\n"
"s_mov_b64 exec, -1\n"
"s_add_u32 m0, s49, 102400\n"
"s_nop 0\n"
"global_load_lds_dwordx4 v1, s[66:67] offset:0 sc1\n"
"global_load_lds_dwordx4 v1, s[66:67] offset:1024 sc1\n"
"global_load_lds_dwordx4 v1, s[66:67] offset:2048 sc1\n"
"global_load_lds_dwordx4 v1, s[66:67] offset:3072 sc1\n"
"ds_read_b128 a[0:3], v1 offset:0\n"
"ds_read_b128 a[4:7], v1 offset:1024\n"
"ds_read_b128 a[8:11], v1 offset:2048\n"
"ds_read_b128 a[12:15], v1 offset:3072\n"
"ds_read_b128 a[16:19], v1 offset:4096\n"
"ds_read_b128 a[20:23], v1 offset:5120\n"
"ds_read_b128 a[24:27], v1 offset:6144\n"
"ds_read_b128 a[28:31], v1 offset:7168\n"
"ds_read_b128 a[32:35], v1 offset:8192\n"
"ds_read_b128 a[36:39], v1 offset:9216\n"
"s_mov_b64 exec, 1\n"
"global_atomic_add v110, v3, v2, s[4:5] sc0\n"
"s_mov_b64 exec, -1\n"
"s_add_u32 m0, s49, 106496\n"
"s_add_u32 s66, s66, 4096\n"
"s_addc_u32 s67, s67, 0\n"
"global_load_lds_dwordx4 v1, s[66:67] offset:0 sc1\n"
"global_load_lds_dwordx4 v1, s[66:67] offset:1024 sc1\n"
"global_load_lds_dwordx4 v1, s[66:67] offset:2048 sc1\n"
"ds_read_b128 a[40:43], v1 offset:10240\n"
"ds_read_b128 a[44:47], v1 offset:11264\n"
"ds_read_b128 a[48:51], v1 offset:12288\n"
"ds_read_b128 a[52:55], v1 offset:13312\n"
"ds_read_b128 a[56:59], v1 offset:14336\n"
"ds_read_b128 a[60:63], v1 offset:15360\n"
"ds_read_b128 a[64:67], v1 offset:16384\n"
"ds_read_b128 a[68:71], v1 offset:17408\n"
"ds_read_b128 a[72:75], v1 offset:18432\n"
"ds_read_b128 a[76:79], v1 offset:19456\n"
"s_mov_b64 exec, 1\n"
"global_atomic_add v111, v3, v2, s[4:5] sc0\n"
"s_mov_b64 exec, -1\n"
"ds_read_b128 a[80:83], v1 offset:20480\n"
"ds_read_b128 a[84:87], v1 offset:21504\n"
"ds_read_b128 a[88:91], v1 offset:22528\n"
"ds_read_b128 a[92:95], v1 offset:23552\n"
"ds_read_b128 a[96:99], v1 offset:24576\n"
"ds_read_b128 a[100:103], v1 offset:25600\n"
"ds_read_b128 a[104:107], v1 offset:26624\n"
"ds_read_b128 a[108:111], v1 offset:27648\n"
"ds_read_b128 a[112:115], v1 offset:28672\n"
"ds_read_b128 a[116:119], v1 offset:29696\n"
"v_mov_b32 v152, v142\n"
"v_mov_b32 v155, 0x100\n"
"v_mov_b32 v172, 3\n"
"s_add_u32 s23, s23, 1\n"
"s_mov_b32 s82, 0\n"
"s_add_u32 s27, s27, 524288\n"
"s_add_u32 s28, s28, 163840\n"
"s_add_u32 s62, s62, 65536\n"
"s_add_u32 s29, s29, 2621440\n"
"s_add_u32 s30, s30, 2883584\n"
"s_add_u32 s31, s31, 7077888\n"
"s_mul_i32 s52, s20, 5120\n"
"s_add_u32 s52, s52, s28\n"
"s_cmp_lt_u32 s21, 4\n"
"s_cselect_b32 s52, s52, 2147418112\n"
"s_lshl_b32 s53, s20, 2\n"
"s_add_u32 s53, s53, s21\n"
"s_lshl_b32 s53, s53, 7\n"
"s_add_u32 s53, s53, s62\n"
"s_cmp_lt_u32 s21, 4\n"
"s_cselect_b32 s53, s53, 2147418112\n"
"s_cmp_eq_u32 s22, 0\n"
"s_cselect_b32 s53, s53, 2147418112\n"
"s_mov_b64 exec, 1\n"
"s_waitcnt vmcnt(10)\n"
"v_readfirstlane_b32 s45, v108\n"
"s_nop 0\n"
"s_and_b32 s45, s45, 255\n"
"s_cmp_ge_u32 s45, 8\n"
"s_cbranch_scc1 Ldone57_%=\n"
"global_atomic_add v108, v3, v2, s[4:5] sc0\n"
"s_waitcnt vmcnt(10)\n"
"v_readfirstlane_b32 s45, v109\n"
"s_nop 0\n"
"s_and_b32 s45, s45, 255\n"
"s_cmp_ge_u32 s45, 8\n"
"s_cbranch_scc1 Ldone57_%=\n"
"global_atomic_add v109, v3, v2, s[4:5] sc0\n"
"s_waitcnt vmcnt(6)\n"
"v_readfirstlane_b32 s45, v110\n"
"s_nop 0\n"
"s_and_b32 s45, s45, 255\n"
"s_cmp_ge_u32 s45, 8\n"
"s_cbranch_scc1 Ldone57_%=\n"
"global_atomic_add v110, v3, v2, s[4:5] sc0\n"
"s_waitcnt vmcnt(3)\n"
"v_readfirstlane_b32 s45, v111\n"
"s_nop 0\n"
"s_and_b32 s45, s45, 255\n"
"s_cmp_ge_u32 s45, 8\n"
"s_cbranch_scc1 Ldone57_%=\n"
"global_atomic_add v111, v3, v2, s[4:5] sc0\n"
"Lpoll56_%=:\n"
"s_waitcnt vmcnt(3)\n"
"v_readfirstlane_b32 s45, v108\n"
"s_nop 0\n"
"s_and_b32 s45, s45, 255\n"
"s_cmp_ge_u32 s45, 8\n"
"s_cbranch_scc1 Ldone57_%=\n"
"global_atomic_add v108, v3, v2, s[4:5] sc0\n"
"s_waitcnt vmcnt(3)\n"
"v_readfirstlane_b32 s45, v109\n"
"s_nop 0\n"
"s_and_b32 s45, s45, 255\n"
"s_cmp_ge_u32 s45, 8\n"
"s_cbranch_scc1 Ldone57_%=\n"
"global_atomic_add v109, v3, v2, s[4:5] sc0\n"
"s_waitcnt vmcnt(3)\n"
"v_readfirstlane_b32 s45, v110\n"
"s_nop 0\n"
"s_and_b32 s45, s45, 255\n"
"s_cmp_ge_u32 s45, 8\n"
"s_cbranch_scc1 Ldone57_%=\n"
"global_atomic_add v110, v3, v2, s[4:5] sc0\n"
"s_waitcnt vmcnt(3)\n"
"v_readfirstlane_b32 s45, v111\n"
"s_nop 0\n"
"s_and_b32 s45, s45, 255\n"
"s_cmp_ge_u32 s45, 8\n"
"s_cbranch_scc1 Ldone57_%=\n"
"global_atomic_add v111, v3, v2, s[4:5] sc0\n"
"s_add_u32 s25, s25, 4\n"
"s_cmp_lt_u32 s25, 1048576\n"
"s_cbranch_scc1 Lpoll56_%=\n"
"Ldone57_%=:\n"
"s_mov_b64 exec, -1\n"
"s_add_u32 s46, s51, 20480\n"
"buffer_load_dwordx2 v[140:141], v1, s[12:15], s46 offen sc1\n"
"buffer_load_dwordx2 v[142:143], v1, s[12:15], s46 offen offset:1024 sc1\n"
"global_load_dwordx4 v[32:35], v1, s[64:65] offset:-4096 sc1\n"
"ds_read_b128 a[120:123], v1 offset:30720\n"
"ds_read_b128 a[124:127], v1 offset:31744\n"
"ds_read_b128 a[128:131], v1 offset:32768\n"
"global_load_dwordx4 v[36:39], v1, s[64:65] offset:-3072 sc1\n"
"ds_read_b128 a[132:135], v1 offset:33792\n"
"ds_read_b128 a[136:139], v1 offset:34816\n"
"ds_read_b128 a[140:143], v1 offset:35840\n"
"global_load_dwordx4 v[40:43], v1, s[64:65] offset:-2048 sc1\n"
"ds_read_b128 a[144:147], v1 offset:36864\n"
"ds_read_b128 a[148:151], v1 offset:37888\n"
"ds_read_b128 a[152:155], v1 offset:38912\n"
"ds_read_b128 a[156:159], v1 offset:39936\n"
"global_load_dwordx4 v[44:47], v1, s[64:65] offset:-1024 sc1\n"
"ds_read_b128 a[160:163], v1 offset:40960\n"
"ds_read_b128 a[164:167], v1 offset:41984\n"
"ds_read_b128 a[168:171], v1 offset:43008\n"
"global_load_dwordx4 v[48:51], v1, s[64:65] offset:0 sc1\n"
"ds_read_b128 a[172:175], v1 offset:44032\n"
"ds_read_b128 a[176:179], v1 offset:45056\n"
"ds_read_b128 a[180:183], v1 offset:46080\n"
"ds_read_b128 a[184:187], v1 offset:47104\n"
"global_load_dwordx4 v[52:55], v1, s[64:65] offset:1024 sc1\n"
"ds_read_b128 a[188:191], v1 offset:48128\n"
"ds_read_b128 a[192:195], v1 offset:49152\n"
"ds_read_b128 a[196:199], v1 offset:50176\n"
"global_load_dwordx4 v[56:59], v1, s[64:65] offset:2048 sc1\n"
"ds_read_b128 a[200:203], v1 offset:51200\n"
"ds_read_b128 a[204:207], v1 offset:52224\n"
"ds_read_b128 a[208:211], v1 offset:53248\n"
"global_load_dwordx4 v[60:63], v1, s[64:65] offset:3072 sc1\n"
"ds_read_b128 a[212:215], v1 offset:54272\n"
"ds_read_b128 a[216:219], v1 offset:55296\n"
"ds_read_b128 a[220:223], v1 offset:56320\n"
"ds_read_b128 a[224:227], v1 offset:57344\n"
"s_add_u32 s64, s64, 8192\n"
"s_addc_u32 s65, s65, 0\n"
"global_load_dwordx4 v[64:67], v1, s[64:65] offset:-4096 sc1\n"
"ds_read_b128 a[228:231], v1 offset:58368\n"
"ds_read_b128 a[232:235], v1 offset:59392\n"
"ds_read_b128 a[236:239], v1 offset:60416\n"
"global_load_dwordx4 v[68:71], v1, s[64:65] offset:-3072 sc1\n"
"ds_read_b128 a[240:243], v1 offset:61440\n"
"ds_read_b128 a[244:247], v1 offset:62464\n"
"ds_read_b128 a[248:251], v1 offset:63488\n"
"ds_read_b128 a[252:255], v1 offset:64512\n"
"s_lshl_b32 s46, s20, 12\n"
"s_add_u32 s46, s46, 1082368\n"
"v_lshl_add_u32 v153, v4, 7, s46\n"
"s_lshl_b32 s46, s26, 2\n"
"s_add_u32 s46, s46, s22\n"
"s_lshl_b32 s46, s46, 5\n"
"s_lshr_b32 s47, s6, 3\n"
"s_add_u32 s46, s46, s47\n"
"s_lshl_b32 s46, s46, 7\n"
"s_add_u32 s46, s46, 1082368\n"
"v_mov_b32 v154, s46\n"
"s_lshl_b32 s63, s26, 2\n"
"s_add_u32 s63, s63, s22\n"
"s_mul_i32 s63, s63, 5120\n"
"s_add_u32 s46, s63, s28\n"
"s_add_u32 s68, s4, s46\n"
"s_addc_u32 s69, s5, 0\n"
"v_and_b32 v144, 48, v4\n"
"v_lshlrev_b32 v145, 2, v4\n"
"v_cmp_eq_u32 vcc, s34, 1\n"
"v_add_u32 v148, 0, v144\n"
"v_add_u32 v149, 4, v144\n"
"v_add_u32 v150, 8, v144\n"
"v_add_u32 v151, 12, v144\n"
"v_cndmask_b32 v148, v145, v148, vcc\n"
"v_cndmask_b32 v149, v145, v149, vcc\n"
"v_cndmask_b32 v150, v145, v150, vcc\n"
"v_cndmask_b32 v151, v145, v151, vcc\n"
"s_waitcnt vmcnt(13)\n"
"s_barrier\n"
"Lre58_%=:\n"
"s_waitcnt vmcnt(8) lgkmcnt(15)\n"
"s_cmp_eq_u32 s34, 1\n"
"s_cbranch_scc1 Lva60_%=\n"
"v_mfma_f32_16x16x32_bf16 v[136:139], a[0:3], v[32:35], 0\n"
"ds_read_b128 v[176:179], v8 offset:0\n"
"v_add_f32 v140, v140, v142\n"
"v_mfma_f32_16x16x32_bf16 v[136:139], a[16:19], v[36:39], v[136:139]\n"
"global_load_dwordx4 v[72:75], v1, s[64:65] offset:-2048 sc1\n"
"v_add_f32 v141, v141, v143\n"
"v_mfma_f32_16x16x32_bf16 v[116:119], a[4:7], v[32:35], 0\n"
"ds_read_b128 v[180:183], v8 offset:1024\n"
"v_permlane16_swap_b32 v140, v141\n"
"v_mfma_f32_16x16x32_bf16 v[116:119], a[20:23], v[36:39], v[116:119]\n"
"ds_read_b128 v[184:187], v8 offset:2048\n"
"v_add_f32 v140, v140, v141\n"
"v_mfma_f32_16x16x32_bf16 v[120:123], a[8:11], v[32:35], 0\n"
"ds_read_b128 v[188:191], v8 offset:3072\n"
"v_mov_b32 v141, v140\n"
"v_mfma_f32_16x16x32_bf16 v[120:123], a[24:27], v[36:39], v[120:123]\n"
"global_load_dwordx4 v[76:79], v1, s[64:65] offset:-1024 sc1\n"
"v_permlane32_swap_b32 v140, v141\n"
"v_mfma_f32_16x16x32_bf16 v[124:127], a[12:15], v[32:35], 0\n"
"ds_read_b128 v[192:195], v8 offset:4096\n"
"v_add_f32 v140, v140, v141\n"
"v_mfma_f32_16x16x32_bf16 v[124:127], a[28:31], v[36:39], v[124:127]\n"
"s_waitcnt vmcnt(8)\n"
"ds_read_b128 v[196:199], v8 offset:5120\n"
"v_mov_b32 v141, v140\n"
"v_mfma_f32_16x16x32_bf16 v[136:139], a[32:35], v[40:43], v[136:139]\n"
"ds_read_b128 v[200:203], v8 offset:6144\n"
"v_permlane16_swap_b32 v140, v141\n"
"v_mfma_f32_16x16x32_bf16 v[136:139], a[48:51], v[44:47], v[136:139]\n"
"global_load_dwordx4 v[80:83], v1, s[64:65] offset:0 sc1\n"
"v_mul_f32 v140, 0xbacccccd, v140\n"
"v_mfma_f32_16x16x32_bf16 v[116:119], a[36:39], v[40:43], v[116:119]\n"
"ds_read_b128 v[204:207], v8 offset:7168\n"
"v_mul_f32 v141, 0x3acccccd, v141\n"
"v_mfma_f32_16x16x32_bf16 v[116:119], a[52:55], v[44:47], v[116:119]\n"
"ds_read_b128 v[208:211], v8 offset:8192\n"
"v_fma_f32 v141, -v140, v140, v141\n"
"v_mfma_f32_16x16x32_bf16 v[120:123], a[40:43], v[40:43], v[120:123]\n"
"ds_read_b128 v[212:215], v8 offset:9216\n"
"v_max_f32 v142, 0, v141\n"
"v_mfma_f32_16x16x32_bf16 v[120:123], a[56:59], v[44:47], v[120:123]\n"
"global_load_dwordx4 v[84:87], v1, s[64:65] offset:1024 sc1\n"
"v_fma_f32 v141, v141, 0, v142\n"
"v_mfma_f32_16x16x32_bf16 v[124:127], a[44:47], v[40:43], v[124:127]\n"
"ds_read_b128 v[216:219], v8 offset:10240\n"
"v_add_f32 v142, 0x3727c5ac, v141\n"
"v_mfma_f32_16x16x32_bf16 v[124:127], a[60:63], v[44:47], v[124:127]\n"
"s_waitcnt vmcnt(8)\n"
"ds_read_b128 v[220:223], v8 offset:11264\n"
"v_mfma_f32_16x16x32_bf16 v[136:139], a[64:67], v[48:51], v[136:139]\n"
"ds_read_b128 v[224:227], v8 offset:12288\n"
"v_mfma_f32_16x16x32_bf16 v[136:139], a[80:83], v[52:55], v[136:139]\n"
"global_load_dwordx4 v[88:91], v1, s[64:65] offset:2048 sc1\n"
"v_rsq_f32 v143, v142\n"
"v_and_b32 v115, 0xffff0000, v140\n"
"v_mfma_f32_16x16x32_bf16 v[116:119], a[68:71], v[48:51], v[116:119]\n"
"ds_read_b128 v[228:231], v8 offset:13312\n"
"v_sub_f32 v114, v140, v115\n"
"v_mfma_f32_16x16x32_bf16 v[116:119], a[84:87], v[52:55], v[116:119]\n"
"ds_read_b128 v[232:235], v8 offset:14336\n"
"v_cvt_pk_bf16_f32 v112, v115, v114\n"
"v_mfma_f32_16x16x32_bf16 v[120:123], a[72:75], v[48:51], v[120:123]\n"
"ds_read_b128 v[236:239], v8 offset:15360\n"
"v_mov_b32 v140, v143\n"
"v_mfma_f32_16x16x32_bf16 v[120:123], a[88:91], v[52:55], v[120:123]\n"
"global_load_dwordx4 v[92:95], v1, s[64:65] offset:3072 sc1\n"
"v_cvt_pk_bf16_f32 v113, v115, v2\n"
"v_mfma_f32_16x16x32_bf16 v[124:127], a[76:79], v[48:51], v[124:127]\n"
"ds_read_b128 v[240:243], v8 offset:16384\n"
"v_mov_b32 v114, 0\n"
"v_mfma_f32_16x16x32_bf16 v[124:127], a[92:95], v[52:55], v[124:127]\n"
"s_waitcnt vmcnt(8)\n"
"ds_read_b128 v[244:247], v8 offset:17408\n"
"v_mov_b32 v115, 0\n"
"v_mfma_f32_16x16x32_bf16 v[136:139], a[96:99], v[56:59], v[136:139]\n"
"ds_read_b128 v[248:251], v8 offset:18432\n"
"v_mfma_f32_16x16x32_bf16 v[136:139], a[112:115], v[60:63], v[136:139]\n"
"s_add_u32 s64, s64, 8192\n"
"s_addc_u32 s65, s65, 0\n"
"ds_bpermute_b32 v148, v148, v140\n"
"global_load_dwordx4 v[96:99], v1, s[64:65] offset:-4096 sc1\n"
"v_mfma_f32_16x16x32_bf16 v[116:119], a[100:103], v[56:59], v[116:119]\n"
"ds_read_b128 v[252:255], v8 offset:19456\n"
"v_mfma_f32_16x16x32_bf16 v[116:119], a[116:119], v[60:63], v[116:119]\n"
"ds_bpermute_b32 v149, v149, v140\n"
"ds_read_b128 a[0:3], v8 offset:20480\n"
"v_mfma_f32_16x16x32_bf16 v[120:123], a[104:107], v[56:59], v[120:123]\n"
"ds_read_b128 a[4:7], v8 offset:21504\n"
"v_mfma_f32_16x16x32_bf16 v[120:123], a[120:123], v[60:63], v[120:123]\n"
"ds_bpermute_b32 v150, v150, v140\n"
"global_load_dwordx4 v[100:103], v1, s[64:65] offset:-3072 sc1\n"
"v_mfma_f32_16x16x32_bf16 v[124:127], a[108:111], v[56:59], v[124:127]\n"
"ds_read_b128 a[8:11], v8 offset:22528\n"
"v_mfma_f32_16x16x32_bf16 v[124:127], a[124:127], v[60:63], v[124:127]\n"
"s_waitcnt vmcnt(8)\n"
"ds_bpermute_b32 v151, v151, v140\n"
"ds_read_b128 a[12:15], v8 offset:23552\n"
"v_mul_f32 v142, 0x3ea52b5c, v140\n"
"v_mfma_f32_16x16x32_bf16 v[136:139], a[128:131], v[64:67], v[136:139]\n"
"ds_read_b128 a[16:19], v8 offset:24576\n"
"v_mfma_f32_16x16x32_bf16 v[136:139], a[144:147], v[68:71], v[136:139]\n"
"global_load_dwordx4 v[104:107], v1, s[64:65] offset:-2048 sc1\n"
"v_mfma_f32_16x16x32_bf16 v[116:119], a[132:135], v[64:67], v[116:119]\n"
"ds_read_b128 a[20:23], v8 offset:25600\n"
"v_mfma_f32_16x16x32_bf16 v[116:119], a[148:151], v[68:71], v[116:119]\n"
"ds_read_b128 a[24:27], v8 offset:26624\n"
"v_mfma_f32_16x16x32_bf16 v[120:123], a[136:139], v[64:67], v[120:123]\n"
"ds_read_b128 a[28:31], v8 offset:27648\n"
"v_mfma_f32_16x16x32_bf16 v[120:123], a[152:155], v[68:71], v[120:123]\n"
"global_load_dwordx4 v[108:111], v1, s[64:65] offset:-1024 sc1\n"
"v_mfma_f32_16x16x32_bf16 v[124:127], a[140:143], v[64:67], v[124:127]\n"
"ds_read_b128 a[32:35], v8 offset:28672\n"
"v_mfma_f32_16x16x32_bf16 v[124:127], a[156:159], v[68:71], v[124:127]\n"
"s_waitcnt vmcnt(8)\n"
"ds_read_b128 a[36:39], v8 offset:29696\n"
"v_mfma_f32_16x16x32_bf16 v[136:139], a[160:163], v[72:75], v[136:139]\n"
"ds_read_b128 a[40:43], v8 offset:30720\n"
"v_mfma_f32_16x16x32_bf16 v[136:139], a[176:179], v[76:79], v[136:139]\n"
"ds_read_b128 a[44:47], v8 offset:31744\n"
"v_mfma_f32_16x16x32_bf16 v[116:119], a[164:167], v[72:75], v[116:119]\n"
"ds_read_b128 a[48:51], v8 offset:32768\n"
"v_mfma_f32_16x16x32_bf16 v[116:119], a[180:183], v[76:79], v[116:119]\n"
"ds_read_b128 a[52:55], v8 offset:33792\n"
"v_mfma_f32_16x16x32_bf16 v[120:123], a[168:171], v[72:75], v[120:123]\n"
"ds_read_b128 a[56:59], v8 offset:34816\n"
"v_mfma_f32_16x16x32_bf16 v[120:123], a[184:187], v[76:79], v[120:123]\n"
"s_mov_b64 exec, 0xff\n"
"global_atomic_add v152, v155, s[4:5]\n"
"s_mov_b64 exec, -1\n"
"ds_read_b128 a[60:63], v8 offset:35840\n"
"v_mfma_f32_16x16x32_bf16 v[124:127], a[172:175], v[72:75], v[124:127]\n"
"ds_read_b128 a[64:67], v8 offset:36864\n"
"v_mfma_f32_16x16x32_bf16 v[124:127], a[188:191], v[76:79], v[124:127]\n"
"s_waitcnt vmcnt(7) lgkmcnt(15)\n"
"ds_read_b128 a[68:71], v8 offset:37888\n"
"v_mfma_f32_16x16x32_bf16 v[136:139], a[192:195], v[80:83], v[136:139]\n"
"ds_read_b128 a[72:75], v8 offset:38912\n"
"v_mfma_f32_16x16x32_bf16 v[136:139], a[208:211], v[84:87], v[136:139]\n"
"ds_read_b128 a[76:79], v8 offset:39936\n"
"v_mfma_f32_16x16x32_bf16 v[116:119], a[196:199], v[80:83], v[116:119]\n"
"ds_read_b128 a[80:83], v8 offset:40960\n"
"v_mfma_f32_16x16x32_bf16 v[116:119], a[212:215], v[84:87], v[116:119]\n"
"ds_read_b128 a[84:87], v8 offset:41984\n"
"v_mfma_f32_16x16x32_bf16 v[120:123], a[200:203], v[80:83], v[120:123]\n"
"ds_read_b128 a[88:91], v8 offset:43008\n"
"v_mfma_f32_16x16x32_bf16 v[120:123], a[216:219], v[84:87], v[120:123]\n"
"ds_read_b128 a[92:95], v8 offset:44032\n"
"v_mfma_f32_16x16x32_bf16 v[124:127], a[204:207], v[80:83], v[124:127]\n"
"ds_read_b128 a[96:99], v8 offset:45056\n"
"v_mfma_f32_16x16x32_bf16 v[124:127], a[220:223], v[84:87], v[124:127]\n"
"s_waitcnt vmcnt(5)\n"
"ds_read_b128 a[100:103], v8 offset:46080\n"
"v_mfma_f32_16x16x32_bf16 v[136:139], a[224:227], v[88:91], v[136:139]\n"
"ds_read_b128 a[104:107], v8 offset:47104\n"
"v_mfma_f32_16x16x32_bf16 v[136:139], a[240:243], v[92:95], v[136:139]\n"
"ds_read_b128 a[108:111], v8 offset:48128\n"
"v_mfma_f32_16x16x32_bf16 v[116:119], a[228:231], v[88:91], v[116:119]\n"
"ds_read_b128 a[112:115], v8 offset:49152\n"
"v_mfma_f32_16x16x32_bf16 v[116:119], a[244:247], v[92:95], v[116:119]\n"
"ds_read_b128 a[116:119], v8 offset:50176\n"
"v_mfma_f32_16x16x32_bf16 v[120:123], a[232:235], v[88:91], v[120:123]\n"
"ds_read_b128 a[120:123], v8 offset:51200\n"
"v_mfma_f32_16x16x32_bf16 v[120:123], a[248:251], v[92:95], v[120:123]\n"
"ds_read_b128 a[124:127], v8 offset:52224\n"
"v_mfma_f32_16x16x32_bf16 v[124:127], a[236:239], v[88:91], v[124:127]\n"
"ds_read_b128 a[128:131], v8 offset:53248\n"
"v_mfma_f32_16x16x32_bf16 v[124:127], a[252:255], v[92:95], v[124:127]\n"
"s_waitcnt vmcnt(3)\n"
"ds_read_b128 a[132:135], v8 offset:54272\n"
"v_mfma_f32_16x16x32_bf16 v[136:139], v[176:179], v[96:99], v[136:139]\n"
"ds_read_b128 a[136:139], v8 offset:55296\n"
"v_mfma_f32_16x16x32_bf16 v[136:139], v[192:195], v[100:103], v[136:139]\n"
"ds_read_b128 a[140:143], v8 offset:56320\n"
"v_mfma_f32_16x16x32_bf16 v[116:119], v[180:183], v[96:99], v[116:119]\n"
"ds_read_b128 a[144:147], v8 offset:57344\n"
"v_mfma_f32_16x16x32_bf16 v[116:119], v[196:199], v[100:103], v[116:119]\n"
"ds_read_b128 a[148:151], v8 offset:58368\n"
"v_mfma_f32_16x16x32_bf16 v[120:123], v[184:187], v[96:99], v[120:123]\n"
"ds_read_b128 a[152:155], v8 offset:59392\n"
"v_mfma_f32_16x16x32_bf16 v[120:123], v[200:203], v[100:103], v[120:123]\n"
"ds_read_b128 a[156:159], v8 offset:60416\n"
"v_mfma_f32_16x16x32_bf16 v[124:127], v[188:191], v[96:99], v[124:127]\n"
"ds_read_b128 a[160:163], v8 offset:61440\n"
"v_mfma_f32_16x16x32_bf16 v[124:127], v[204:207], v[100:103], v[124:127]\n"
"s_waitcnt vmcnt(1)\n"
"ds_read_b128 a[164:167], v8 offset:62464\n"
"v_mfma_f32_16x16x32_bf16 v[136:139], v[208:211], v[104:107], v[136:139]\n"
"v_mfma_f32_16x16x32_bf16 v[136:139], v[224:227], v[108:111], v[136:139]\n"
"v_mfma_f32_16x16x32_bf16 v[116:119], v[212:215], v[104:107], v[116:119]\n"
"v_mfma_f32_16x16x32_bf16 v[116:119], v[228:231], v[108:111], v[116:119]\n"
"v_mfma_f32_16x16x32_bf16 v[120:123], v[216:219], v[104:107], v[120:123]\n"
"v_mfma_f32_16x16x32_bf16 v[120:123], v[232:235], v[108:111], v[120:123]\n"
"v_mfma_f32_16x16x32_bf16 v[124:127], v[220:223], v[104:107], v[124:127]\n"
"v_mfma_f32_16x16x32_bf16 v[124:127], v[236:239], v[108:111], v[124:127]\n"
"s_waitcnt lgkmcnt(15)\n"
"v_mfma_f32_16x16x32_bf16 v[136:139], v[240:243], v[112:115], v[136:139]\n"
"s_branch Lve61_%=\n"
"Lva60_%=:\n"
"v_mfma_f32_16x16x32_bf16 v[136:139], v[32:35], a[0:3], 0\n"
"ds_read_b128 v[176:179], v8 offset:0\n"
"v_add_f32 v140, v140, v142\n"
"v_mfma_f32_16x16x32_bf16 v[136:139], v[36:39], a[16:19], v[136:139]\n"
"global_load_dwordx4 v[72:75], v1, s[64:65] offset:-2048 sc1\n"
"v_add_f32 v141, v141, v143\n"
"v_mfma_f32_16x16x32_bf16 v[116:119], a[4:7], v[32:35], 0\n"
"ds_read_b128 v[180:183], v8 offset:1024\n"
"v_permlane16_swap_b32 v140, v141\n"
"v_mfma_f32_16x16x32_bf16 v[116:119], a[20:23], v[36:39], v[116:119]\n"
"ds_read_b128 v[184:187], v8 offset:2048\n"
"v_add_f32 v140, v140, v141\n"
"v_mfma_f32_16x16x32_bf16 v[120:123], a[8:11], v[32:35], 0\n"
"ds_read_b128 v[188:191], v8 offset:3072\n"
"v_mov_b32 v141, v140\n"
"v_mfma_f32_16x16x32_bf16 v[120:123], a[24:27], v[36:39], v[120:123]\n"
"global_load_dwordx4 v[76:79], v1, s[64:65] offset:-1024 sc1\n"
"v_permlane32_swap_b32 v140, v141\n"
"v_mfma_f32_16x16x32_bf16 v[124:127], a[12:15], v[32:35], 0\n"
"ds_read_b128 v[192:195], v8 offset:4096\n"
"v_add_f32 v140, v140, v141\n"
"v_mfma_f32_16x16x32_bf16 v[124:127], a[28:31], v[36:39], v[124:127]\n"
"s_waitcnt vmcnt(8)\n"
"ds_read_b128 v[196:199], v8 offset:5120\n"
"v_mov_b32 v141, v140\n"
"v_mfma_f32_16x16x32_bf16 v[136:139], v[40:43], a[32:35], v[136:139]\n"
"ds_read_b128 v[200:203], v8 offset:6144\n"
"v_permlane16_swap_b32 v140, v141\n"
"v_mfma_f32_16x16x32_bf16 v[136:139], v[44:47], a[48:51], v[136:139]\n"
"global_load_dwordx4 v[80:83], v1, s[64:65] offset:0 sc1\n"
"v_mul_f32 v140, 0xbacccccd, v140\n"
"v_mfma_f32_16x16x32_bf16 v[116:119], a[36:39], v[40:43], v[116:119]\n"
"Lpad12_%=:\n"
"s_cbranch_execz Lpadback2_%=\n"
"ds_read_b128 v[204:207], v8 offset:7168\n"
"v_mul_f32 v141, 0x3acccccd, v141\n"
"v_mfma_f32_16x16x32_bf16 v[116:119], a[52:55], v[44:47], v[116:119]\n"
"ds_read_b128 v[208:211], v8 offset:8192\n"
"v_fma_f32 v141, -v140, v140, v141\n"
"v_mfma_f32_16x16x32_bf16 v[120:123], a[40:43], v[40:43], v[120:123]\n"
"ds_read_b128 v[212:215], v8 offset:9216\n"
"v_max_f32 v142, 0, v141\n"
"v_mfma_f32_16x16x32_bf16 v[120:123], a[56:59], v[44:47], v[120:123]\n"
"global_load_dwordx4 v[84:87], v1, s[64:65] offset:1024 sc1\n"
"v_fma_f32 v141, v141, 0, v142\n"
"v_mfma_f32_16x16x32_bf16 v[124:127], a[44:47], v[40:43], v[124:127]\n"
"ds_read_b128 v[216:219], v8 offset:10240\n"
"v_add_f32 v142, 0x3727c5ac, v141\n"
"v_mfma_f32_16x16x32_bf16 v[124:127], a[60:63], v[44:47], v[124:127]\n"
"s_waitcnt vmcnt(8)\n"
"ds_read_b128 v[220:223], v8 offset:11264\n"
"v_mfma_f32_16x16x32_bf16 v[136:139], v[48:51], a[64:67], v[136:139]\n"
"ds_read_b128 v[224:227], v8 offset:12288\n"
"v_mfma_f32_16x16x32_bf16 v[136:139], v[52:55], a[80:83], v[136:139]\n"
"global_load_dwordx4 v[88:91], v1, s[64:65] offset:2048 sc1\n"
"v_rsq_f32 v143, v142\n"
"v_and_b32 v115, 0xffff0000, v140\n"
"v_mfma_f32_16x16x32_bf16 v[116:119], a[68:71], v[48:51], v[116:119]\n"
"ds_read_b128 v[228:231], v8 offset:13312\n"
"v_sub_f32 v114, v140, v115\n"
"v_mfma_f32_16x16x32_bf16 v[116:119], a[84:87], v[52:55], v[116:119]\n"
"ds_read_b128 v[232:235], v8 offset:14336\n"
"v_cvt_pk_bf16_f32 v112, v115, v114\n"
"v_mfma_f32_16x16x32_bf16 v[120:123], a[72:75], v[48:51], v[120:123]\n"
"ds_read_b128 v[236:239], v8 offset:15360\n"
"v_mov_b32 v140, v143\n"
"v_mfma_f32_16x16x32_bf16 v[120:123], a[88:91], v[52:55], v[120:123]\n"
"global_load_dwordx4 v[92:95], v1, s[64:65] offset:3072 sc1\n"
"v_cvt_pk_bf16_f32 v113, v115, v2\n"
"v_mfma_f32_16x16x32_bf16 v[124:127], a[76:79], v[48:51], v[124:127]\n"
"ds_read_b128 v[240:243], v8 offset:16384\n"
"v_mov_b32 v114, 0\n"
"v_mfma_f32_16x16x32_bf16 v[124:127], a[92:95], v[52:55], v[124:127]\n"
"s_waitcnt vmcnt(8)\n"
"ds_read_b128 v[244:247], v8 offset:17408\n"
"v_mov_b32 v115, 0\n"
"v_mfma_f32_16x16x32_bf16 v[136:139], v[56:59], a[96:99], v[136:139]\n"
"ds_read_b128 v[248:251], v8 offset:18432\n"
"v_mfma_f32_16x16x32_bf16 v[136:139], v[60:63], a[112:115], v[136:139]\n"
"s_add_u32 s64, s64, 8192\n"
"s_addc_u32 s65, s65, 0\n"
"ds_bpermute_b32 v148, v148, v140\n"
"global_load_dwordx4 v[96:99], v1, s[64:65] offset:-4096 sc1\n"
"v_mfma_f32_16x16x32_bf16 v[116:119], a[100:103], v[56:59], v[116:119]\n"
"ds_read_b128 v[252:255], v8 offset:19456\n"
"v_mfma_f32_16x16x32_bf16 v[116:119], a[116:119], v[60:63], v[116:119]\n"
"ds_bpermute_b32 v149, v149, v140\n"
"ds_read_b128 a[0:3], v8 offset:20480\n"
"v_mfma_f32_16x16x32_bf16 v[120:123], a[104:107], v[56:59], v[120:123]\n"
"ds_read_b128 a[4:7], v8 offset:21504\n"
"v_mfma_f32_16x16x32_bf16 v[120:123], a[120:123], v[60:63], v[120:123]\n"
"ds_bpermute_b32 v150, v150, v140\n"
"global_load_dwordx4 v[100:103], v1, s[64:65] offset:-3072 sc1\n"
"v_mfma_f32_16x16x32_bf16 v[124:127], a[108:111], v[56:59], v[124:127]\n"
"ds_read_b128 a[8:11], v8 offset:22528\n"
"v_mfma_f32_16x16x32_bf16 v[124:127], a[124:127], v[60:63], v[124:127]\n"
"s_waitcnt vmcnt(8)\n"
"ds_bpermute_b32 v151, v151, v140\n"
"ds_read_b128 a[12:15], v8 offset:23552\n"
"v_mul_f32 v142, 0x3ea52b5c, v140\n"
"v_mfma_f32_16x16x32_bf16 v[136:139], v[64:67], a[128:131], v[136:139]\n"
"ds_read_b128 a[16:19], v8 offset:24576\n"
"v_mfma_f32_16x16x32_bf16 v[136:139], v[68:71], a[144:147], v[136:139]\n"
"global_load_dwordx4 v[104:107], v1, s[64:65] offset:-2048 sc1\n"
"v_mfma_f32_16x16x32_bf16 v[116:119], a[132:135], v[64:67], v[116:119]\n"
"ds_read_b128 a[20:23], v8 offset:25600\n"
"v_mfma_f32_16x16x32_bf16 v[116:119], a[148:151], v[68:71], v[116:119]\n"
"ds_read_b128 a[24:27], v8 offset:26624\n"
"v_mfma_f32_16x16x32_bf16 v[120:123], a[136:139], v[64:67], v[120:123]\n"
"ds_read_b128 a[28:31], v8 offset:27648\n"
"v_mfma_f32_16x16x32_bf16 v[120:123], a[152:155], v[68:71], v[120:123]\n"
"global_load_dwordx4 v[108:111], v1, s[64:65] offset:-1024 sc1\n"
"v_mfma_f32_16x16x32_bf16 v[124:127], a[140:143], v[64:67], v[124:127]\n"
"ds_read_b128 a[32:35], v8 offset:28672\n"
"v_mfma_f32_16x16x32_bf16 v[124:127], a[156:159], v[68:71], v[124:127]\n"
"s_waitcnt vmcnt(8)\n"
"ds_read_b128 a[36:39], v8 offset:29696\n"
"v_mfma_f32_16x16x32_bf16 v[136:139], v[72:75], a[160:163], v[136:139]\n"
"ds_read_b128 a[40:43], v8 offset:30720\n"
"v_mfma_f32_16x16x32_bf16 v[136:139], v[76:79], a[176:179], v[136:139]\n"
"ds_read_b128 a[44:47], v8 offset:31744\n"
"v_mfma_f32_16x16x32_bf16 v[116:119], a[164:167], v[72:75], v[116:119]\n"
"ds_read_b128 a[48:51], v8 offset:32768\n"
"v_mfma_f32_16x16x32_bf16 v[116:119], a[180:183], v[76:79], v[116:119]\n"
"ds_read_b128 a[52:55], v8 offset:33792\n"
"v_mfma_f32_16x16x32_bf16 v[120:123], a[168:171], v[72:75], v[120:123]\n"
"ds_read_b128 a[56:59], v8 offset:34816\n"
"v_mfma_f32_16x16x32_bf16 v[120:123], a[184:187], v[76:79], v[120:123]\n"
"s_mov_b64 exec, 0xff\n"
"global_atomic_add v152, v155, s[4:5]\n"
"s_mov_b64 exec, -1\n"
"ds_read_b128 a[60:63], v8 offset:35840\n"
"v_mfma_f32_16x16x32_bf16 v[124:127], a[172:175], v[72:75], v[124:127]\n"
"ds_read_b128 a[64:67], v8 offset:36864\n"
"v_mfma_f32_16x16x32_bf16 v[124:127], a[188:191], v[76:79], v[124:127]\n"
"s_waitcnt vmcnt(7) lgkmcnt(15)\n"
"ds_read_b128 a[68:71], v8 offset:37888\n"
"v_mfma_f32_16x16x32_bf16 v[136:139], v[80:83], a[192:195], v[136:139]\n"
"ds_read_b128 a[72:75], v8 offset:38912\n"
"v_mfma_f32_16x16x32_bf16 v[136:139], v[84:87], a[208:211], v[136:139]\n"
"ds_read_b128 a[76:79], v8 offset:39936\n"
"v_mfma_f32_16x16x32_bf16 v[116:119], a[196:199], v[80:83], v[116:119]\n"
"ds_read_b128 a[80:83], v8 offset:40960\n"
"v_mfma_f32_16x16x32_bf16 v[116:119], a[212:215], v[84:87], v[116:119]\n"
"ds_read_b128 a[84:87], v8 offset:41984\n"
"v_mfma_f32_16x16x32_bf16 v[120:123], a[200:203], v[80:83], v[120:123]\n"
"ds_read_b128 a[88:91], v8 offset:43008\n"
"v_mfma_f32_16x16x32_bf16 v[120:123], a[216:219], v[84:87], v[120:123]\n"
"ds_read_b128 a[92:95], v8 offset:44032\n"
"v_mfma_f32_16x16x32_bf16 v[124:127], a[204:207], v[80:83], v[124:127]\n"
"ds_read_b128 a[96:99], v8 offset:45056\n"
"v_mfma_f32_16x16x32_bf16 v[124:127], a[220:223], v[84:87], v[124:127]\n"
"s_waitcnt vmcnt(5)\n"
"ds_read_b128 a[100:103], v8 offset:46080\n"
"v_mfma_f32_16x16x32_bf16 v[136:139], v[88:91], a[224:227], v[136:139]\n"
"ds_read_b128 a[104:107], v8 offset:47104\n"
"v_mfma_f32_16x16x32_bf16 v[136:139], v[92:95], a[240:243], v[136:139]\n"
"ds_read_b128 a[108:111], v8 offset:48128\n"
"v_mfma_f32_16x16x32_bf16 v[116:119], a[228:231], v[88:91], v[116:119]\n"
"ds_read_b128 a[112:115], v8 offset:49152\n"
"v_mfma_f32_16x16x32_bf16 v[116:119], a[244:247], v[92:95], v[116:119]\n"
"ds_read_b128 a[116:119], v8 offset:50176\n"
"v_mfma_f32_16x16x32_bf16 v[120:123], a[232:235], v[88:91], v[120:123]\n"
"ds_read_b128 a[120:123], v8 offset:51200\n"
"v_mfma_f32_16x16x32_bf16 v[120:123], a[248:251], v[92:95], v[120:123]\n"
"ds_read_b128 a[124:127], v8 offset:52224\n"
"v_mfma_f32_16x16x32_bf16 v[124:127], a[236:239], v[88:91], v[124:127]\n"
"ds_read_b128 a[128:131], v8 offset:53248\n"
"v_mfma_f32_16x16x32_bf16 v[124:127], a[252:255], v[92:95], v[124:127]\n"
"s_waitcnt vmcnt(3)\n"
"ds_read_b128 a[132:135], v8 offset:54272\n"
"v_mfma_f32_16x16x32_bf16 v[136:139], v[96:99], v[176:179], v[136:139]\n"
"ds_read_b128 a[136:139], v8 offset:55296\n"
"v_mfma_f32_16x16x32_bf16 v[136:139], v[100:103], v[192:195], v[136:139]\n"
"ds_read_b128 a[140:143], v8 offset:56320\n"
"v_mfma_f32_16x16x32_bf16 v[116:119], v[180:183], v[96:99], v[116:119]\n"
"ds_read_b128 a[144:147], v8 offset:57344\n"
"v_mfma_f32_16x16x32_bf16 v[116:119], v[196:199], v[100:103], v[116:119]\n"
"ds_read_b128 a[148:151], v8 offset:58368\n"
"v_mfma_f32_16x16x32_bf16 v[120:123], v[184:187], v[96:99], v[120:123]\n"
"ds_read_b128 a[152:155], v8 offset:59392\n"
"v_mfma_f32_16x16x32_bf16 v[120:123], v[200:203], v[100:103], v[120:123]\n"
"ds_read_b128 a[156:159], v8 offset:60416\n"
"v_mfma_f32_16x16x32_bf16 v[124:127], v[188:191], v[96:99], v[124:127]\n"
"ds_read_b128 a[160:163], v8 offset:61440\n"
"v_mfma_f32_16x16x32_bf16 v[124:127], v[204:207], v[100:103], v[124:127]\n"
"s_waitcnt vmcnt(1)\n"
"ds_read_b128 a[164:167], v8 offset:62464\n"
"v_mfma_f32_16x16x32_bf16 v[136:139], v[104:107], v[208:211], v[136:139]\n"
"v_mfma_f32_16x16x32_bf16 v[136:139], v[108:111], v[224:227], v[136:139]\n"
"v_mfma_f32_16x16x32_bf16 v[116:119], v[212:215], v[104:107], v[116:119]\n"
"v_mfma_f32_16x16x32_bf16 v[116:119], v[228:231], v[108:111], v[116:119]\n"
"v_mfma_f32_16x16x32_bf16 v[120:123], v[216:219], v[104:107], v[120:123]\n"
"v_mfma_f32_16x16x32_bf16 v[120:123], v[232:235], v[108:111], v[120:123]\n"
"v_mfma_f32_16x16x32_bf16 v[124:127], v[220:223], v[104:107], v[124:127]\n"
"v_mfma_f32_16x16x32_bf16 v[124:127], v[236:239], v[108:111], v[124:127]\n"
"s_waitcnt lgkmcnt(15)\n"
"v_mfma_f32_16x16x32_bf16 v[136:139], v[112:115], v[240:243], v[136:139]\n"
"Lve61_%=:\n"
"s_nop 1\n"
"v_mul_f32 v156, v116, v140\n"
"v_cmp_class_f32 vcc, v156, v172\n"
"s_nop 4\n"
"s_cbranch_vccnz Lse59_%=\n"
"v_mul_f32 v136, v136, v148\n"
"v_mul_f32 v137, v137, v149\n"
"v_mul_f32 v138, v138, v150\n"
"v_mul_f32 v139, v139, v151\n"
"v_cvt_pk_bf16_f32 v136, v136, v137\n"
"v_cvt_pk_bf16_f32 v137, v138, v139\n"
"s_mov_b64 exec, s[38:39]\n"
"buffer_store_dwordx2 v[136:137], v7, s[12:15], s52 offen sc1\n"
"s_mov_b64 exec, -1\n"
"s_cmp_ge_u32 s21, 4\n"
"s_cbranch_scc1 Lka62_%=\n"
"s_mov_b32 exec_hi, 0\n"
"global_atomic_add v153, v0, s[4:5]\n"
"s_mov_b64 exec, -1\n"
"Lka62_%=:\n"
"s_mov_b64 exec, 1\n"
"global_atomic_add v168, v154, v2, s[4:5] sc0\n"
"s_mov_b64 exec, -1\n"
"s_waitcnt lgkmcnt(0)\n"
"v_mfma_f32_16x16x32_bf16 v[128:131], v[244:247], v[32:35], 0\n"
"v_mfma_f32_16x16x32_bf16 v[128:131], v[252:255], v[36:39], v[128:131]\n"
"v_mfma_f32_16x16x32_bf16 v[128:131], a[4:7], v[40:43], v[128:131]\n"
"v_mfma_f32_16x16x32_bf16 v[128:131], a[12:15], v[44:47], v[128:131]\n"
"v_mfma_f32_16x16x32_bf16 v[128:131], a[20:23], v[48:51], v[128:131]\n"
"v_mfma_f32_16x16x32_bf16 v[128:131], a[28:31], v[52:55], v[128:131]\n"
"v_mfma_f32_16x16x32_bf16 v[128:131], a[36:39], v[56:59], v[128:131]\n"
"v_mfma_f32_16x16x32_bf16 v[128:131], a[44:47], v[60:63], v[128:131]\n"
"v_mfma_f32_16x16x32_bf16 v[128:131], a[52:55], v[64:67], v[128:131]\n"
"v_mfma_f32_16x16x32_bf16 v[128:131], a[60:63], v[68:71], v[128:131]\n"
"v_mfma_f32_16x16x32_bf16 v[128:131], a[68:71], v[72:75], v[128:131]\n"
"v_mfma_f32_16x16x32_bf16 v[128:131], a[76:79], v[76:79], v[128:131]\n"
"s_mov_b64 exec, 1\n"
"global_atomic_add v169, v154, v2, s[4:5] sc0\n"
"s_mov_b64 exec, -1\n"
"v_mfma_f32_16x16x32_bf16 v[128:131], a[84:87], v[80:83], v[128:131]\n"
"v_mfma_f32_16x16x32_bf16 v[128:131], a[92:95], v[84:87], v[128:131]\n"
"v_mfma_f32_16x16x32_bf16 v[128:131], a[100:103], v[88:91], v[128:131]\n"
"v_mfma_f32_16x16x32_bf16 v[128:131], a[108:111], v[92:95], v[128:131]\n"
"v_mfma_f32_16x16x32_bf16 v[128:131], a[116:119], v[96:99], v[128:131]\n"
"v_mfma_f32_16x16x32_bf16 v[128:131], a[124:127], v[100:103], v[128:131]\n"
"v_mfma_f32_16x16x32_bf16 v[128:131], a[132:135], v[104:107], v[128:131]\n"
"v_mfma_f32_16x16x32_bf16 v[128:131], a[140:143], v[108:111], v[128:131]\n"
"v_mfma_f32_16x16x32_bf16 v[128:131], a[160:163], v[112:115], v[128:131]\n"
"v_mfma_f32_16x16x32_bf16 v[132:135], v[248:251], v[32:35], 0\n"
"v_mfma_f32_16x16x32_bf16 v[132:135], a[0:3], v[36:39], v[132:135]\n"
"v_mfma_f32_16x16x32_bf16 v[132:135], a[8:11], v[40:43], v[132:135]\n"
"s_mov_b64 exec, 1\n"
"global_atomic_add v170, v154, v2, s[4:5] sc0\n"
"s_mov_b64 exec, -1\n"
"v_mfma_f32_16x16x32_bf16 v[132:135], a[16:19], v[44:47], v[132:135]\n"
"v_mfma_f32_16x16x32_bf16 v[132:135], a[24:27], v[48:51], v[132:135]\n"
"v_mfma_f32_16x16x32_bf16 v[132:135], a[32:35], v[52:55], v[132:135]\n"
"v_mfma_f32_16x16x32_bf16 v[132:135], a[40:43], v[56:59], v[132:135]\n"
"v_mfma_f32_16x16x32_bf16 v[132:135], a[48:51], v[60:63], v[132:135]\n"
"v_mfma_f32_16x16x32_bf16 v[132:135], a[56:59], v[64:67], v[132:135]\n"
"v_mfma_f32_16x16x32_bf16 v[132:135], a[64:67], v[68:71], v[132:135]\n"
"v_mfma_f32_16x16x32_bf16 v[132:135], a[72:75], v[72:75], v[132:135]\n"
"v_mfma_f32_16x16x32_bf16 v[132:135], a[80:83], v[76:79], v[132:135]\n"
"v_mfma_f32_16x16x32_bf16 v[132:135], a[88:91], v[80:83], v[132:135]\n"
"v_mfma_f32_16x16x32_bf16 v[132:135], a[96:99], v[84:87], v[132:135]\n"
"v_mfma_f32_16x16x32_bf16 v[132:135], a[104:107], v[88:91], v[132:135]\n"
"s_mov_b64 exec, 1\n"
"global_atomic_add v171, v154, v2, s[4:5] sc0\n"
"s_mov_b64 exec, -1\n"
"v_mfma_f32_16x16x32_bf16 v[132:135], a[112:115], v[92:95], v[132:135]\n"
"v_mfma_f32_16x16x32_bf16 v[132:135], a[120:123], v[96:99], v[132:135]\n"
"v_mfma_f32_16x16x32_bf16 v[132:135], a[128:131], v[100:103], v[132:135]\n"
"v_mfma_f32_16x16x32_bf16 v[132:135], a[136:139], v[104:107], v[132:135]\n"
"v_mfma_f32_16x16x32_bf16 v[132:135], a[144:147], v[108:111], v[132:135]\n"
"v_mfma_f32_16x16x32_bf16 v[132:135], a[164:167], v[112:115], v[132:135]\n"
"v_mfma_f32_16x16x32_bf16 v[116:119], a[148:151], v[112:115], v[116:119]\n"
"v_mfma_f32_16x16x32_bf16 v[120:123], a[152:155], v[112:115], v[120:123]\n"
"v_mfma_f32_16x16x32_bf16 v[124:127], a[156:159], v[112:115], v[124:127]\n"
"s_mov_b64 exec, 1\n"
"s_waitcnt vmcnt(3)\n"
"v_readfirstlane_b32 s45, v168\n"
"s_nop 0\n"
"s_cmp_ge_u32 s45, 16\n"
"s_cbranch_scc1 Ldone64_%=\n"
"global_atomic_add v168, v154, v2, s[4:5] sc0\n"
"s_waitcnt vmcnt(3)\n"
"v_readfirstlane_b32 s45, v169\n"
"s_nop 0\n"
"s_cmp_ge_u32 s45, 16\n"
"s_cbranch_scc1 Ldone64_%=\n"
"global_atomic_add v169, v154, v2, s[4:5] sc0\n"
"s_waitcnt vmcnt(3)\n"
"v_readfirstlane_b32 s45, v170\n"
"s_nop 0\n"
"s_cmp_ge_u32 s45, 16\n"
"s_cbranch_scc1 Ldone64_%=\n"
"global_atomic_add v170, v154, v2, s[4:5] sc0\n"
"s_waitcnt vmcnt(3)\n"
"v_readfirstlane_b32 s45, v171\n"
"s_nop 0\n"
"s_cmp_ge_u32 s45, 16\n"
"s_cbranch_scc1 Ldone64_%=\n"
"global_atomic_add v171, v154, v2, s[4:5] sc0\n"
"Lpoll63_%=:\n"
"s_waitcnt vmcnt(3)\n"
"v_readfirstlane_b32 s45, v168\n"
"s_nop 0\n"
"s_cmp_ge_u32 s45, 16\n"
"s_cbranch_scc1 Ldone64_%=\n"
"global_atomic_add v168, v154, v2, s[4:5] sc0\n"
"s_waitcnt vmcnt(3)\n"
"v_readfirstlane_b32 s45, v169\n"
"s_nop 0\n"
"s_cmp_ge_u32 s45, 16\n"
"s_cbranch_scc1 Ldone64_%=\n"
"global_atomic_add v169, v154, v2, s[4:5] sc0\n"
"s_waitcnt vmcnt(3)\n"
"v_readfirstlane_b32 s45, v170\n"
"s_nop 0\n"
"s_cmp_ge_u32 s45, 16\n"
"s_cbranch_scc1 Ldone64_%=\n"
"global_atomic_add v170, v154, v2, s[4:5] sc0\n"
"s_waitcnt vmcnt(3)\n"
"v_readfirstlane_b32 s45, v171\n"
"s_nop 0\n"
"s_cmp_ge_u32 s45, 16\n"
"s_cbranch_scc1 Ldone64_%=\n"
"global_atomic_add v171, v154, v2, s[4:5] sc0\n"
"s_add_u32 s25, s25, 4\n"
"s_cmp_lt_u32 s25, 1048576\n"
"s_cbranch_scc1 Lpoll63_%=\n"
"Ldone64_%=:\n"
"s_mov_b64 exec, -1\n"
"s_add_u32 m0, s78, 143168\n"
"s_nop 0\n"
"global_load_lds_dwordx4 v1, s[68:69] offset:0 sc1\n"
"global_load_lds_dwordx4 v1, s[68:69] offset:1024 sc1\n"
"global_load_lds_dwordx4 v1, s[68:69] offset:2048 sc1\n"
"global_load_lds_dwordx4 v1, s[68:69] offset:3072 sc1\n"
"s_add_u32 m0, s78, 147264\n"
"s_add_u32 s68, s68, 4096\n"
"s_addc_u32 s69, s69, 0\n"
"global_load_lds_dwordx4 v1, s[68:69] offset:0 sc1\n"
"s_barrier\n"
"s_mov_b64 exec, 1\n"
"v_mov_b32 v162, s40\n"
"v_mov_b32 v163, s41\n"
"buffer_store_dwordx2 v[162:163], v2, s[12:15], s53 offen sc1\n"
"s_mov_b64 exec, -1\n"
"v_pk_mul_f32 v[116:117], v[116:117], v[142:143] op_sel_hi:[1,0]\n"
"v_pk_mul_f32 v[118:119], v[118:119], v[142:143] op_sel_hi:[1,0]\n"
"v_pk_mul_f32 v[120:121], v[120:121], v[142:143] op_sel_hi:[1,0]\n"
"v_pk_mul_f32 v[122:123], v[122:123], v[142:143] op_sel_hi:[1,0]\n"
"v_pk_mul_f32 v[124:125], v[124:125], v[142:143] op_sel_hi:[1,0]\n"
"v_pk_mul_f32 v[126:127], v[126:127], v[142:143] op_sel_hi:[1,0]\n"
"v_pk_mul_f32 v[128:129], v[128:129], v[142:143] op_sel_hi:[1,0]\n"
"v_pk_mul_f32 v[130:131], v[130:131], v[142:143] op_sel_hi:[1,0]\n"
"v_pk_mul_f32 v[132:133], v[132:133], v[142:143] op_sel_hi:[1,0]\n"
"v_pk_mul_f32 v[134:135], v[134:135], v[142:143] op_sel_hi:[1,0]\n"
"v_cvt_pk_bf16_f32 v32, v116, v117\n"
"v_cvt_pk_bf16_f32 v33, v118, v119\n"
"v_cvt_pk_bf16_f32 v34, v120, v121\n"
"v_cvt_pk_bf16_f32 v35, v122, v123\n"
"v_cvt_pk_bf16_f32 v36, v124, v125\n"
"v_cvt_pk_bf16_f32 v37, v126, v127\n"
"v_cvt_pk_bf16_f32 v38, v128, v129\n"
"v_cvt_pk_bf16_f32 v39, v130, v131\n"
"v_cvt_pk_bf16_f32 v40, v132, v133\n"
"v_cvt_pk_bf16_f32 v41, v134, v135\n"
"v_add_u32 v60, 122496, v1\n"
"v_add_u32 v61, 124544, v1\n"
"v_and_b32 v156, 0x1f0, v1\n"
"v_min_u32 v157, 32, v1\n"
"s_add_u32 s46, s78, 127616\n"
"v_add_u32 v164, s46, v157\n"
"v_mov_b32 v63, 127648\n"
"v_mov_b32 v64, 127648\n"
"v_mov_b32 v65, 127648\n"
"v_mov_b32 v66, 127648\n"
"v_mov_b64 v[160:161], 0\n"
"v_mov_b64 v[162:163], 0\n"
"s_mov_b32 exec_lo, 0\n"
"v_mov_b32 v61, 127632\n"
"v_add_u32 v156, 320, v156\n"
"s_mov_b32 exec_lo, 0x11000000\n"
"s_mov_b32 exec_hi, 0x11000000\n"
"v_mov_b32 v63, 127616\n"
"v_mov_b32 v64, 127616\n"
"v_mov_b32 v65, 127616\n"
"v_mov_b32 v66, 127616\n"
"s_mov_b32 exec_lo, 0xfffff\n"
"s_mov_b32 exec_hi, 0xfffff\n"
"v_add_u32 v63, 125056, v156\n"
"v_add_u32 v64, 125696, v156\n"
"v_add_u32 v65, 126336, v156\n"
"v_add_u32 v66, 126976, v156\n"
"s_mov_b64 exec, 3\n"
"v_mov_b32 v160, 0x3f803f80\n"
"v_mov_b32 v161, 0x3f803f80\n"
"v_mov_b32 v162, 0x3f803f80\n"
"v_mov_b32 v163, 0x3f803f80\n"
"s_mov_b64 exec, 2\n"
"v_mov_b32 v161, 0x3f80\n"
"v_mov_b32 v163, 0x3f80\n"
"s_mov_b64 exec, -1\n"
"ds_write_b128 v164, v[160:163] offset:20672\n"
"v_mov_b32 v62, 0xdddddddd\n"
"v_mov_b64 v[116:117], 0\n"
"v_mov_b64 v[118:119], 0\n"
"v_mov_b64 v[120:121], 0\n"
"v_mov_b64 v[122:123], 0\n"
"v_mov_b64 v[124:125], 0\n"
"v_mov_b64 v[126:127], 0\n"
"v_mov_b64 v[128:129], 0\n"
"v_mov_b64 v[130:131], 0\n"
"v_mov_b64 v[132:133], 0\n"
"v_mov_b64 v[134:135], 0\n"
"v_mov_b64 v[136:137], 0\n"
"v_mov_b64 v[138:139], 0\n"
"v_mov_b64 v[140:141], 0\n"
"v_mov_b64 v[142:143], 0\n"
"v_mov_b64 v[144:145], 0\n"
"v_mov_b64 v[146:147], 0\n"
"v_mov_b64 v[42:43], 0\n"
"v_permlane32_swap_b32 v32, v36\n"
"v_permlane32_swap_b32 v33, v37\n"
"v_permlane32_swap_b32 v34, v38\n"
"v_permlane32_swap_b32 v35, v39\n"
"v_permlane32_swap_b32 v40, v42\n"
"v_permlane32_swap_b32 v41, v43\n"
"s_waitcnt vmcnt(1)\n"
"s_branch Lhead1_%=\n"
"Llast2_%=:\n"
"v_cvt_pk_bf16_f32 v32, v12, v13\n"
"v_cvt_pk_bf16_f32 v33, v14, v15\n"
"v_cvt_pk_bf16_f32 v34, v16, v17\n"
"v_cvt_pk_bf16_f32 v35, v18, v19\n"
"v_cvt_pk_bf16_f32 v36, v20, v21\n"
"v_cvt_pk_bf16_f32 v37, v22, v23\n"
"v_cvt_pk_bf16_f32 v38, v24, v25\n"
"v_cvt_pk_bf16_f32 v39, v26, v27\n"
"v_cvt_pk_bf16_f32 v40, v28, v29\n"
"v_cvt_pk_bf16_f32 v41, v30, v31\n"
"ds_write_b128 v136, v[32:35]\n"
"ds_write_b128 v136, v[36:39] offset:64\n"
"ds_write_b64 v137, v[40:41]\n"
"s_waitcnt lgkmcnt(0)\n"
"ds_read_b128 v[44:47], v138 offset:0\n"
"ds_read_b128 v[48:51], v138 offset:1024\n"
"ds_read_b128 v[52:55], v138 offset:2048\n"
"s_waitcnt lgkmcnt(2)\n"
"buffer_store_dwordx4 v[44:47], v133, s[16:19], 0 offen\n"
"s_waitcnt lgkmcnt(1)\n"
"buffer_store_dwordx4 v[48:51], v134, s[16:19], 0 offen\n"
"s_mov_b32 exec_hi, 0\n"
"s_waitcnt lgkmcnt(0)\n"
"buffer_store_dwordx4 v[52:55], v135, s[16:19], 0 offen\n"
"s_mov_b32 exec_hi, -1\n"
"s_branch Lend3_%=\n"
"Les52_%=:\n"
"s_mov_b64 exec, 1\n"
"global_atomic_add v128, v124, v2, s[4:5] sc0\n"
"s_sleep 3\n"
"global_atomic_add v129, v124, v2, s[4:5] sc0\n"
"s_sleep 3\n"
"global_atomic_add v130, v124, v2, s[4:5] sc0\n"
"s_sleep 3\n"
"global_atomic_add v131, v124, v2, s[4:5] sc0\n"
"s_waitcnt vmcnt(3)\n"
"v_readfirstlane_b32 s45, v128\n"
"s_nop 0\n"
"s_and_b32 s45, s45, 255\n"
"s_cmp_ge_u32 s45, 8\n"
"s_cbranch_scc1 Ldone66_%=\n"
"global_atomic_add v128, v124, v2, s[4:5] sc0\n"
"s_waitcnt vmcnt(3)\n"
"v_readfirstlane_b32 s45, v129\n"
"s_nop 0\n"
"s_and_b32 s45, s45, 255\n"
"s_cmp_ge_u32 s45, 8\n"
"s_cbranch_scc1 Ldone66_%=\n"
"global_atomic_add v129, v124, v2, s[4:5] sc0\n"
"s_waitcnt vmcnt(3)\n"
"v_readfirstlane_b32 s45, v130\n"
"s_nop 0\n"
"s_and_b32 s45, s45, 255\n"
"s_cmp_ge_u32 s45, 8\n"
"s_cbranch_scc1 Ldone66_%=\n"
"global_atomic_add v130, v124, v2, s[4:5] sc0\n"
"s_waitcnt vmcnt(3)\n"
"v_readfirstlane_b32 s45, v131\n"
"s_nop 0\n"
"s_and_b32 s45, s45, 255\n"
"s_cmp_ge_u32 s45, 8\n"
"s_cbranch_scc1 Ldone66_%=\n"
"global_atomic_add v131, v124, v2, s[4:5] sc0\n"
"Lpoll65_%=:\n"
"s_waitcnt vmcnt(3)\n"
"v_readfirstlane_b32 s45, v128\n"
"s_nop 0\n"
"s_and_b32 s45, s45, 255\n"
"s_cmp_ge_u32 s45, 8\n"
"s_cbranch_scc1 Ldone66_%=\n"
"global_atomic_add v128, v124, v2, s[4:5] sc0\n"
"s_waitcnt vmcnt(3)\n"
"v_readfirstlane_b32 s45, v129\n"
"s_nop 0\n"
"s_and_b32 s45, s45, 255\n"
"s_cmp_ge_u32 s45, 8\n"
"s_cbranch_scc1 Ldone66_%=\n"
"global_atomic_add v129, v124, v2, s[4:5] sc0\n"
"s_waitcnt vmcnt(3)\n"
"v_readfirstlane_b32 s45, v130\n"
"s_nop 0\n"
"s_and_b32 s45, s45, 255\n"
"s_cmp_ge_u32 s45, 8\n"
"s_cbranch_scc1 Ldone66_%=\n"
"global_atomic_add v130, v124, v2, s[4:5] sc0\n"
"s_waitcnt vmcnt(3)\n"
"v_readfirstlane_b32 s45, v131\n"
"s_nop 0\n"
"s_and_b32 s45, s45, 255\n"
"s_cmp_ge_u32 s45, 8\n"
"s_cbranch_scc1 Ldone66_%=\n"
"global_atomic_add v131, v124, v2, s[4:5] sc0\n"
"s_add_u32 s25, s25, 4\n"
"s_cmp_lt_u32 s25, 1048576\n"
"s_cbranch_scc1 Lpoll65_%=\n"
"Ldone66_%=:\n"
"s_mov_b64 exec, -1\n"
"s_branch Leb53_%=\n"
"Lls54_%=:\n"
"s_mov_b64 exec, 1\n"
"global_atomic_add v128, v124, v2, s[4:5] sc0\n"
"s_sleep 3\n"
"global_atomic_add v129, v124, v2, s[4:5] sc0\n"
"s_sleep 3\n"
"global_atomic_add v130, v124, v2, s[4:5] sc0\n"
"s_sleep 3\n"
"global_atomic_add v131, v124, v2, s[4:5] sc0\n"
"s_waitcnt vmcnt(3)\n"
"v_readfirstlane_b32 s45, v128\n"
"s_nop 0\n"
"s_cmp_ge_u32 s45, 2048\n"
"s_cbranch_scc1 Ldone68_%=\n"
"global_atomic_add v128, v124, v2, s[4:5] sc0\n"
"s_waitcnt vmcnt(3)\n"
"v_readfirstlane_b32 s45, v129\n"
"s_nop 0\n"
"s_cmp_ge_u32 s45, 2048\n"
"s_cbranch_scc1 Ldone68_%=\n"
"global_atomic_add v129, v124, v2, s[4:5] sc0\n"
"s_waitcnt vmcnt(3)\n"
"v_readfirstlane_b32 s45, v130\n"
"s_nop 0\n"
"s_cmp_ge_u32 s45, 2048\n"
"s_cbranch_scc1 Ldone68_%=\n"
"global_atomic_add v130, v124, v2, s[4:5] sc0\n"
"s_waitcnt vmcnt(3)\n"
"v_readfirstlane_b32 s45, v131\n"
"s_nop 0\n"
"s_cmp_ge_u32 s45, 2048\n"
"s_cbranch_scc1 Ldone68_%=\n"
"global_atomic_add v131, v124, v2, s[4:5] sc0\n"
"Lpoll67_%=:\n"
"s_waitcnt vmcnt(3)\n"
"v_readfirstlane_b32 s45, v128\n"
"s_nop 0\n"
"s_cmp_ge_u32 s45, 2048\n"
"s_cbranch_scc1 Ldone68_%=\n"
"global_atomic_add v128, v124, v2, s[4:5] sc0\n"
"s_waitcnt vmcnt(3)\n"
"v_readfirstlane_b32 s45, v129\n"
"s_nop 0\n"
"s_cmp_ge_u32 s45, 2048\n"
"s_cbranch_scc1 Ldone68_%=\n"
"global_atomic_add v129, v124, v2, s[4:5] sc0\n"
"s_waitcnt vmcnt(3)\n"
"v_readfirstlane_b32 s45, v130\n"
"s_nop 0\n"
"s_cmp_ge_u32 s45, 2048\n"
"s_cbranch_scc1 Ldone68_%=\n"
"global_atomic_add v130, v124, v2, s[4:5] sc0\n"
"s_waitcnt vmcnt(3)\n"
"v_readfirstlane_b32 s45, v131\n"
"s_nop 0\n"
"s_cmp_ge_u32 s45, 2048\n"
"s_cbranch_scc1 Ldone68_%=\n"
"global_atomic_add v131, v124, v2, s[4:5] sc0\n"
"s_add_u32 s25, s25, 4\n"
"s_cmp_lt_u32 s25, 1048576\n"
"s_cbranch_scc1 Lpoll67_%=\n"
"Ldone68_%=:\n"
"s_mov_b64 exec, -1\n"
"s_branch Llb55_%=\n"
"Lkp12_%=:\n"
"s_waitcnt vmcnt(0) lgkmcnt(0)\n"
"s_nop 15\n"
"s_mov_b64 exec, 0xff\n"
"buffer_load_dwordx2 v[166:167], v175, s[12:15], s62 offen sc1\n"
"s_add_u32 s25, s25, 1\n"
"s_waitcnt vmcnt(0)\n"
"v_cmp_ne_u64 s[8:9], s[40:41], v[166:167]\n"
"s_mov_b64 exec, -1\n"
"s_cmp_ge_u32 s25, 1048576\n"
"s_cbranch_scc1 Lkd13_%=\n"
"s_cmp_lg_u32 s8, 0\n"
"s_cbranch_scc1 Lkp12_%=\n"
"s_branch Lkd13_%=\n"
"Lcs18_%=:\n"
"s_waitcnt vmcnt(0) lgkmcnt(0)\n"
"s_nop 15\n"
"buffer_load_dwordx2 v[172:173], v175, s[12:15], s62 offen sc1\n"
"s_add_u32 s25, s25, 1\n"
"s_waitcnt vmcnt(0)\n"
"v_cmp_ne_u64 s[8:9], s[40:41], v[172:173]\n"
"s_cmp_ge_u32 s25, 1048576\n"
"s_cbranch_scc1 Lcb19_%=\n"
"s_bfe_u32 s45, s8, 0x80008\n"
"s_cbranch_scc1 Lcs18_%=\n"
"s_branch Lcb19_%=\n"
"Lcs20_%=:\n"
"s_waitcnt vmcnt(0) lgkmcnt(0)\n"
"s_nop 15\n"
"buffer_load_dwordx2 v[172:173], v175, s[12:15], s62 offen sc1\n"
"s_add_u32 s25, s25, 1\n"
"s_waitcnt vmcnt(0)\n"
"v_cmp_ne_u64 s[8:9], s[40:41], v[172:173]\n"
"s_cmp_ge_u32 s25, 1048576\n"
"s_cbranch_scc1 Lcb21_%=\n"
"s_bfe_u32 s45, s8, 0x80010\n"
"s_cbranch_scc1 Lcs20_%=\n"
"s_branch Lcb21_%=\n"
"Lcs22_%=:\n"
"s_waitcnt vmcnt(0) lgkmcnt(0)\n"
"s_nop 15\n"
"buffer_load_dwordx2 v[172:173], v175, s[12:15], s62 offen sc1\n"
"s_add_u32 s25, s25, 1\n"
"s_waitcnt vmcnt(0)\n"
"v_cmp_ne_u64 s[8:9], s[40:41], v[172:173]\n"
"s_cmp_ge_u32 s25, 1048576\n"
"s_cbranch_scc1 Lcb23_%=\n"
"s_bfe_u32 s45, s8, 0x80018\n"
"s_cbranch_scc1 Lcs22_%=\n"
"s_branch Lcb23_%=\n"
"Lcs24_%=:\n"
"s_waitcnt vmcnt(0) lgkmcnt(0)\n"
"s_nop 15\n"
"buffer_load_dwordx2 v[172:173], v175, s[12:15], s62 offen sc1\n"
"s_add_u32 s25, s25, 1\n"
"s_waitcnt vmcnt(0)\n"
"v_cmp_ne_u64 s[8:9], s[40:41], v[172:173]\n"
"s_cmp_ge_u32 s25, 1048576\n"
"s_cbranch_scc1 Lcb25_%=\n"
"s_bfe_u32 s45, s9, 0x80000\n"
"s_cbranch_scc1 Lcs24_%=\n"
"s_branch Lcb25_%=\n"
"Lcs26_%=:\n"
"s_waitcnt vmcnt(0) lgkmcnt(0)\n"
"s_nop 15\n"
"buffer_load_dwordx2 v[172:173], v175, s[12:15], s62 offen sc1\n"
"s_add_u32 s25, s25, 1\n"
"s_waitcnt vmcnt(0)\n"
"v_cmp_ne_u64 s[8:9], s[40:41], v[172:173]\n"
"s_cmp_ge_u32 s25, 1048576\n"
"s_cbranch_scc1 Lcb27_%=\n"
"s_bfe_u32 s45, s9, 0x80008\n"
"s_cbranch_scc1 Lcs26_%=\n"
"s_branch Lcb27_%=\n"
"Lcs28_%=:\n"
"s_waitcnt vmcnt(0) lgkmcnt(0)\n"
"s_nop 15\n"
"buffer_load_dwordx2 v[172:173], v175, s[12:15], s62 offen sc1\n"
"s_add_u32 s25, s25, 1\n"
"s_waitcnt vmcnt(0)\n"
"v_cmp_ne_u64 s[8:9], s[40:41], v[172:173]\n"
"s_cmp_ge_u32 s25, 1048576\n"
"s_cbranch_scc1 Lcb29_%=\n"
"s_bfe_u32 s45, s9, 0x80010\n"
"s_cbranch_scc1 Lcs28_%=\n"
"s_branch Lcb29_%=\n"
"Lcs30_%=:\n"
"s_waitcnt vmcnt(0) lgkmcnt(0)\n"
"s_nop 15\n"
"buffer_load_dwordx2 v[172:173], v175, s[12:15], s62 offen sc1\n"
"s_add_u32 s25, s25, 1\n"
"s_waitcnt vmcnt(0)\n"
"v_cmp_ne_u64 s[8:9], s[40:41], v[172:173]\n"
"s_cmp_ge_u32 s25, 1048576\n"
"s_cbranch_scc1 Lcb31_%=\n"
"s_bfe_u32 s45, s9, 0x80018\n"
"s_cbranch_scc1 Lcs30_%=\n"
"s_branch Lcb31_%=\n"
"Lws37_%=:\n"
"s_waitcnt vmcnt(0) lgkmcnt(0)\n"
"s_nop 15\n"
"s_sleep 0\n"
"buffer_load_dwordx2 v[166:167], v67, s[12:15], s53 offen sc1\n"
"s_add_u32 s25, s25, 1\n"
"s_waitcnt vmcnt(0)\n"
"v_cmp_ne_u64 vcc, s[40:41], v[166:167]\n"
"s_cmp_ge_u32 s25, 1048576\n"
"s_cbranch_scc1 Lwb38_%=\n"
"s_nop 2\n"
"s_cbranch_vccnz Lws37_%=\n"
"s_branch Lwb38_%=\n"
"Lfb39_%=:\n"
"s_waitcnt vmcnt(0) lgkmcnt(0)\n"
"s_nop 15\n"
"s_lshl_b32 s46, s26, 11\n"
"s_add_u32 s46, s46, s62\n"
"v_and_b32 v156, 15, v4\n"
"v_lshlrev_b32 v156, 7, v156\n"
"Lfw69_%=:\n"
"buffer_load_dwordx2 v[158:159], v156, s[12:15], s46 offen sc1\n"
"s_add_u32 s25, s25, 1\n"
"s_waitcnt vmcnt(0)\n"
"v_cmp_ne_u64 vcc, s[40:41], v[158:159]\n"
"s_cmp_ge_u32 s25, 1048576\n"
"s_cbranch_scc1 Lfx70_%=\n"
"s_nop 2\n"
"s_cbranch_vccnz Lfw69_%=\n"
"Lfx70_%=:\n"
"v_and_b32 v156, 31, v4\n"
"v_lshlrev_b32 v161, 4, v156\n"
"Lpad13_%=:\n"
"s_cbranch_execz Lpadback3_%=\n"
"v_lshrrev_b32 v157, 5, v4\n"
"v_mul_u32_u24 v157, 320, v157\n"
"v_min_u32 v158, 19, v156\n"
"v_lshl_add_u32 v162, v158, 4, v157\n"
"v_cmp_gt_u32 s[50:51], 20, v156\n"
"v_mov_b32 v164, 0x3f803f80\n"
"v_mov_b32 v165, 0x3f80\n"
"v_mov_b32 v166, 0x3f803f80\n"
"s_mov_b32 s49, 0\n"
"Lfs71_%=:\n"
"s_waitcnt vmcnt(0) lgkmcnt(0)\n"
"s_nop 15\n"
"v_cndmask_b32 v40, v40, v2, s[36:37]\n"
"v_cndmask_b32 v41, v41, v2, s[36:37]\n"
"v_cndmask_b32 v42, v42, v2, s[36:37]\n"
"v_cndmask_b32 v43, v43, v2, s[36:37]\n"
"v_mov_b32 v159, 0xff800000\n"
"v_mov_b32 v160, 0xff800000\n"
"v_mov_b32 v116, 0\n"
"v_mov_b32 v117, 0\n"
"v_mov_b32 v118, 0\n"
"v_mov_b32 v119, 0\n"
"v_mov_b32 v120, 0\n"
"v_mov_b32 v121, 0\n"
"v_mov_b32 v122, 0\n"
"v_mov_b32 v123, 0\n"
"v_mov_b32 v124, 0\n"
"v_mov_b32 v125, 0\n"
"v_mov_b32 v126, 0\n"
"v_mov_b32 v127, 0\n"
"v_mov_b32 v128, 0\n"
"v_mov_b32 v129, 0\n"
"v_mov_b32 v130, 0\n"
"v_mov_b32 v131, 0\n"
"v_mov_b32 v132, 0\n"
"v_mov_b32 v133, 0\n"
"v_mov_b32 v134, 0\n"
"v_mov_b32 v135, 0\n"
"v_mov_b32 v136, 0\n"
"v_mov_b32 v137, 0\n"
"v_mov_b32 v138, 0\n"
"v_mov_b32 v139, 0\n"
"v_mov_b32 v140, 0\n"
"v_mov_b32 v141, 0\n"
"v_mov_b32 v142, 0\n"
"v_mov_b32 v143, 0\n"
"v_mov_b32 v144, 0\n"
"v_mov_b32 v145, 0\n"
"v_mov_b32 v146, 0\n"
"v_mov_b32 v147, 0\n"
"s_mul_i32 s52, s26, 20480\n"
"s_mov_b32 s47, 32\n"
"Lfbl73_%=:\n"
"s_waitcnt vmcnt(0) lgkmcnt(0)\n"
"s_nop 15\n"
"s_add_u32 s46, s52, s28\n"
"buffer_load_dwordx4 v[44:47], v1, s[12:15], s46 offen offset:0 sc1\n"
"buffer_load_dwordx2 v[48:49], v161, s[12:15], s46 offen offset:2048 sc1\n"
"s_waitcnt vmcnt(0)\n"
"v_cndmask_b32 v48, v48, v164, s[36:37]\n"
"v_cndmask_b32 v49, v49, v165, s[36:37]\n"
"s_nop 1\n"
"v_mfma_f32_32x32x8_bf16 v[68:83], v[48:49], v[40:41], 0\n"
"v_mfma_f32_32x32x16_bf16 v[68:83], v[44:47], v[32:35], v[68:83]\n"
"s_nop 11\n"
"v_max3_f32 v159, v159, v68, v69\n"
"v_max3_f32 v159, v159, v70, v71\n"
"v_max3_f32 v159, v159, v72, v73\n"
"v_max3_f32 v159, v159, v74, v75\n"
"v_max3_f32 v159, v159, v76, v77\n"
"v_max3_f32 v159, v159, v78, v79\n"
"v_max3_f32 v159, v159, v80, v81\n"
"v_max3_f32 v159, v159, v82, v83\n"
"v_mfma_f32_32x32x8_bf16 v[84:99], v[48:49], v[42:43], 0\n"
"v_mfma_f32_32x32x16_bf16 v[84:99], v[44:47], v[36:39], v[84:99]\n"
"s_nop 11\n"
"v_max3_f32 v160, v160, v84, v85\n"
"v_max3_f32 v160, v160, v86, v87\n"
"v_max3_f32 v160, v160, v88, v89\n"
"v_max3_f32 v160, v160, v90, v91\n"
"v_max3_f32 v160, v160, v92, v93\n"
"v_max3_f32 v160, v160, v94, v95\n"
"v_max3_f32 v160, v160, v96, v97\n"
"v_max3_f32 v160, v160, v98, v99\n"
"s_cmp_eq_u32 s49, 0\n"
"s_cbranch_scc1 Lfbq74_%=\n"
"buffer_load_dwordx4 v[44:47], v1, s[12:15], s46 offen offset:1024 sc1\n"
"buffer_load_dwordx2 v[48:49], v161, s[12:15], s46 offen offset:2056 sc1\n"
"s_waitcnt vmcnt(0)\n"
"v_cndmask_b32 v48, v48, v164, s[36:37]\n"
"v_cndmask_b32 v49, v49, v165, s[36:37]\n"
"s_nop 1\n"
"v_mfma_f32_32x32x8_bf16 v[68:83], v[48:49], v[40:41], 0\n"
"v_mfma_f32_32x32x16_bf16 v[68:83], v[44:47], v[32:35], v[68:83]\n"
"s_nop 11\n"
"v_max3_f32 v159, v159, v68, v69\n"
"v_max3_f32 v159, v159, v70, v71\n"
"v_max3_f32 v159, v159, v72, v73\n"
"v_max3_f32 v159, v159, v74, v75\n"
"v_max3_f32 v159, v159, v76, v77\n"
"v_max3_f32 v159, v159, v78, v79\n"
"v_max3_f32 v159, v159, v80, v81\n"
"v_max3_f32 v159, v159, v82, v83\n"
"v_mfma_f32_32x32x8_bf16 v[84:99], v[48:49], v[42:43], 0\n"
"v_mfma_f32_32x32x16_bf16 v[84:99], v[44:47], v[36:39], v[84:99]\n"
"s_nop 11\n"
"v_max3_f32 v160, v160, v84, v85\n"
"v_max3_f32 v160, v160, v86, v87\n"
"v_max3_f32 v160, v160, v88, v89\n"
"v_max3_f32 v160, v160, v90, v91\n"
"v_max3_f32 v160, v160, v92, v93\n"
"v_max3_f32 v160, v160, v94, v95\n"
"v_max3_f32 v160, v160, v96, v97\n"
"v_max3_f32 v160, v160, v98, v99\n"
"s_add_u32 s52, s52, 5120\n"
"s_cmp_ge_u32 s52, 163840\n"
"s_cselect_b32 s52, 0, s52\n"
"s_sub_u32 s47, s47, 1\n"
"s_cmp_lg_u32 s47, 0\n"
"s_cbranch_scc1 Lfbl73_%=\n"
"Lfbq74_%=:\n"
"s_waitcnt vmcnt(0) lgkmcnt(0)\n"
"s_nop 15\n"
"v_mov_b32 v156, v159\n"
"s_nop 1\n"
"v_permlane32_swap_b32 v159, v156\n"
"v_max_f32 v159, v159, v156\n"
"v_and_b32 v156, 0xffff0000, v159\n"
"v_sub_f32 v159, v159, v156\n"
"v_and_b32 v157, 0xffff0000, v159\n"
"v_sub_f32 v159, v159, v157\n"
"s_mov_b32 exec_lo, 0\n"
"v_cvt_pk_bf16_f32 v40, -v156, -v157\n"
"v_cvt_pk_bf16_f32 v41, -v159, v2\n"
"s_mov_b32 exec_lo, -1\n"
"v_mov_b32 v156, v160\n"
"s_nop 1\n"
"v_permlane32_swap_b32 v160, v156\n"
"v_max_f32 v160, v160, v156\n"
"v_and_b32 v156, 0xffff0000, v160\n"
"v_sub_f32 v160, v160, v156\n"
"v_and_b32 v157, 0xffff0000, v160\n"
"v_sub_f32 v160, v160, v157\n"
"s_mov_b32 exec_lo, 0\n"
"v_cvt_pk_bf16_f32 v42, -v156, -v157\n"
"v_cvt_pk_bf16_f32 v43, -v160, v2\n"
"s_mov_b32 exec_lo, -1\n"
"s_mul_i32 s52, s26, 20480\n"
"s_mov_b32 s47, 32\n"
"Lfbl75_%=:\n"
"s_waitcnt vmcnt(0) lgkmcnt(0)\n"
"s_nop 15\n"
"s_add_u32 s46, s52, s28\n"
"buffer_load_dwordx4 v[44:47], v1, s[12:15], s46 offen offset:0 sc1\n"
"buffer_load_dwordx2 v[48:49], v161, s[12:15], s46 offen offset:2048 sc1\n"
"s_waitcnt vmcnt(0)\n"
"v_cndmask_b32 v48, v48, v164, s[36:37]\n"
"v_cndmask_b32 v49, v49, v165, s[36:37]\n"
"s_add_u32 s48, s46, 2560\n"
"buffer_load_dwordx4 v[52:55], v162, s[12:15], s48 offen offset:0 sc1\n"
"buffer_load_dwordx4 v[56:59], v162, s[12:15], s48 offen offset:640 sc1\n"
"s_waitcnt vmcnt(1)\n"
"v_cndmask_b32 v52, v166, v52, s[50:51]\n"
"v_cndmask_b32 v53, v166, v53, s[50:51]\n"
"v_cndmask_b32 v54, v166, v54, s[50:51]\n"
"v_cndmask_b32 v55, v166, v55, s[50:51]\n"
"s_waitcnt vmcnt(0)\n"
"v_cndmask_b32 v56, v166, v56, s[50:51]\n"
"v_cndmask_b32 v57, v166, v57, s[50:51]\n"
"v_cndmask_b32 v58, v166, v58, s[50:51]\n"
"v_cndmask_b32 v59, v166, v59, s[50:51]\n"
"v_mfma_f32_32x32x8_bf16 v[68:83], v[48:49], v[40:41], 0\n"
"v_mfma_f32_32x32x16_bf16 v[68:83], v[44:47], v[32:35], v[68:83]\n"
"s_nop 11\n"
"v_exp_f32 v68, v68\n"
"v_exp_f32 v69, v69\n"
"v_exp_f32 v70, v70\n"
"v_exp_f32 v71, v71\n"
"v_exp_f32 v72, v72\n"
"v_exp_f32 v73, v73\n"
"v_exp_f32 v74, v74\n"
"v_exp_f32 v75, v75\n"
"v_exp_f32 v76, v76\n"
"v_exp_f32 v77, v77\n"
"v_exp_f32 v78, v78\n"
"v_exp_f32 v79, v79\n"
"v_exp_f32 v80, v80\n"
"v_exp_f32 v81, v81\n"
"v_exp_f32 v82, v82\n"
"v_exp_f32 v83, v83\n"
"v_smfmac_f32_32x32x32_bf16 v[116:131], v[52:55], v[68:75], v62\n"
"s_nop 0\n"
"v_smfmac_f32_32x32x32_bf16 v[116:131], v[56:59], v[76:83], v62\n"
"v_mfma_f32_32x32x8_bf16 v[84:99], v[48:49], v[42:43], 0\n"
"v_mfma_f32_32x32x16_bf16 v[84:99], v[44:47], v[36:39], v[84:99]\n"
"s_nop 11\n"
"v_exp_f32 v84, v84\n"
"v_exp_f32 v85, v85\n"
"v_exp_f32 v86, v86\n"
"v_exp_f32 v87, v87\n"
"v_exp_f32 v88, v88\n"
"v_exp_f32 v89, v89\n"
"v_exp_f32 v90, v90\n"
"v_exp_f32 v91, v91\n"
"v_exp_f32 v92, v92\n"
"v_exp_f32 v93, v93\n"
"v_exp_f32 v94, v94\n"
"v_exp_f32 v95, v95\n"
"v_exp_f32 v96, v96\n"
"v_exp_f32 v97, v97\n"
"v_exp_f32 v98, v98\n"
"v_exp_f32 v99, v99\n"
"v_smfmac_f32_32x32x32_bf16 v[132:147], v[52:55], v[84:91], v62\n"
"s_nop 0\n"
"v_smfmac_f32_32x32x32_bf16 v[132:147], v[56:59], v[92:99], v62\n"
"buffer_load_dwordx4 v[44:47], v1, s[12:15], s46 offen offset:1024 sc1\n"
"buffer_load_dwordx2 v[48:49], v161, s[12:15], s46 offen offset:2056 sc1\n"
"s_waitcnt vmcnt(0)\n"
"v_cndmask_b32 v48, v48, v164, s[36:37]\n"
"v_cndmask_b32 v49, v49, v165, s[36:37]\n"
"s_add_u32 s48, s46, 3840\n"
"buffer_load_dwordx4 v[52:55], v162, s[12:15], s48 offen offset:0 sc1\n"
"buffer_load_dwordx4 v[56:59], v162, s[12:15], s48 offen offset:640 sc1\n"
"s_waitcnt vmcnt(1)\n"
"v_cndmask_b32 v52, v166, v52, s[50:51]\n"
"v_cndmask_b32 v53, v166, v53, s[50:51]\n"
"v_cndmask_b32 v54, v166, v54, s[50:51]\n"
"v_cndmask_b32 v55, v166, v55, s[50:51]\n"
"s_waitcnt vmcnt(0)\n"
"v_cndmask_b32 v56, v166, v56, s[50:51]\n"
"v_cndmask_b32 v57, v166, v57, s[50:51]\n"
"v_cndmask_b32 v58, v166, v58, s[50:51]\n"
"v_cndmask_b32 v59, v166, v59, s[50:51]\n"
"v_mfma_f32_32x32x8_bf16 v[68:83], v[48:49], v[40:41], 0\n"
"v_mfma_f32_32x32x16_bf16 v[68:83], v[44:47], v[32:35], v[68:83]\n"
"s_nop 11\n"
"v_exp_f32 v68, v68\n"
"v_exp_f32 v69, v69\n"
"v_exp_f32 v70, v70\n"
"v_exp_f32 v71, v71\n"
"v_exp_f32 v72, v72\n"
"v_exp_f32 v73, v73\n"
"v_exp_f32 v74, v74\n"
"v_exp_f32 v75, v75\n"
"v_exp_f32 v76, v76\n"
"v_exp_f32 v77, v77\n"
"v_exp_f32 v78, v78\n"
"v_exp_f32 v79, v79\n"
"v_exp_f32 v80, v80\n"
"v_exp_f32 v81, v81\n"
"v_exp_f32 v82, v82\n"
"v_exp_f32 v83, v83\n"
"v_smfmac_f32_32x32x32_bf16 v[116:131], v[52:55], v[68:75], v62\n"
"s_nop 0\n"
"v_smfmac_f32_32x32x32_bf16 v[116:131], v[56:59], v[76:83], v62\n"
"v_mfma_f32_32x32x8_bf16 v[84:99], v[48:49], v[42:43], 0\n"
"v_mfma_f32_32x32x16_bf16 v[84:99], v[44:47], v[36:39], v[84:99]\n"
"s_nop 11\n"
"v_exp_f32 v84, v84\n"
"v_exp_f32 v85, v85\n"
"v_exp_f32 v86, v86\n"
"v_exp_f32 v87, v87\n"
"v_exp_f32 v88, v88\n"
"v_exp_f32 v89, v89\n"
"v_exp_f32 v90, v90\n"
"v_exp_f32 v91, v91\n"
"v_exp_f32 v92, v92\n"
"v_exp_f32 v93, v93\n"
"v_exp_f32 v94, v94\n"
"v_exp_f32 v95, v95\n"
"v_exp_f32 v96, v96\n"
"v_exp_f32 v97, v97\n"
"v_exp_f32 v98, v98\n"
"v_exp_f32 v99, v99\n"
"v_smfmac_f32_32x32x32_bf16 v[132:147], v[52:55], v[84:91], v62\n"
"s_nop 0\n"
"v_smfmac_f32_32x32x32_bf16 v[132:147], v[56:59], v[92:99], v62\n"
"s_add_u32 s52, s52, 5120\n"
"s_cmp_ge_u32 s52, 163840\n"
"s_cselect_b32 s52, 0, s52\n"
"s_sub_u32 s47, s47, 1\n"
"s_cmp_lg_u32 s47, 0\n"
"s_cbranch_scc1 Lfbl75_%=\n"
"Lfbq76_%=:\n"
"s_waitcnt vmcnt(0) lgkmcnt(0)\n"
"s_nop 15\n"
"s_cmp_lg_u32 s49, 0\n"
"s_cbranch_scc1 Lfe72_%=\n"
"s_mov_b32 s49, 1\n"
"s_mov_b32 s46, 0x53800000\n"
"v_add_f32 v156, v116, v117\n"
"v_add_f32 v156, v156, v118\n"
"v_add_f32 v156, v156, v119\n"
"v_add_f32 v156, v156, v120\n"
"v_add_f32 v156, v156, v121\n"
"v_add_f32 v156, v156, v122\n"
"v_add_f32 v156, v156, v123\n"
"v_add_f32 v156, v156, v124\n"
"v_add_f32 v156, v156, v125\n"
"v_add_f32 v156, v156, v126\n"
"v_add_f32 v156, v156, v127\n"
"v_fma_f32 v156, v156, 0, v128\n"
"v_add_f32 v157, v132, v133\n"
"v_add_f32 v157, v157, v134\n"
"v_add_f32 v157, v157, v135\n"
"v_add_f32 v157, v157, v136\n"
"v_add_f32 v157, v157, v137\n"
"v_add_f32 v157, v157, v138\n"
"v_add_f32 v157, v157, v139\n"
"v_add_f32 v157, v157, v140\n"
"v_add_f32 v157, v157, v141\n"
"v_add_f32 v157, v157, v142\n"
"v_add_f32 v157, v157, v143\n"
"v_fma_f32 v157, v157, 0, v144\n"
"v_cmp_nge_f32 s[48:49], s46, v156\n"
"v_cmp_nge_f32 vcc, s46, v157\n"
"s_nop 0\n"
"s_or_b64 s[48:49], vcc, s[48:49]\n"
"s_cbranch_scc1 Lfs71_%=\n"
"Lfe72_%=:\n"
"s_waitcnt vmcnt(0) lgkmcnt(0)\n"
"s_nop 15\n"
"s_branch Lback40_%=\n"
"Lsb45_%=:\n"
"s_waitcnt vmcnt(0) lgkmcnt(0)\n"
"s_nop 15\n"
"s_mov_b64 exec, 1\n"
"global_atomic_add v108, v3, v2, s[4:5] sc0\n"
"s_sleep 3\n"
"global_atomic_add v109, v3, v2, s[4:5] sc0\n"
"s_sleep 3\n"
"global_atomic_add v110, v3, v2, s[4:5] sc0\n"
"s_sleep 3\n"
"global_atomic_add v111, v3, v2, s[4:5] sc0\n"
"s_waitcnt vmcnt(3)\n"
"v_readfirstlane_b32 s45, v108\n"
"s_nop 0\n"
"s_cmp_ge_u32 s45, 2048\n"
"s_cbranch_scc1 Ldone78_%=\n"
"global_atomic_add v108, v3, v2, s[4:5] sc0\n"
"s_waitcnt vmcnt(3)\n"
"v_readfirstlane_b32 s45, v109\n"
"s_nop 0\n"
"s_cmp_ge_u32 s45, 2048\n"
"s_cbranch_scc1 Ldone78_%=\n"
"global_atomic_add v109, v3, v2, s[4:5] sc0\n"
"s_waitcnt vmcnt(3)\n"
"v_readfirstlane_b32 s45, v110\n"
"s_nop 0\n"
"s_cmp_ge_u32 s45, 2048\n"
"s_cbranch_scc1 Ldone78_%=\n"
"global_atomic_add v110, v3, v2, s[4:5] sc0\n"
"s_waitcnt vmcnt(3)\n"
"v_readfirstlane_b32 s45, v111\n"
"s_nop 0\n"
"s_cmp_ge_u32 s45, 2048\n"
"s_cbranch_scc1 Ldone78_%=\n"
"global_atomic_add v111, v3, v2, s[4:5] sc0\n"
"Lpoll77_%=:\n"
"s_waitcnt vmcnt(3)\n"
"v_readfirstlane_b32 s45, v108\n"
"s_nop 0\n"
"s_cmp_ge_u32 s45, 2048\n"
"s_cbranch_scc1 Ldone78_%=\n"
"global_atomic_add v108, v3, v2, s[4:5] sc0\n"
"s_waitcnt vmcnt(3)\n"
"v_readfirstlane_b32 s45, v109\n"
"s_nop 0\n"
"s_cmp_ge_u32 s45, 2048\n"
"s_cbranch_scc1 Ldone78_%=\n"
"global_atomic_add v109, v3, v2, s[4:5] sc0\n"
"s_waitcnt vmcnt(3)\n"
"v_readfirstlane_b32 s45, v110\n"
"s_nop 0\n"
"s_cmp_ge_u32 s45, 2048\n"
"s_cbranch_scc1 Ldone78_%=\n"
"global_atomic_add v110, v3, v2, s[4:5] sc0\n"
"s_waitcnt vmcnt(3)\n"
"v_readfirstlane_b32 s45, v111\n"
"s_nop 0\n"
"s_cmp_ge_u32 s45, 2048\n"
"s_cbranch_scc1 Ldone78_%=\n"
"global_atomic_add v111, v3, v2, s[4:5] sc0\n"
"s_add_u32 s25, s25, 4\n"
"s_cmp_lt_u32 s25, 1048576\n"
"s_cbranch_scc1 Lpoll77_%=\n"
"Ldone78_%=:\n"
"s_mov_b64 exec, -1\n"
"s_waitcnt vmcnt(0) lgkmcnt(0)\n"
"s_nop 15\n"
"ds_read_b128 v[12:15], v173 offset:0\n"
"ds_read_b128 v[16:19], v173 offset:4096\n"
"ds_read_b128 v[20:23], v173 offset:8192\n"
"ds_read_b128 v[24:27], v173 offset:12288\n"
"ds_read_b128 v[28:31], v173 offset:16384\n"
"ds_read_b128 v[112:115], v1 offset:0\n"
"ds_read_b128 v[116:119], v1 offset:5120\n"
"ds_read_b128 v[120:123], v1 offset:1024\n"
"ds_read_b128 v[124:127], v1 offset:6144\n"
"ds_read_b128 v[128:131], v1 offset:2048\n"
"ds_read_b128 v[132:135], v1 offset:7168\n"
"ds_read_b128 v[136:139], v1 offset:3072\n"
"ds_read_b128 v[140:143], v1 offset:8192\n"
"s_add_u32 s64, s29, 4096\n"
"s_add_u32 s64, s4, s64\n"
"s_addc_u32 s65, s5, 0\n"
"s_sub_u32 s60, s60, 8192\n"
"s_subb_u32 s61, s61, 0\n"
"v_mov_b32 v0, 0\n"
"v_mov_b32 v172, 0\n"
"s_waitcnt vmcnt(0) lgkmcnt(0)\n"
"s_nop 15\n"
"s_branch Lrb44_%=\n"
"Lmc50_%=:\n"
"s_waitcnt vmcnt(0) lgkmcnt(0)\n"
"s_nop 15\n"
"ds_write_b32 v8, v8 offset:21504\n"
"s_waitcnt vmcnt(0) lgkmcnt(0)\n"
"s_nop 15\n"
"s_branch Lmb51_%=\n"
"Lsc49_%=:\n"
"s_waitcnt vmcnt(0) lgkmcnt(0)\n"
"s_nop 15\n"
"s_barrier\n"
"ds_write_b32 v8, v2 offset:21504\n"
"s_mov_b64 exec, 1\n"
"global_atomic_add v104, v3, v2, s[4:5] sc0\n"
"s_sleep 3\n"
"global_atomic_add v105, v3, v2, s[4:5] sc0\n"
"s_sleep 3\n"
"global_atomic_add v106, v3, v2, s[4:5] sc0\n"
"s_sleep 3\n"
"global_atomic_add v107, v3, v2, s[4:5] sc0\n"
"s_waitcnt vmcnt(3)\n"
"v_readfirstlane_b32 s45, v104\n"
"s_nop 0\n"
"s_cmp_ge_u32 s45, 2048\n"
"s_cbranch_scc1 Ldone80_%=\n"
"global_atomic_add v104, v3, v2, s[4:5] sc0\n"
"s_waitcnt vmcnt(3)\n"
"v_readfirstlane_b32 s45, v105\n"
"s_nop 0\n"
"s_cmp_ge_u32 s45, 2048\n"
"s_cbranch_scc1 Ldone80_%=\n"
"global_atomic_add v105, v3, v2, s[4:5] sc0\n"
"s_waitcnt vmcnt(3)\n"
"v_readfirstlane_b32 s45, v106\n"
"s_nop 0\n"
"s_cmp_ge_u32 s45, 2048\n"
"s_cbranch_scc1 Ldone80_%=\n"
"global_atomic_add v106, v3, v2, s[4:5] sc0\n"
"s_waitcnt vmcnt(3)\n"
"v_readfirstlane_b32 s45, v107\n"
"s_nop 0\n"
"s_cmp_ge_u32 s45, 2048\n"
"s_cbranch_scc1 Ldone80_%=\n"
"global_atomic_add v107, v3, v2, s[4:5] sc0\n"
"Lpoll79_%=:\n"
"s_waitcnt vmcnt(3)\n"
"v_readfirstlane_b32 s45, v104\n"
"s_nop 0\n"
"s_cmp_ge_u32 s45, 2048\n"
"s_cbranch_scc1 Ldone80_%=\n"
"global_atomic_add v104, v3, v2, s[4:5] sc0\n"
"s_waitcnt vmcnt(3)\n"
"v_readfirstlane_b32 s45, v105\n"
"s_nop 0\n"
"s_cmp_ge_u32 s45, 2048\n"
"s_cbranch_scc1 Ldone80_%=\n"
"global_atomic_add v105, v3, v2, s[4:5] sc0\n"
"s_waitcnt vmcnt(3)\n"
"v_readfirstlane_b32 s45, v106\n"
"s_nop 0\n"
"s_cmp_ge_u32 s45, 2048\n"
"s_cbranch_scc1 Ldone80_%=\n"
"global_atomic_add v106, v3, v2, s[4:5] sc0\n"
"s_waitcnt vmcnt(3)\n"
"v_readfirstlane_b32 s45, v107\n"
"s_nop 0\n"
"s_cmp_ge_u32 s45, 2048\n"
"s_cbranch_scc1 Ldone80_%=\n"
"global_atomic_add v107, v3, v2, s[4:5] sc0\n"
"s_add_u32 s25, s25, 4\n"
"s_cmp_lt_u32 s25, 1048576\n"
"s_cbranch_scc1 Lpoll79_%=\n"
"Ldone80_%=:\n"
"s_mov_b64 exec, -1\n"
"s_cmp_lt_u32 s21, 6\n"
"s_cselect_b32 s46, 14, 12\n"
"s_cselect_b32 s47, 0, 12\n"
"s_mul_i32 s46, s46, s21\n"
"s_add_u32 s46, s46, s47\n"
"s_mul_i32 s47, s22, 3\n"
"s_add_u32 s47, s47, s46\n"
"s_mul_i32 s47, s47, 21504\n"
"s_add_u32 s64, s47, 2364416\n"
"s_add_u32 s64, s4, s64\n"
"s_addc_u32 s65, s5, 0\n"
"s_lshr_b32 s47, s22, 1\n"
"s_add_u32 s47, s47, s46\n"
"s_mul_i32 s47, s47, 21504\n"
"s_add_u32 s47, s47, 2618368\n"
"s_cmp_lt_u32 s21, 6\n"
"s_cselect_b32 s47, s47, 2147418112\n"
"s_mov_b32 s58, s47\n"
"global_load_dwordx4 a[168:171], v1, s[64:65] offset:-4096 sc1\n"
"global_load_dwordx4 a[172:175], v1, s[64:65] offset:-3072 sc1\n"
"global_load_dwordx4 a[176:179], v1, s[64:65] offset:-2048 sc1\n"
"global_load_dwordx4 a[180:183], v1, s[64:65] offset:-1024 sc1\n"
"global_load_dwordx4 a[184:187], v1, s[64:65] offset:0 sc1\n"
"global_load_dwordx4 a[188:191], v1, s[64:65] offset:1024 sc1\n"
"global_load_dwordx4 a[192:195], v1, s[64:65] offset:2048 sc1\n"
"global_load_dwordx4 a[196:199], v1, s[64:65] offset:3072 sc1\n"
"s_add_u32 s64, s64, 8192\n"
"s_addc_u32 s65, s65, 0\n"
"global_load_dwordx4 a[200:203], v1, s[64:65] offset:-4096 sc1\n"
"global_load_dwordx4 a[204:207], v1, s[64:65] offset:-3072 sc1\n"
"global_load_dwordx4 a[208:211], v1, s[64:65] offset:-2048 sc1\n"
"global_load_dwordx4 a[212:215], v1, s[64:65] offset:-1024 sc1\n"
"global_load_dwordx4 a[216:219], v1, s[64:65] offset:0 sc1\n"
"global_load_dwordx4 a[220:223], v1, s[64:65] offset:1024 sc1\n"
"global_load_dwordx4 a[224:227], v1, s[64:65] offset:2048 sc1\n"
"global_load_dwordx4 a[228:231], v1, s[64:65] offset:3072 sc1\n"
"s_add_u32 s64, s64, 8192\n"
"s_addc_u32 s65, s65, 0\n"
"global_load_dwordx4 a[232:235], v1, s[64:65] offset:-4096 sc1\n"
"global_load_dwordx4 a[236:239], v1, s[64:65] offset:-3072 sc1\n"
"global_load_dwordx4 a[240:243], v1, s[64:65] offset:-2048 sc1\n"
"global_load_dwordx4 a[244:247], v1, s[64:65] offset:-1024 sc1\n"
"global_load_dwordx4 a[248:251], v1, s[64:65] offset:0 sc1\n"
"global_load_dwordx4 a[252:255], v1, s[64:65] offset:1024 sc1\n"
"global_load_dwordx4 v[176:179], v1, s[64:65] offset:2048 sc1\n"
"global_load_dwordx4 v[180:183], v1, s[64:65] offset:3072 sc1\n"
"s_add_u32 s64, s64, 8192\n"
"s_addc_u32 s65, s65, 0\n"
"global_load_dwordx4 v[184:187], v1, s[64:65] offset:-4096 sc1\n"
"global_load_dwordx4 v[188:191], v1, s[64:65] offset:-3072 sc1\n"
"global_load_dwordx4 v[192:195], v1, s[64:65] offset:-2048 sc1\n"
"global_load_dwordx4 v[196:199], v1, s[64:65] offset:-1024 sc1\n"
"global_load_dwordx4 v[200:203], v1, s[64:65] offset:0 sc1\n"
"global_load_dwordx4 v[204:207], v1, s[64:65] offset:1024 sc1\n"
"global_load_dwordx4 v[208:211], v1, s[64:65] offset:2048 sc1\n"
"global_load_dwordx4 v[212:215], v1, s[64:65] offset:3072 sc1\n"
"s_add_u32 s64, s64, 8192\n"
"s_addc_u32 s65, s65, 0\n"
"global_load_dwordx4 v[216:219], v1, s[64:65] offset:-4096 sc1\n"
"global_load_dwordx4 v[220:223], v1, s[64:65] offset:-3072 sc1\n"
"global_load_dwordx4 v[224:227], v1, s[64:65] offset:-2048 sc1\n"
"global_load_dwordx4 v[228:231], v1, s[64:65] offset:-1024 sc1\n"
"global_load_dwordx4 v[232:235], v1, s[64:65] offset:0 sc1\n"
"global_load_dwordx4 v[236:239], v1, s[64:65] offset:1024 sc1\n"
"global_load_dwordx4 v[240:243], v1, s[64:65] offset:2048 sc1\n"
"global_load_dwordx4 v[244:247], v1, s[64:65] offset:3072 sc1\n"
"s_add_u32 s64, s64, 8192\n"
"s_addc_u32 s65, s65, 0\n"
"global_load_dwordx4 v[248:251], v1, s[64:65] offset:-4096 sc1\n"
"global_load_dwordx4 v[252:255], v1, s[64:65] offset:-3072 sc1\n"
"global_load_dwordx4 a[0:3], v1, s[64:65] offset:-2048 sc1\n"
"global_load_dwordx4 a[4:7], v1, s[64:65] offset:-1024 sc1\n"
"global_load_dwordx4 a[8:11], v1, s[64:65] offset:0 sc1\n"
"global_load_dwordx4 a[12:15], v1, s[64:65] offset:1024 sc1\n"
"global_load_dwordx4 a[16:19], v1, s[64:65] offset:2048 sc1\n"
"global_load_dwordx4 a[20:23], v1, s[64:65] offset:3072 sc1\n"
"s_add_u32 s64, s64, 8192\n"
"s_addc_u32 s65, s65, 0\n"
"global_load_dwordx4 a[24:27], v1, s[64:65] offset:-4096 sc1\n"
"global_load_dwordx4 a[28:31], v1, s[64:65] offset:-3072 sc1\n"
"global_load_dwordx4 a[32:35], v1, s[64:65] offset:-2048 sc1\n"
"global_load_dwordx4 a[36:39], v1, s[64:65] offset:-1024 sc1\n"
"global_load_dwordx4 a[40:43], v1, s[64:65] offset:0 sc1\n"
"global_load_dwordx4 a[44:47], v1, s[64:65] offset:1024 sc1\n"
"global_load_dwordx4 a[48:51], v1, s[64:65] offset:2048 sc1\n"
"global_load_dwordx4 a[52:55], v1, s[64:65] offset:3072 sc1\n"
"s_add_u32 s64, s64, 8192\n"
"s_addc_u32 s65, s65, 0\n"
"global_load_dwordx4 a[56:59], v1, s[64:65] offset:-4096 sc1\n"
"global_load_dwordx4 a[60:63], v1, s[64:65] offset:-3072 sc1\n"
"global_load_dwordx4 a[64:67], v1, s[64:65] offset:-2048 sc1\n"
"global_load_dwordx4 a[68:71], v1, s[64:65] offset:-1024 sc1\n"
"global_load_dwordx4 a[72:75], v1, s[64:65] offset:0 sc1\n"
"global_load_dwordx4 a[76:79], v1, s[64:65] offset:1024 sc1\n"
"global_load_dwordx4 a[80:83], v1, s[64:65] offset:2048 sc1\n"
"buffer_load_dwordx4 a[84:87], v1, s[12:15], s58 offen offset:0 sc1\n"
"buffer_load_dwordx4 a[88:91], v1, s[12:15], s58 offen offset:1024 sc1\n"
"buffer_load_dwordx4 a[92:95], v1, s[12:15], s58 offen offset:2048 sc1\n"
"buffer_load_dwordx4 a[96:99], v1, s[12:15], s58 offen offset:3072 sc1\n"
"s_add_u32 s58, s58, 4096\n"
"buffer_load_dwordx4 a[100:103], v1, s[12:15], s58 offen offset:0 sc1\n"
"buffer_load_dwordx4 a[104:107], v1, s[12:15], s58 offen offset:1024 sc1\n"
"buffer_load_dwordx4 a[108:111], v1, s[12:15], s58 offen offset:2048 sc1\n"
"buffer_load_dwordx4 a[112:115], v1, s[12:15], s58 offen offset:3072 sc1\n"
"s_add_u32 s58, s58, 4096\n"
"buffer_load_dwordx4 a[116:119], v1, s[12:15], s58 offen offset:0 sc1\n"
"buffer_load_dwordx4 a[120:123], v1, s[12:15], s58 offen offset:1024 sc1\n"
"buffer_load_dwordx4 a[124:127], v1, s[12:15], s58 offen offset:2048 sc1\n"
"buffer_load_dwordx4 a[128:131], v1, s[12:15], s58 offen offset:3072 sc1\n"
"s_add_u32 s58, s58, 4096\n"
"buffer_load_dwordx4 a[132:135], v1, s[12:15], s58 offen offset:0 sc1\n"
"buffer_load_dwordx4 a[136:139], v1, s[12:15], s58 offen offset:1024 sc1\n"
"buffer_load_dwordx4 a[140:143], v1, s[12:15], s58 offen offset:2048 sc1\n"
"buffer_load_dwordx4 a[144:147], v1, s[12:15], s58 offen offset:3072 sc1\n"
"s_add_u32 s58, s58, 4096\n"
"buffer_load_dwordx4 a[148:151], v1, s[12:15], s58 offen offset:0 sc1\n"
"buffer_load_dwordx4 a[152:155], v1, s[12:15], s58 offen offset:1024 sc1\n"
"buffer_load_dwordx4 a[156:159], v1, s[12:15], s58 offen offset:2048 sc1\n"
"buffer_load_dwordx4 a[160:163], v1, s[12:15], s58 offen offset:3072 sc1\n"
"s_add_u32 s58, s58, 4096\n"
"buffer_load_dwordx4 a[164:167], v1, s[12:15], s58 offen offset:0 sc1\n"
"s_sub_u32 s66, s66, 40960\n"
"s_subb_u32 s67, s67, 0\n"
"s_add_u32 s64, s4, s30\n"
"s_addc_u32 s65, s5, 0\n"
"s_add_u32 s46, s30, 20480\n"
"buffer_load_dwordx2 v[112:113], v1, s[12:15], s46 offen sc1\n"
"buffer_load_dwordx2 v[114:115], v1, s[12:15], s46 offen offset:1024 sc1\n"
"s_add_u32 m0, s51, 0\n"
"s_nop 0\n"
"global_load_lds_dwordx4 v1, s[64:65] offset:0 sc1\n"
"global_load_lds_dwordx4 v1, s[64:65] offset:1024 sc1\n"
"global_load_lds_dwordx4 v1, s[64:65] offset:2048 sc1\n"
"global_load_lds_dwordx4 v1, s[64:65] offset:3072 sc1\n"
"s_add_u32 m0, s51, 4096\n"
"s_add_u32 s64, s64, 4096\n"
"s_addc_u32 s65, s65, 0\n"
"global_load_lds_dwordx4 v1, s[64:65] offset:0 sc1\n"
"global_load_lds_dwordx4 v1, s[64:65] offset:1024 sc1\n"
"global_load_lds_dwordx4 v1, s[64:65] offset:2048 sc1\n"
"global_load_lds_dwordx4 v1, s[64:65] offset:3072 sc1\n"
"v_mov_b32 v0, 0\n"
"s_mov_b32 s50, 0\n"
"s_waitcnt vmcnt(0) lgkmcnt(0)\n"
"s_nop 15\n"
"s_branch Lrc48_%=\n"
"Lse59_%=:\n"
"s_waitcnt vmcnt(0) lgkmcnt(0)\n"
"s_nop 15\n"
"s_mov_b64 exec, 1\n"
"global_atomic_add v108, v3, v2, s[4:5] sc0\n"
"s_sleep 3\n"
"global_atomic_add v109, v3, v2, s[4:5] sc0\n"
"s_sleep 3\n"
"global_atomic_add v110, v3, v2, s[4:5] sc0\n"
"s_sleep 3\n"
"global_atomic_add v111, v3, v2, s[4:5] sc0\n"
"s_waitcnt vmcnt(3)\n"
"v_readfirstlane_b32 s45, v108\n"
"s_nop 0\n"
"s_cmp_ge_u32 s45, 2048\n"
"s_cbranch_scc1 Ldone82_%=\n"
"global_atomic_add v108, v3, v2, s[4:5] sc0\n"
"s_waitcnt vmcnt(3)\n"
"v_readfirstlane_b32 s45, v109\n"
"s_nop 0\n"
"s_cmp_ge_u32 s45, 2048\n"
"s_cbranch_scc1 Ldone82_%=\n"
"global_atomic_add v109, v3, v2, s[4:5] sc0\n"
"s_waitcnt vmcnt(3)\n"
"v_readfirstlane_b32 s45, v110\n"
"s_nop 0\n"
"s_cmp_ge_u32 s45, 2048\n"
"s_cbranch_scc1 Ldone82_%=\n"
"global_atomic_add v110, v3, v2, s[4:5] sc0\n"
"s_waitcnt vmcnt(3)\n"
"v_readfirstlane_b32 s45, v111\n"
"s_nop 0\n"
"s_cmp_ge_u32 s45, 2048\n"
"s_cbranch_scc1 Ldone82_%=\n"
"global_atomic_add v111, v3, v2, s[4:5] sc0\n"
"Lpoll81_%=:\n"
"s_waitcnt vmcnt(3)\n"
"v_readfirstlane_b32 s45, v108\n"
"s_nop 0\n"
"s_cmp_ge_u32 s45, 2048\n"
"s_cbranch_scc1 Ldone82_%=\n"
"global_atomic_add v108, v3, v2, s[4:5] sc0\n"
"s_waitcnt vmcnt(3)\n"
"v_readfirstlane_b32 s45, v109\n"
"s_nop 0\n"
"s_cmp_ge_u32 s45, 2048\n"
"s_cbranch_scc1 Ldone82_%=\n"
"global_atomic_add v109, v3, v2, s[4:5] sc0\n"
"s_waitcnt vmcnt(3)\n"
"v_readfirstlane_b32 s45, v110\n"
"s_nop 0\n"
"s_cmp_ge_u32 s45, 2048\n"
"s_cbranch_scc1 Ldone82_%=\n"
"global_atomic_add v110, v3, v2, s[4:5] sc0\n"
"s_waitcnt vmcnt(3)\n"
"v_readfirstlane_b32 s45, v111\n"
"s_nop 0\n"
"s_cmp_ge_u32 s45, 2048\n"
"s_cbranch_scc1 Ldone82_%=\n"
"global_atomic_add v111, v3, v2, s[4:5] sc0\n"
"s_add_u32 s25, s25, 4\n"
"s_cmp_lt_u32 s25, 1048576\n"
"s_cbranch_scc1 Lpoll81_%=\n"
"Ldone82_%=:\n"
"s_mov_b64 exec, -1\n"
"s_waitcnt vmcnt(0) lgkmcnt(0)\n"
"s_nop 15\n"
"s_add_u32 s64, s51, 4096\n"
"s_add_u32 s64, s4, s64\n"
"s_addc_u32 s65, s5, 0\n"
"s_add_u32 s46, s51, 20480\n"
"buffer_load_dwordx2 v[140:141], v1, s[12:15], s46 offen sc1\n"
"buffer_load_dwordx2 v[142:143], v1, s[12:15], s46 offen offset:1024 sc1\n"
"global_load_dwordx4 v[32:35], v1, s[64:65] offset:-4096 sc1\n"
"global_load_dwordx4 v[36:39], v1, s[64:65] offset:-3072 sc1\n"
"global_load_dwordx4 v[40:43], v1, s[64:65] offset:-2048 sc1\n"
"global_load_dwordx4 v[44:47], v1, s[64:65] offset:-1024 sc1\n"
"global_load_dwordx4 v[48:51], v1, s[64:65] offset:0 sc1\n"
"Lpad14_%=:\n"
"s_cbranch_execz Lpadback3_%=\n"
"global_load_dwordx4 v[52:55], v1, s[64:65] offset:1024 sc1\n"
"global_load_dwordx4 v[56:59], v1, s[64:65] offset:2048 sc1\n"
"global_load_dwordx4 v[60:63], v1, s[64:65] offset:3072 sc1\n"
"s_add_u32 s64, s64, 8192\n"
"s_addc_u32 s65, s65, 0\n"
"global_load_dwordx4 v[64:67], v1, s[64:65] offset:-4096 sc1\n"
"global_load_dwordx4 v[68:71], v1, s[64:65] offset:-3072 sc1\n"
"ds_read_b128 a[0:3], v1 offset:0\n"
"ds_read_b128 a[4:7], v1 offset:1024\n"
"ds_read_b128 a[8:11], v1 offset:2048\n"
"ds_read_b128 a[12:15], v1 offset:3072\n"
"ds_read_b128 a[16:19], v1 offset:4096\n"
"ds_read_b128 a[20:23], v1 offset:5120\n"
"ds_read_b128 a[24:27], v1 offset:6144\n"
"ds_read_b128 a[28:31], v1 offset:7168\n"
"ds_read_b128 a[32:35], v1 offset:8192\n"
"ds_read_b128 a[36:39], v1 offset:9216\n"
"ds_read_b128 a[40:43], v1 offset:10240\n"
"ds_read_b128 a[44:47], v1 offset:11264\n"
"ds_read_b128 a[48:51], v1 offset:12288\n"
"ds_read_b128 a[52:55], v1 offset:13312\n"
"ds_read_b128 a[56:59], v1 offset:14336\n"
"ds_read_b128 a[60:63], v1 offset:15360\n"
"ds_read_b128 a[64:67], v1 offset:16384\n"
"ds_read_b128 a[68:71], v1 offset:17408\n"
"ds_read_b128 a[72:75], v1 offset:18432\n"
"ds_read_b128 a[76:79], v1 offset:19456\n"
"ds_read_b128 a[80:83], v1 offset:20480\n"
"ds_read_b128 a[84:87], v1 offset:21504\n"
"ds_read_b128 a[88:91], v1 offset:22528\n"
"ds_read_b128 a[92:95], v1 offset:23552\n"
"ds_read_b128 a[96:99], v1 offset:24576\n"
"ds_read_b128 a[100:103], v1 offset:25600\n"
"ds_read_b128 a[104:107], v1 offset:26624\n"
"ds_read_b128 a[108:111], v1 offset:27648\n"
"ds_read_b128 a[112:115], v1 offset:28672\n"
"ds_read_b128 a[116:119], v1 offset:29696\n"
"ds_read_b128 a[120:123], v1 offset:30720\n"
"ds_read_b128 a[124:127], v1 offset:31744\n"
"ds_read_b128 a[128:131], v1 offset:32768\n"
"ds_read_b128 a[132:135], v1 offset:33792\n"
"ds_read_b128 a[136:139], v1 offset:34816\n"
"ds_read_b128 a[140:143], v1 offset:35840\n"
"ds_read_b128 a[144:147], v1 offset:36864\n"
"ds_read_b128 a[148:151], v1 offset:37888\n"
"ds_read_b128 a[152:155], v1 offset:38912\n"
"ds_read_b128 a[156:159], v1 offset:39936\n"
"ds_read_b128 a[160:163], v1 offset:40960\n"
"ds_read_b128 a[164:167], v1 offset:41984\n"
"ds_read_b128 a[168:171], v1 offset:43008\n"
"ds_read_b128 a[172:175], v1 offset:44032\n"
"ds_read_b128 a[176:179], v1 offset:45056\n"
"ds_read_b128 a[180:183], v1 offset:46080\n"
"ds_read_b128 a[184:187], v1 offset:47104\n"
"ds_read_b128 a[188:191], v1 offset:48128\n"
"ds_read_b128 a[192:195], v1 offset:49152\n"
"ds_read_b128 a[196:199], v1 offset:50176\n"
"ds_read_b128 a[200:203], v1 offset:51200\n"
"ds_read_b128 a[204:207], v1 offset:52224\n"
"ds_read_b128 a[208:211], v1 offset:53248\n"
"ds_read_b128 a[212:215], v1 offset:54272\n"
"ds_read_b128 a[216:219], v1 offset:55296\n"
"ds_read_b128 a[220:223], v1 offset:56320\n"
"ds_read_b128 a[224:227], v1 offset:57344\n"
"ds_read_b128 a[228:231], v1 offset:58368\n"
"ds_read_b128 a[232:235], v1 offset:59392\n"
"ds_read_b128 a[236:239], v1 offset:60416\n"
"ds_read_b128 a[240:243], v1 offset:61440\n"
"ds_read_b128 a[244:247], v1 offset:62464\n"
"ds_read_b128 a[248:251], v1 offset:63488\n"
"ds_read_b128 a[252:255], v1 offset:64512\n"
"v_and_b32 v144, 48, v4\n"
"v_lshlrev_b32 v145, 2, v4\n"
"v_cmp_eq_u32 vcc, s34, 1\n"
"v_add_u32 v148, 0, v144\n"
"v_add_u32 v149, 4, v144\n"
"v_add_u32 v150, 8, v144\n"
"v_add_u32 v151, 12, v144\n"
"v_cndmask_b32 v148, v145, v148, vcc\n"
"v_cndmask_b32 v149, v145, v149, vcc\n"
"v_cndmask_b32 v150, v145, v150, vcc\n"
"v_cndmask_b32 v151, v145, v151, vcc\n"
"v_mov_b32 v155, 0\n"
"v_mov_b32 v172, 0\n"
"s_waitcnt vmcnt(0) lgkmcnt(0)\n"
"s_nop 15\n"
"s_branch Lre58_%=\n"
"Lend3_%=:\n"
"Lfin_%=:\n"
"s_waitcnt vmcnt(0) lgkmcnt(0)\n"
  : "+v"(tid)
  : "s"(__builtin_amdgcn_kernarg_segment_ptr()), "s"(blockIdx.x)
  : "v1","v2","v3","v4","v5","v6","v7","v8","v9","v10","v11","v12","v13","v14","v15","v16","v17","v18","v19","v20","v21","v22","v23","v24","v25","v26","v27","v28","v29","v30","v31","v32","v33","v34","v35","v36","v37","v38","v39","v40","v41","v42","v43","v44","v45","v46","v47","v48","v49","v50","v51","v52","v53","v54","v55","v56","v57","v58","v59","v60","v61","v62","v63","v64","v65","v66","v67","v68","v69","v70","v71","v72","v73","v74","v75","v76","v77","v78","v79","v80","v81","v82","v83","v84","v85","v86","v87","v88","v89","v90","v91","v92","v93","v94","v95","v96","v97","v98","v99","v100","v101","v102","v103","v104","v105","v106","v107","v108","v109","v110","v111","v112","v113","v114","v115","v116","v117","v118","v119","v120","v121","v122","v123","v124","v125","v126","v127","v128","v129","v130","v131","v132","v133","v134","v135","v136","v137","v138","v139","v140","v141","v142","v143","v144","v145","v146","v147","v148","v149","v150","v151","v152","v153","v154","v155","v156","v157","v158","v159","v160","v161","v162","v163","v164","v165","v166","v167","v168","v169","v170","v171","v172","v173","v174","v175","v176","v177","v178","v179","v180","v181","v182","v183","v184","v185","v186","v187","v188","v189","v190","v191","v192","v193","v194","v195","v196","v197","v198","v199","v200","v201","v202","v203","v204","v205","v206","v207","v208","v209","v210","v211","v212","v213","v214","v215","v216","v217","v218","v219","v220","v221","v222","v223","v224","v225","v226","v227","v228","v229","v230","v231","v232","v233","v234","v235","v236","v237","v238","v239","v240","v241","v242","v243","v244","v245","v246","v247","v248","v249","v250","v251","v252","v253","v254","v255","a0","a1","a2","a3","a4","a5","a6","a7","a8","a9","a10","a11","a12","a13","a14","a15","a16","a17","a18","a19","a20","a21","a22","a23","a24","a25","a26","a27","a28","a29","a30","a31","a32","a33","a34","a35","a36","a37","a38","a39","a40","a41","a42","a43","a44","a45","a46","a47","a48","a49","a50","a51","a52","a53","a54","a55","a56","a57","a58","a59","a60","a61","a62","a63","a64","a65","a66","a67","a68","a69","a70","a71","a72","a73","a74","a75","a76","a77","a78","a79","a80","a81","a82","a83","a84","a85","a86","a87","a88","a89","a90","a91","a92","a93","a94","a95","a96","a97","a98","a99","a100","a101","a102","a103","a104","a105","a106","a107","a108","a109","a110","a111","a112","a113","a114","a115","a116","a117","a118","a119","a120","a121","a122","a123","a124","a125","a126","a127","a128","a129","a130","a131","a132","a133","a134","a135","a136","a137","a138","a139","a140","a141","a142","a143","a144","a145","a146","a147","a148","a149","a150","a151","a152","a153","a154","a155","a156","a157","a158","a159","a160","a161","a162","a163","a164","a165","a166","a167","a168","a169","a170","a171","a172","a173","a174","a175","a176","a177","a178","a179","a180","a181","a182","a183","a184","a185","a186","a187","a188","a189","a190","a191","a192","a193","a194","a195","a196","a197","a198","a199","a200","a201","a202","a203","a204","a205","a206","a207","a208","a209","a210","a211","a212","a213","a214","a215","a216","a217","a218","a219","a220","a221","a222","a223","a224","a225","a226","a227","a228","a229","a230","a231","a232","a233","a234","a235","a236","a237","a238","a239","a240","a241","a242","a243","a244","a245","a246","a247","a248","a249","a250","a251","a252","a253","a254","a255","s4","s5","s6","s7","s8","s9","s10","s11","s12","s13","s14","s15","s16","s17","s18","s19","s20","s21","s22","s23","s24","s25","s26","s27","s28","s29","s30","s31","s34","s35","s36","s37","s38","s39","s40","s41","s42","s43","s44","s45","s46","s47","s48","s49","s50","s51","s52","s53","s54","s55","s56","s57","s58","s59","s60","s61","s62","s63","s64","s65","s66","s67","s68","s69","s70","s71","s72","s73","s74","s75","s76","s77","s78","s79","s80","s81","s82","s83","s84","s85","s86","s87","s88","s89","s90","s91","s92","s93","s94","s95","s96","s97","s98","s99","vcc","scc","memory");
}

extern "C" void kernel_launch(void* const* d_in, const int* in_sizes, int n_in, void* d_out, int out_size, void* d_ws, size_t ws_size,
                              hipStream_t stream) {
  fused_kernel<<<256, 256, 0, stream>>>(d_in[0], d_in[1], d_in[2], d_in[3], d_in[4], d_in[6], d_in[7], d_in[8], d_in[9], d_out, d_ws);
}
```
